```python
import torch

def _softmax(x, axis=-1):
    return torch.softmax(x, dim=axis)
def _gelu(x):
    return torch.nn.functional.gelu(x, approximate="tanh")
def _silu(x):
    return torch.nn.functional.silu(x)
def _sigmoid(x):
    return torch.sigmoid(x)
def _layernorm(x, axis=-1, eps=1e-5):
    m = x.mean(dim=axis, keepdim=True)
    v = x.var(dim=axis, keepdim=True, unbiased=False)
    return (x - m) / torch.sqrt(v + eps)
def _scatter_add(tgt, idx, src, axis=0):
    return tgt.clone().index_add_(axis, idx, src)
def _segment_sum(src, seg, n):
    out = torch.zeros((n,) + src.shape[1:], dtype=src.dtype)
    return out.index_add_(0, seg, src)
def _pad(x, pw):
    flat = tuple(v for pair in reversed(pw) for v in pair)
    return torch.nn.functional.pad(x, flat)

kernel_name = 'interleave_layers__mha_block__dense_activation_mish__prompt'

def setup_inputs():

    _w = (lambda a: (lambda t: t.to(torch.bfloat16) if t.is_floating_point() else t)(torch.as_tensor(a)))
    def _A_setup():
        B, S, D, H = 1, 2048, 512, 32
        return {
            "x": _w((lambda *s: torch.randn(*s, dtype=torch.bfloat16) * 0.05)(B, S, D)),
            "wqkv": _w((lambda *s: torch.randn(*s, dtype=torch.bfloat16) * 0.05)(D, 3 * D)),
            "wo": _w((lambda *s: torch.randn(*s, dtype=torch.bfloat16) * 0.05)(D, D)),
        }
    _A_in = _A_setup()

    def _B_setup():
        B, S, D, F = 1, 2048, 512, 1376
        return {
            "x": _w((lambda *s: torch.randn(*s, dtype=torch.bfloat16) * 0.05)(B, S, D)),
            "w1": _w((lambda *s: torch.randn(*s, dtype=torch.bfloat16) * 1.6)(D, F) * (D ** -0.5)),
            "b1": _w((lambda *s: torch.randn(*s, dtype=torch.bfloat16) * 0.2)(F)),
            "w2": _w((lambda *s: torch.randn(*s, dtype=torch.bfloat16) * 0.2)(F, D) * (F ** -0.5)),
        }
    _B_in = _B_setup()

    return {
        "A_x": _A_in["x"],
        "A_wqkv": _A_in["wqkv"],
        "A_wo": _A_in["wo"],
        "B_x": _B_in["x"],
        "B_w1": _B_in["w1"],
        "B_b1": _B_in["b1"],
        "B_w2": _B_in["w2"],
    }

def reference(A_x, A_wqkv, A_wo, B_x, B_w1, B_b1, B_w2):
    def _A_ref(x, wqkv, wo):
        B, S, D = x.shape
        H = 32
        Dh = D // H
        qkv = (lambda x, *d: x.permute(*d))((x @ wqkv).reshape(B, S, 3, H, Dh), 2, 0, 3, 1, 4)
        q, k, v = qkv[0], qkv[1], qkv[2]
        scores = torch.einsum("bhqd,bhkd->bhqk", q, k) * (Dh ** -0.5)
        p = _softmax(scores, axis=-1)
        o = torch.einsum("bhqk,bhkd->bhqd", p, v)
        return (lambda x, *d: x.permute(*d))(o, 0, 2, 1, 3).reshape(B, S, D) @ wo

    def _B_ref(x, w1, b1, w2):
        h = torch.einsum("bsd,df->bsf", x, w1) + b1
        sp = torch.log1p(torch.exp(-torch.abs(h))) + torch.where(h > 0, h, torch.zeros_like(h))
        act = h * torch.tanh(sp)
        return torch.einsum("bsf,fd->bsd", act, w2)

    x = A_x
    x = x + _A_ref(_layernorm(x, axis=-1), A_wqkv, A_wo)
    x = x + _B_ref(_layernorm(x, axis=-1), B_w1, B_b1, B_w2)
    x = x + _A_ref(_layernorm(x, axis=-1), A_wqkv, A_wo)
    x = x + _B_ref(_layernorm(x, axis=-1), B_w1, B_b1, B_w2)
    return x
```

```cpp
#include <hip/hip_runtime.h>
#include <hip/hip_bf16.h>
#include <stdint.h>

#define PERSIST 0
#define STAMPS 0

#define LAS __attribute__((address_space(3)))
#define GAS __attribute__((address_space(1)))
typedef unsigned v4u __attribute__((ext_vector_type(4)));
typedef unsigned v2u __attribute__((ext_vector_type(2)));
typedef float f32x4 __attribute__((ext_vector_type(4)));
typedef float f32x16 __attribute__((ext_vector_type(16)));
typedef __bf16 bf16x2 __attribute__((ext_vector_type(2)));
typedef __bf16 bf16x8 __attribute__((ext_vector_type(8)));
typedef __bf16 bf16x16 __attribute__((ext_vector_type(16)));
typedef short s16x4 __attribute__((ext_vector_type(4)));
typedef unsigned v8u __attribute__((ext_vector_type(8)));
typedef __amdgpu_buffer_rsrc_t rsrc_t;
typedef unsigned long long u64;

constexpr unsigned MB = 1u << 20;
constexpr unsigned OFF_EPOCH = 0;
constexpr unsigned OFF_FLAGS = 4096;
constexpr unsigned OFF_KN = 32768;
constexpr unsigned OFF_STAMP = 65536;
constexpr unsigned OFF_QF = MB;
constexpr unsigned OFF_KV = OFF_QF + 4 * MB;
constexpr unsigned OFF_OF = OFF_KV + 8 * MB;
constexpr unsigned OFF_X1 = OFF_OF + 4 * MB;
constexpr unsigned OFF_X2 = OFF_X1 + 4 * MB;
constexpr unsigned OFF_HF = OFF_X2 + 2 * MB;
constexpr unsigned HF_LAYER = 6 * MB;
constexpr unsigned WS_BYTES = OFF_HF + 2 * HF_LAYER;
constexpr unsigned LDS_BYTES = 163840;
constexpr unsigned L_VSCR = 98304;
constexpr unsigned L_XCH = 131072;
constexpr unsigned L_MISC = 159744;
constexpr unsigned L_CSP = L_MISC, L_CS = L_MISC + 1536, L_BI = L_MISC + 1920;
constexpr unsigned KVP = 2064;
constexpr int SC1 = 16;
enum { PH_QKV = 0, PH_ATT = 1, PH_WO = 2, PH_UP = 3, PH_DOWN = 4 };

struct Params {
  const void *x, *wqkv, *wo, *w1, *b1, *w2;
  void *out;
  char *ws;
};

struct Ctx {
  rsrc_t rws;
  LAS char *lds;
  unsigned lds0;
  int w;
  int bx, bj;
  u64 tag;
};

#define BAR() asm volatile("s_waitcnt lgkmcnt(0)\n\ts_barrier" ::: "memory")
#define VMWAIT(n) asm volatile("s_waitcnt vmcnt(" #n ")" ::: "memory")
__device__ __forceinline__ rsrc_t mkrsrc(const void *p, unsigned bytes) { return __builtin_amdgcn_make_buffer_rsrc((void *)p, (short)0, (int)bytes, 0x00020000); }
__device__ __forceinline__ int lane_id() { int l; asm volatile("v_mbcnt_lo_u32_b32 %0, -1, 0\n\tv_mbcnt_hi_u32_b32 %0, -1, %0" : "=v"(l)); return l; }
__device__ __forceinline__ f32x16 mfma(v4u a, v4u b, f32x16 c) {
  return __builtin_amdgcn_mfma_f32_32x32x16_bf16(__builtin_bit_cast(bf16x8, a), __builtin_bit_cast(bf16x8, b), c, 0, 0, 0);
}
__device__ __forceinline__ unsigned pk2(float lo, float hi) { bf16x2 v = {(__bf16)lo, (__bf16)hi}; return __builtin_bit_cast(unsigned, v); }
__device__ __forceinline__ float dot2(unsigned a, unsigned b, float c) {
  return __builtin_amdgcn_fdot2_f32_bf16(__builtin_bit_cast(bf16x2, a), __builtin_bit_cast(bf16x2, b), c, false);
}
constexpr unsigned ONES2 = 0x3f803f80u;
__device__ __forceinline__ float bf_lo(unsigned u) { return __builtin_bit_cast(float, u << 16); }
__device__ __forceinline__ float bf_hi(unsigned u) { return __builtin_bit_cast(float, u & 0xffff0000u); }
__device__ __forceinline__ float halfsum2(float a, float b) {
  auto r = __builtin_amdgcn_permlane32_swap(__builtin_bit_cast(unsigned, a), __builtin_bit_cast(unsigned, b), false, false);
  const unsigned r0 = r[0], r1 = r[1];
  return __uint_as_float(r0) + __uint_as_float(r1);
}
template <int CTRL, int RMASK> __device__ __forceinline__ float dppmax(float v) {
  const int o = __builtin_amdgcn_update_dpp(0, __builtin_bit_cast(int, v), CTRL, RMASK, 0xf, true);
  return __builtin_fmaxf(v, __builtin_bit_cast(float, o));
}
__device__ __forceinline__ float max32(float v) {
  v = dppmax<0x111, 0xf>(v); v = dppmax<0x112, 0xf>(v); v = dppmax<0x114, 0xf>(v); v = dppmax<0x118, 0xf>(v);
  return dppmax<0x142, 0xa>(v);
}
template <bool DEV> __device__ __forceinline__ void glds(rsrc_t rs, unsigned voff, unsigned soff, unsigned ldsaddr) {
  unsigned keep;
  if constexpr (DEV)
    asm volatile("s_mov_b32 %0, m0\n\ts_mov_b32 m0, %1\n\ts_nop 2\n\tbuffer_load_dwordx4 %2, %3, %4 offen sc1 lds\n\ts_mov_b32 m0, %0"
                 : "=&s"(keep) : "s"(ldsaddr), "v"(voff), "s"(rs), "s"(soff) : "memory");
  else
    asm volatile("s_mov_b32 %0, m0\n\ts_mov_b32 m0, %1\n\ts_nop 2\n\tbuffer_load_dwordx4 %2, %3, %4 offen lds\n\ts_mov_b32 m0, %0"
                 : "=&s"(keep) : "s"(ldsaddr), "v"(voff), "s"(rs), "s"(soff) : "memory");
}
__device__ __forceinline__ v2u trd(const LAS char *p) {
  return __builtin_bit_cast(v2u, __builtin_amdgcn_ds_read_tr16_b64_v4i16((LAS s16x4 *)p));
}
__device__ __forceinline__ v4u trfrag(const LAS char *p0, const LAS char *p1) { const v2u a = trd(p0), b = trd(p1); return v4u{a.x, a.y, b.x, b.y}; }
__device__ __forceinline__ v4u ld16(rsrc_t rs, unsigned voff, unsigned soff) { return __builtin_amdgcn_raw_buffer_load_b128(rs, voff, soff, SC1); }
__device__ __forceinline__ void st16(v4u v, rsrc_t rs, unsigned voff, unsigned soff) { __builtin_amdgcn_raw_buffer_store_b128(v, rs, voff, soff, SC1); }

#define STAMP(c, layer, ph, i) do {} while (0)

__device__ __forceinline__ GAS u64 *flag_ptr(const Params &p, int layer, int ph, int idx) {
  return (GAS u64 *)(p.ws + OFF_FLAGS + (layer * 5 + ph) * 2048 + idx * 8);
}
__device__ __forceinline__ void publish(const Params &p, const Ctx &c, int layer, int ph, int idx) {
  if (PERSIST && c.w == 0 && lane_id() == 0) __hip_atomic_store(flag_ptr(p, layer, ph, idx), c.tag, __ATOMIC_RELAXED, __HIP_MEMORY_SCOPE_AGENT);
}
__device__ __forceinline__ void poll(const Params &p, const Ctx &c, int layer, int ph, int idx  ) {
  if (!PERSIST) return;
  GAS u64 *f = flag_ptr(p, layer, ph, idx);
  for (unsigned spins = 0; spins < (1u << 20); ++spins) {
    const u64 v = __hip_atomic_load(f, __ATOMIC_RELAXED, __HIP_MEMORY_SCOPE_AGENT);
    if (__all(v == c.tag)) break;
    __builtin_amdgcn_s_sleep(1);
  }
  __builtin_amdgcn_fence(__ATOMIC_ACQUIRE, "wavefront");
}

__device__ __forceinline__ int up_nt0(int cb) { return cb < 11 ? 3 * cb : 33 + 2 * (cb - 11); }
template <int PH> __device__ __forceinline__ void stage_w(const Params &p, const Ctx &c) {
  const int l = lane_id(), hj = c.w - 4;
  if constexpr (PH == PH_QKV || PH == PH_UP) {
    constexpr unsigned pitch = PH == PH_QKV ? 3072 : 2752;
    const rsrc_t rw = mkrsrc(PH == PH_QKV ? p.wqkv : p.w1, 512 * pitch);
    const int cb = c.bj & 15, nt0 = PH == PH_QKV ? 3 * cb : up_nt0(cb);
    const unsigned voff = (l >> 2) * pitch + (l & 3) * 16;
#pragma unroll
    for (int i = 0; i < 8; ++i) {
      const int kb = 4 * i + hj;
#pragma unroll
      for (int nt = 0; nt < 3; ++nt) {
        const int NT = PH == PH_QKV ? nt0 + nt : (nt0 + nt > 42 ? 42 : nt0 + nt);
        glds<false>(rw, voff, kb * 16 * pitch + NT * 64, c.lds0 + nt * 32768 + kb * 1024);
      }
    }
  } else {
    const rsrc_t rw = mkrsrc(PH == PH_WO ? p.wo : p.w2, (PH == PH_WO ? 512 : 1376) * 1024);
    const int cb = c.bj & 7;
    const unsigned voff = (l >> 2) * 1024 + (l & 3) * 16;
    if constexpr (PH == PH_WO) {
#pragma unroll
      for (int i = 0; i < 8; ++i) {
        const int kb = 4 * i + hj;
#pragma unroll
        for (int nt = 0; nt < 2; ++nt) glds<false>(rw, voff, kb * 16384 + cb * 128 + nt * 64, c.lds0 + kb * 2048 + nt * 1024);
      }
    } else {
      const int kh = hj >> 1, nt = hj & 1;
#pragma unroll
      for (int ks = 0; ks < 39; ++ks) glds<false>(rw, voff, (kh * 43 + ks) * 16384 + cb * 128 + nt * 64, c.lds0 + (kh * 39 + ks) * 2048 + nt * 1024);
    }
  }
}
__device__ __forceinline__ void stage_w2_tail(const Params &p, const Ctx &c) {
  const int l = lane_id(), hj = c.w - 4, kh = hj >> 1, nt = hj & 1, cb = c.bj & 7;
  const rsrc_t rw = mkrsrc(p.w2, 1376 * 1024);
  const unsigned voff = (l >> 2) * 1024 + (l & 3) * 16;
#pragma unroll
  for (int ks = 39; ks < 43; ++ks) glds<false>(rw, voff, (kh * 43 + ks) * 16384 + cb * 128 + nt * 64, c.lds0 + (kh * 39 + ks - 39) * 2048 + nt * 1024);
}
__device__ __forceinline__ unsigned tr_off(int l, int quad) { return quad * 256 + ((l & 15) >> 2) * 64 + ((l >> 4) & 1) * 32 + (l & 3) * 8; }

__device__ __forceinline__ float mish(float h) {
  const float e = __builtin_amdgcn_exp2f(h * 1.4426950408889634f);
  const float t = e + 1.f;
  const float r = __builtin_amdgcn_rcpf(__builtin_fmaf(t, t, 1.f));
  return h * __builtin_fmaf(r, -2.f, 1.f);
}

template <int PH> __device__ __forceinline__ void phase_ln_gemm(const Params &p, const Ctx &c, int layer) {
  const int rb = 2 * c.bx + (c.bj >> 4), cb = c.bj & 15;
  const int nt0 = PH == PH_QKV ? 3 * cb : up_nt0(cb);
  if (c.w >= 4) {
    const int l = lane_id(), hj = c.w - 4;
    if (!PERSIST) stage_w<PH>(p, c);
    float bias = 0.f;
    if (PH == PH_UP && hj < 2) {
      const int n = hj * 64 + l, col = nt0 * 32 + n;
      if (n < 96 && col < 1376) bias = bf_lo(((const GAS unsigned short *)p.b1)[col]);
    }
    VMWAIT(0);
    const LAS char *b0 = c.lds + tr_off(l, l >> 5), *b1 = c.lds + tr_off(l, 2 + (l >> 5));
    float cs[3] = {0.f, 0.f, 0.f};
#pragma unroll
    for (int i = 0; i < 8; ++i)
#pragma unroll
      for (int nt = 0; nt < 3; ++nt) {
        const unsigned o = nt * 32768 + (4 * i + hj) * 1024;
        const v4u f = trfrag(b0 + o, b1 + o);
        cs[nt] = dot2(f.w, ONES2, dot2(f.z, ONES2, dot2(f.y, ONES2, dot2(f.x, ONES2, cs[nt]))));
      }
    const float c01 = halfsum2(cs[0], cs[1]), c2 = halfsum2(cs[2], cs[2]);
    LAS float *csp = (LAS float *)(c.lds + L_CSP) + hj * 96;
    csp[l] = c01;
    if (l < 32) csp[64 + l] = c2;
    BAR();
    if (hj < 2) {
      const int n = hj * 64 + l;
      if (n < 96) {
        const LAS float *q = (const LAS float *)(c.lds + L_CSP) + n;
        ((LAS float *)(c.lds + L_CS))[n] = (q[0] + q[96]) + (q[192] + q[288]);
        ((LAS float *)(c.lds + L_BI))[n] = bias;
      }
    }
    BAR();
    if (PERSIST && PH == PH_UP) stage_w<PH_DOWN>(p, c);
    BAR();
    return;
  }
  const int l = lane_id(), h = l >> 5, tt = rb * 4 + c.w;
  const bool raw = PH == PH_QKV && layer == 0;
  STAMP(c, layer, PH, 0);
  if (c.w == 0 && !raw) { if (PH == PH_QKV) poll(p, c, 0, PH_DOWN, rb * 16 + (l & 15)); else poll(p, c, layer, PH_WO, rb * 16 + (l & 15)); }
  BAR();
  STAMP(c, layer, PH, 1);
  v4u a[32];
  if (raw) {
    const rsrc_t rx = mkrsrc(p.x, 2048 * 1024);
    const unsigned voff = (l & 31) * 1024 + h * 16;
#pragma unroll
    for (int ks = 0; ks < 32; ++ks) a[ks] = __builtin_amdgcn_raw_buffer_load_b128(rx, voff + ks * 32, tt * 32768, 0);
  } else {
    const unsigned base = (PH == PH_QKV ? OFF_X2 : OFF_X1 + layer * 2 * MB) + tt * 32768;
#pragma unroll
    for (int ks = 0; ks < 32; ++ks) a[ks] = ld16(c.rws, l * 16 + (ks & 3) * 1024, base + (ks >> 2) * 4096);
  }
  const LAS char *b0 = c.lds + tr_off(l, raw ? 2 * h : h), *b1 = c.lds + tr_off(l, raw ? 2 * h + 1 : 2 + h);
  f32x16 acc[3] = {};
  float s1 = 0.f, s2 = 0.f, s1b = 0.f, s2b = 0.f;
#pragma unroll
  for (int ks = 0; ks < 32; ++ks) {
#pragma unroll
    for (int nt = 0; nt < 3; ++nt) {
      const unsigned o = nt * 32768 + ks * 1024;
      acc[nt] = mfma(trfrag(b0 + o, b1 + o), a[ks], acc[nt]);
    }
    s1 = dot2(a[ks].x, ONES2, s1); s2 = dot2(a[ks].x, a[ks].x, s2); s1b = dot2(a[ks].y, ONES2, s1b); s2b = dot2(a[ks].y, a[ks].y, s2b);
    s1 = dot2(a[ks].z, ONES2, s1); s2 = dot2(a[ks].z, a[ks].z, s2); s1b = dot2(a[ks].w, ONES2, s1b); s2b = dot2(a[ks].w, a[ks].w, s2b);
  }
  s1 += s1b; s2 += s2b;
  const float mean = halfsum2(s1, s1) * (1.f / 512.f);
  const float var = __builtin_fmaxf(halfsum2(s2, s2) * (1.f / 512.f) - mean * mean, 0.f);
  const float rstd = __builtin_amdgcn_rsqf(var + 1e-5f);
  BAR();
  STAMP(c, layer, PH, 2);
#pragma unroll
  for (int nt = 0; nt < 3; ++nt) {
    const int NT = nt0 + nt;
    float v[16];
    const LAS f32x4 *cs4 = (const LAS f32x4 *)(c.lds + L_CS + nt * 128 + h * 16);
    if constexpr (PH == PH_QKV) {
      const int type = NT >> 4, head = 2 * (NT & 15);
      const float rs = type == 0 ? rstd * (0.25f * 1.4426950408889634f) : rstd, mr = -mean * rs;
#pragma unroll
      for (int g = 0; g < 4; ++g) {
        const f32x4 cs = cs4[2 * g];
#pragma unroll
        for (int e = 0; e < 4; ++e) v[4 * g + e] = __builtin_fmaf(acc[nt][4 * g + e], rs, mr * cs[e]);
      }
      const v4u wa = {pk2(v[0], v[1]), pk2(v[2], v[3]), pk2(v[4], v[5]), pk2(v[6], v[7])};
      const v4u wb = {pk2(v[8], v[9]), pk2(v[10], v[11]), pk2(v[12], v[13]), pk2(v[14], v[15])};
      if (type == 0) {
        const unsigned so = OFF_QF + layer * 2 * MB + (head * 64 + tt) * 1024;
        st16(wa, c.rws, l * 16, so); st16(wb, c.rws, l * 16, so + 65536);
      } else if (type == 1) {
        const unsigned so = OFF_KV + layer * 4 * MB + (head * 64 + tt) * 2048;
        st16(wa, c.rws, l * 16, so); st16(wb, c.rws, l * 16, so + 131072);
        const float na = dot2(wa.w, wa.w, dot2(wa.z, wa.z, dot2(wa.y, wa.y, dot2(wa.x, wa.x, 0.f))));
        const float nb = dot2(wb.w, wb.w, dot2(wb.z, wb.z, dot2(wb.y, wb.y, dot2(wb.x, wb.x, 0.f))));
        const float m = max32(halfsum2(na, nb));
        if ((l & 31) == 31)
          __builtin_amdgcn_raw_buffer_store_b32(__builtin_bit_cast(unsigned, m), c.rws, h * 256, OFF_KN + layer * 8192 + (head * 64 + tt) * 4, SC1);
      } else {
        LAS char *sb = c.lds + L_VSCR + (c.w * 3 + nt) * 2304;
        *(LAS v4u *)(sb + (l & 31) * 32 + h * 16) = wa;
        *(LAS v4u *)(sb + 1152 + (l & 31) * 32 + h * 16) = wb;
        asm volatile("s_waitcnt lgkmcnt(0)" ::: "memory");
        const LAS char *rp = sb + ((l >> 4) & 1) * 1152 + (8 * h + ((l & 15) >> 2)) * 32 + (l & 3) * 8;
        const v4u f0 = trfrag(rp, rp + 128), f1 = trfrag(rp + 512, rp + 640);
        const int i = l & 15, d = (i & 3) | ((i & 4) << 1) | ((i & 8) >> 1);
        const unsigned vo = ((l >> 4) & 1) * 131072 + h * 256 + d * 16, so = OFF_KV + layer * 4 * MB + (head * 64 + tt) * 2048 + 1024;
        st16(f0, c.rws, vo, so); st16(f1, c.rws, vo, so + 512);
      }
    } else {
      const LAS f32x4 *bi4 = (const LAS f32x4 *)(c.lds + L_BI + nt * 128 + h * 16);
      const float mr = -mean * rstd;
#pragma unroll
      for (int g = 0; g < 4; ++g) {
        const f32x4 cs = cs4[2 * g], bi = bi4[2 * g];
#pragma unroll
        for (int e = 0; e < 4; ++e) v[4 * g + e] = mish(__builtin_fmaf(acc[nt][4 * g + e], rstd, __builtin_fmaf(mr, cs[e], bi[e])));
      }
      const v4u wa = {pk2(v[0], v[1]), pk2(v[2], v[3]), pk2(v[4], v[5]), pk2(v[6], v[7])};
      const v4u wb = {pk2(v[8], v[9]), pk2(v[10], v[11]), pk2(v[12], v[13]), pk2(v[14], v[15])};
      if (nt < 2 || cb < 11) {
        const unsigned so = OFF_HF + layer * HF_LAYER + (tt * 86 + 2 * NT) * 1024;
        st16(wa, c.rws, l * 16, so); st16(wb, c.rws, l * 16, so + 1024);
      }
    }
  }
  VMWAIT(0);
  BAR();
  publish(p, c, layer, PH, PH == PH_QKV ? cb * 16 + rb : rb * 16 + cb);
  STAMP(c, layer, PH, 3);
}

template <int PH> __device__ __forceinline__ void phase_res_gemm(const Params &p, const Ctx &c, int layer) {
  constexpr int KB = PH == PH_WO ? 32 : 86, KH = KB / 2;
  const int rb = 4 * c.bx + (c.bj >> 3), cb = c.bj & 7;
  if (c.w >= 4) {
    if (!PERSIST) stage_w<PH>(p, c);
    VMWAIT(0);
    BAR();
    if constexpr (PH == PH_DOWN) {
      BAR();
      stage_w2_tail(p, c);
      VMWAIT(0);
      BAR();
    }
    BAR();
    if (PERSIST) { if (PH == PH_WO) stage_w<PH_UP>(p, c); else if (layer == 0) stage_w<PH_QKV>(p, c); }
    if constexpr (PH == PH_DOWN) BAR();
    BAR();
    return;
  }
  const int l = lane_id(), h = l >> 5, kh = c.w >> 1, tt = rb * 2 + (c.w & 1);
  const unsigned fm = (tt * 32 + cb * 4 + kh * 2) * 1024;
  STAMP(c, layer, PH, 0);
  v4u res[2];
  if (PH == PH_WO && layer == 0) {
    const rsrc_t rx = mkrsrc(p.x, 2048 * 1024);
    const unsigned voff = (l & 31) * 1024 + h * 8, so = tt * 32768 + cb * 128 + kh * 64;
    const v2u r0 = __builtin_amdgcn_raw_buffer_load_b64(rx, voff, so, 0), r1 = __builtin_amdgcn_raw_buffer_load_b64(rx, voff + 16, so, 0);
    const v2u r2 = __builtin_amdgcn_raw_buffer_load_b64(rx, voff + 32, so, 0), r3 = __builtin_amdgcn_raw_buffer_load_b64(rx, voff + 48, so, 0);
    res[0] = v4u{r0.x, r0.y, r1.x, r1.y}; res[1] = v4u{r2.x, r2.y, r3.x, r3.y};
  } else {
    const unsigned so = (PH == PH_WO ? OFF_X2 : OFF_X1 + layer * 2 * MB) + fm;
    res[0] = ld16(c.rws, l * 16, so); res[1] = ld16(c.rws, l * 16 + 1024, so);
  }
  if (c.w == 0) { if (PH == PH_WO) poll(p, c, layer, PH_ATT, (rb >> 2) * 32 + (l & 31)); else poll(p, c, layer, PH_UP, (rb >> 1) * 16 + (l & 15)); }
  BAR();
  STAMP(c, layer, PH, 1);
  v4u a[KH];
  const unsigned base = (PH == PH_WO ? OFF_OF + layer * 2 * MB : OFF_HF + layer * HF_LAYER) + (tt * KB + kh * KH) * 1024;
#pragma unroll
  for (int ks = 0; ks < KH; ++ks) a[ks] = ld16(c.rws, l * 16 + (ks & 3) * 1024, base + (ks >> 2) * 4096);
  const unsigned wo = kh * (PH == PH_WO ? 16 : 39) * 2048;
  const LAS char *b0 = c.lds + wo + tr_off(l, h), *b1 = c.lds + wo + tr_off(l, 2 + h);
  f32x16 acc[2] = {};
#pragma unroll
  for (int ks = 0; ks < KH; ++ks) {
    if (PH == PH_DOWN && (ks == 4 || ks == 39)) BAR();
    const int slot = ks < 39 ? ks : ks - 39;
#pragma unroll
    for (int nt = 0; nt < 2; ++nt) {
      const unsigned o = slot * 2048 + nt * 1024;
      acc[nt] = mfma(trfrag(b0 + o, b1 + o), a[ks], acc[nt]);
    }
  }
  if constexpr (PH == PH_DOWN) BAR();
  {
    const f32x16 s = kh ? acc[0] : acc[1];
    LAS f32x4 *xw = (LAS f32x4 *)(c.lds + L_XCH + c.w * 4096 + l * 16);
#pragma unroll
    for (int g = 0; g < 4; ++g) xw[g * 64] = f32x4{s[4 * g], s[4 * g + 1], s[4 * g + 2], s[4 * g + 3]};
  }
  BAR();
  STAMP(c, layer, PH, 2);
  const f32x16 m = kh ? acc[1] : acc[0];
  const LAS f32x4 *xr = (const LAS f32x4 *)(c.lds + L_XCH + (c.w ^ 2) * 4096 + l * 16);
  unsigned o[8];
#pragma unroll
  for (int g = 0; g < 4; ++g) {
    const f32x4 q = xr[g * 64];
    const unsigned r0 = res[g >> 1][(g & 1) * 2], r1 = res[g >> 1][(g & 1) * 2 + 1];
    o[2 * g] = pk2(m[4 * g] + q[0] + bf_lo(r0), m[4 * g + 1] + q[1] + bf_hi(r0));
    o[2 * g + 1] = pk2(m[4 * g + 2] + q[2] + bf_lo(r1), m[4 * g + 3] + q[3] + bf_hi(r1));
  }
  if (PH == PH_DOWN && layer == 1) {
    const rsrc_t ro = mkrsrc(p.out, 2048 * 1024);
#pragma unroll
    for (int t = 0; t < 2; ++t) {
      auto s0 = __builtin_amdgcn_permlane32_swap(o[4 * t], o[4 * t + 2], false, false);
      auto s1 = __builtin_amdgcn_permlane32_swap(o[4 * t + 1], o[4 * t + 3], false, false);
      __builtin_amdgcn_raw_buffer_store_b128(v4u{s0[0], s1[0], s0[1], s1[1]}, ro, (l & 31) * 1024 + h * 16 + t * 32, tt * 32768 + cb * 128 + kh * 64, 0);
    }
    BAR();
  } else {
    const unsigned so = (PH == PH_WO ? OFF_X1 + layer * 2 * MB : OFF_X2) + fm;
    st16(v4u{o[0], o[1], o[2], o[3]}, c.rws, l * 16, so); st16(v4u{o[4], o[5], o[6], o[7]}, c.rws, l * 16 + 1024, so);
    VMWAIT(0);
    BAR();
    publish(p, c, layer, PH, rb * 8 + cb);
  }
  STAMP(c, layer, PH, 3);
}

#define ATTN_FAST_ASM \
  "v_mov_b32 v236, %10\n\t" \
  "v_mov_b32 v237, %11\n\t" \
  "v_mov_b32 v238, %12\n\t" \
  "v_mov_b32 v239, %13\n\t" \
  "v_mov_b32 v240, %14\n\t" \
  "v_mov_b32 v241, %15\n\t" \
  "v_mov_b32 v242, %16\n\t" \
  "ds_read_b128 v[224:227], v240\n\t" \
  "v_mov_b32 v243, 0xdddd\n\t" \
  "v_mov_b32 v208, 0\n\t" \
  "v_mov_b32 v209, 0\n\t" \
  "v_mov_b32 v210, 0\n\t" \
  "v_mov_b32 v211, 0\n\t" \
  "v_mov_b32 v212, 0\n\t" \
  "v_mov_b32 v213, 0\n\t" \
  "v_mov_b32 v214, 0\n\t" \
  "v_mov_b32 v215, 0\n\t" \
  "v_mov_b32 v216, 0\n\t" \
  "v_mov_b32 v217, 0\n\t" \
  "v_mov_b32 v218, 0\n\t" \
  "v_mov_b32 v219, 0\n\t" \
  "v_mov_b32 v220, 0\n\t" \
  "v_mov_b32 v221, 0\n\t" \
  "v_mov_b32 v222, 0\n\t" \
  "v_mov_b32 v223, 0\n\t" \
  "v_mov_b32 v200, 0\n\t" \
  "v_mov_b32 v201, 0\n\t" \
  "v_mov_b32 v202, 0\n\t" \
  "v_mov_b32 v203, 0\n\t" \
  "v_mov_b32 v204, 0\n\t" \
  "v_mov_b32 v205, 0\n\t" \
  "v_mov_b32 v206, 0\n\t" \
  "v_mov_b32 v207, 0\n\t" \
  "s_mov_b32 %9, 0\n\t" \
  "s_waitcnt lgkmcnt(0)\n\t" \
  "v_mfma_f32_32x32x16_bf16 v[160:175], v[224:227], v[236:239], 0\n\t" \
  "ds_read_b128 v[224:227], v240 offset:2064\n\t" \
  "ds_read_b128 v[232:235], v242\n\t" \
  "ds_read_b128 v[228:231], v241\n\t" \
  "s_nop 9\n\t" \
  "1:\n\t" \
  "s_waitcnt lgkmcnt(2)\n\t" \
  "v_mfma_f32_32x32x16_bf16 v[176:191], v[224:227], v[236:239], 0\n\t" \
  "ds_read_b128 v[224:227], v240 offset:4128\n\t" \
  "v_exp_f32 v192, v160\n\t" \
  "v_exp_f32 v193, v161\n\t" \
  "v_exp_f32 v194, v162\n\t" \
  "v_exp_f32 v195, v163\n\t" \
  "v_exp_f32 v196, v164\n\t" \
  "s_waitcnt lgkmcnt(2)\n\t" \
  "v_smfmac_f32_32x32x32_bf16 v[208:223], v[232:235], v[200:207], v243\n\t" \
  "ds_read_b128 v[232:235], v242 offset:0\n\t" \
  "v_exp_f32 v197, v165\n\t" \
  "v_exp_f32 v198, v166\n\t" \
  "v_exp_f32 v199, v167\n\t" \
  "v_exp_f32 v200, v168\n\t" \
  "v_exp_f32 v201, v169\n\t" \
  "v_exp_f32 v202, v170\n\t" \
  "s_waitcnt lgkmcnt(2)\n\t" \
  "v_smfmac_f32_32x32x32_bf16 v[208:223], v[228:231], v[192:199], v243\n\t" \
  "ds_read_b128 v[228:231], v241 offset:2064\n\t" \
  "v_exp_f32 v203, v171\n\t" \
  "v_exp_f32 v204, v172\n\t" \
  "v_exp_f32 v205, v173\n\t" \
  "v_exp_f32 v206, v174\n\t" \
  "v_exp_f32 v207, v175\n\t" \
  "s_waitcnt lgkmcnt(2)\n\t" \
  "v_mfma_f32_32x32x16_bf16 v[160:175], v[224:227], v[236:239], 0\n\t" \
  "ds_read_b128 v[224:227], v240 offset:6192\n\t" \
  "v_exp_f32 v192, v176\n\t" \
  "v_exp_f32 v193, v177\n\t" \
  "v_exp_f32 v194, v178\n\t" \
  "v_exp_f32 v195, v179\n\t" \
  "v_exp_f32 v196, v180\n\t" \
  "s_waitcnt lgkmcnt(2)\n\t" \
  "v_smfmac_f32_32x32x32_bf16 v[208:223], v[232:235], v[200:207], v243\n\t" \
  "ds_read_b128 v[232:235], v242 offset:2064\n\t" \
  "v_exp_f32 v197, v181\n\t" \
  "v_exp_f32 v198, v182\n\t" \
  "v_exp_f32 v199, v183\n\t" \
  "v_exp_f32 v200, v184\n\t" \
  "v_exp_f32 v201, v185\n\t" \
  "v_exp_f32 v202, v186\n\t" \
  "s_waitcnt lgkmcnt(2)\n\t" \
  "v_smfmac_f32_32x32x32_bf16 v[208:223], v[228:231], v[192:199], v243\n\t" \
  "ds_read_b128 v[228:231], v241 offset:4128\n\t" \
  "v_exp_f32 v203, v187\n\t" \
  "v_exp_f32 v204, v188\n\t" \
  "v_exp_f32 v205, v189\n\t" \
  "v_exp_f32 v206, v190\n\t" \
  "v_exp_f32 v207, v191\n\t" \
  "s_waitcnt lgkmcnt(2)\n\t" \
  "v_mfma_f32_32x32x16_bf16 v[176:191], v[224:227], v[236:239], 0\n\t" \
  "ds_read_b128 v[224:227], v240 offset:8256\n\t" \
  "v_exp_f32 v192, v160\n\t" \
  "v_exp_f32 v193, v161\n\t" \
  "v_exp_f32 v194, v162\n\t" \
  "v_exp_f32 v195, v163\n\t" \
  "v_exp_f32 v196, v164\n\t" \
  "s_waitcnt lgkmcnt(2)\n\t" \
  "v_smfmac_f32_32x32x32_bf16 v[208:223], v[232:235], v[200:207], v243\n\t" \
  "ds_read_b128 v[232:235], v242 offset:4128\n\t" \
  "v_exp_f32 v197, v165\n\t" \
  "v_exp_f32 v198, v166\n\t" \
  "v_exp_f32 v199, v167\n\t" \
  "v_exp_f32 v200, v168\n\t" \
  "v_exp_f32 v201, v169\n\t" \
  "v_exp_f32 v202, v170\n\t" \
  "s_waitcnt lgkmcnt(2)\n\t" \
  "v_smfmac_f32_32x32x32_bf16 v[208:223], v[228:231], v[192:199], v243\n\t" \
  "ds_read_b128 v[228:231], v241 offset:6192\n\t" \
  "v_exp_f32 v203, v171\n\t" \
  "v_exp_f32 v204, v172\n\t" \
  "v_exp_f32 v205, v173\n\t" \
  "v_exp_f32 v206, v174\n\t" \
  "v_exp_f32 v207, v175\n\t" \
  "s_waitcnt lgkmcnt(2)\n\t" \
  "v_mfma_f32_32x32x16_bf16 v[160:175], v[224:227], v[236:239], 0\n\t" \
  "ds_read_b128 v[224:227], v240 offset:10320\n\t" \
  "v_exp_f32 v192, v176\n\t" \
  "v_exp_f32 v193, v177\n\t" \
  "v_exp_f32 v194, v178\n\t" \
  "v_exp_f32 v195, v179\n\t" \
  "v_exp_f32 v196, v180\n\t" \
  "s_waitcnt lgkmcnt(2)\n\t" \
  "v_smfmac_f32_32x32x32_bf16 v[208:223], v[232:235], v[200:207], v243\n\t" \
  "ds_read_b128 v[232:235], v242 offset:6192\n\t" \
  "v_exp_f32 v197, v181\n\t" \
  "v_exp_f32 v198, v182\n\t" \
  "v_exp_f32 v199, v183\n\t" \
  "v_exp_f32 v200, v184\n\t" \
  "v_exp_f32 v201, v185\n\t" \
  "v_exp_f32 v202, v186\n\t" \
  "s_waitcnt lgkmcnt(2)\n\t" \
  "v_smfmac_f32_32x32x32_bf16 v[208:223], v[228:231], v[192:199], v243\n\t" \
  "ds_read_b128 v[228:231], v241 offset:8256\n\t" \
  "v_exp_f32 v203, v187\n\t" \
  "v_exp_f32 v204, v188\n\t" \
  "v_exp_f32 v205, v189\n\t" \
  "v_exp_f32 v206, v190\n\t" \
  "v_exp_f32 v207, v191\n\t" \
  "s_waitcnt lgkmcnt(2)\n\t" \
  "v_mfma_f32_32x32x16_bf16 v[176:191], v[224:227], v[236:239], 0\n\t" \
  "ds_read_b128 v[224:227], v240 offset:12384\n\t" \
  "v_exp_f32 v192, v160\n\t" \
  "v_exp_f32 v193, v161\n\t" \
  "v_exp_f32 v194, v162\n\t" \
  "v_exp_f32 v195, v163\n\t" \
  "v_exp_f32 v196, v164\n\t" \
  "s_waitcnt lgkmcnt(2)\n\t" \
  "v_smfmac_f32_32x32x32_bf16 v[208:223], v[232:235], v[200:207], v243\n\t" \
  "ds_read_b128 v[232:235], v242 offset:8256\n\t" \
  "v_exp_f32 v197, v165\n\t" \
  "v_exp_f32 v198, v166\n\t" \
  "v_exp_f32 v199, v167\n\t" \
  "v_exp_f32 v200, v168\n\t" \
  "v_exp_f32 v201, v169\n\t" \
  "v_exp_f32 v202, v170\n\t" \
  "s_waitcnt lgkmcnt(2)\n\t" \
  "v_smfmac_f32_32x32x32_bf16 v[208:223], v[228:231], v[192:199], v243\n\t" \
  "ds_read_b128 v[228:231], v241 offset:10320\n\t" \
  "v_exp_f32 v203, v171\n\t" \
  "v_exp_f32 v204, v172\n\t" \
  "v_exp_f32 v205, v173\n\t" \
  "v_exp_f32 v206, v174\n\t" \
  "v_exp_f32 v207, v175\n\t" \
  "s_waitcnt lgkmcnt(2)\n\t" \
  "v_mfma_f32_32x32x16_bf16 v[160:175], v[224:227], v[236:239], 0\n\t" \
  "ds_read_b128 v[224:227], v240 offset:14448\n\t" \
  "v_exp_f32 v192, v176\n\t" \
  "v_exp_f32 v193, v177\n\t" \
  "v_exp_f32 v194, v178\n\t" \
  "v_exp_f32 v195, v179\n\t" \
  "v_exp_f32 v196, v180\n\t" \
  "s_waitcnt lgkmcnt(2)\n\t" \
  "v_smfmac_f32_32x32x32_bf16 v[208:223], v[232:235], v[200:207], v243\n\t" \
  "ds_read_b128 v[232:235], v242 offset:10320\n\t" \
  "v_exp_f32 v197, v181\n\t" \
  "v_exp_f32 v198, v182\n\t" \
  "v_exp_f32 v199, v183\n\t" \
  "v_exp_f32 v200, v184\n\t" \
  "v_exp_f32 v201, v185\n\t" \
  "v_exp_f32 v202, v186\n\t" \
  "s_waitcnt lgkmcnt(2)\n\t" \
  "v_smfmac_f32_32x32x32_bf16 v[208:223], v[228:231], v[192:199], v243\n\t" \
  "ds_read_b128 v[228:231], v241 offset:12384\n\t" \
  "v_exp_f32 v203, v187\n\t" \
  "v_exp_f32 v204, v188\n\t" \
  "v_exp_f32 v205, v189\n\t" \
  "v_exp_f32 v206, v190\n\t" \
  "v_exp_f32 v207, v191\n\t" \
  "s_waitcnt lgkmcnt(2)\n\t" \
  "v_mfma_f32_32x32x16_bf16 v[176:191], v[224:227], v[236:239], 0\n\t" \
  "ds_read_b128 v[224:227], v240 offset:16512\n\t" \
  "v_exp_f32 v192, v160\n\t" \
  "v_exp_f32 v193, v161\n\t" \
  "v_exp_f32 v194, v162\n\t" \
  "v_exp_f32 v195, v163\n\t" \
  "v_exp_f32 v196, v164\n\t" \
  "s_waitcnt lgkmcnt(2)\n\t" \
  "v_smfmac_f32_32x32x32_bf16 v[208:223], v[232:235], v[200:207], v243\n\t" \
  "ds_read_b128 v[232:235], v242 offset:12384\n\t" \
  "v_exp_f32 v197, v165\n\t" \
  "v_exp_f32 v198, v166\n\t" \
  "v_exp_f32 v199, v167\n\t" \
  "v_exp_f32 v200, v168\n\t" \
  "v_exp_f32 v201, v169\n\t" \
  "v_exp_f32 v202, v170\n\t" \
  "s_waitcnt lgkmcnt(2)\n\t" \
  "v_smfmac_f32_32x32x32_bf16 v[208:223], v[228:231], v[192:199], v243\n\t" \
  "ds_read_b128 v[228:231], v241 offset:14448\n\t" \
  "v_exp_f32 v203, v171\n\t" \
  "v_exp_f32 v204, v172\n\t" \
  "v_exp_f32 v205, v173\n\t" \
  "v_exp_f32 v206, v174\n\t" \
  "v_exp_f32 v207, v175\n\t" \
  "s_waitcnt lgkmcnt(2)\n\t" \
  "v_mfma_f32_32x32x16_bf16 v[160:175], v[224:227], v[236:239], 0\n\t" \
  "ds_read_b128 v[224:227], v240 offset:18576\n\t" \
  "v_exp_f32 v192, v176\n\t" \
  "v_exp_f32 v193, v177\n\t" \
  "v_exp_f32 v194, v178\n\t" \
  "v_exp_f32 v195, v179\n\t" \
  "v_exp_f32 v196, v180\n\t" \
  "s_waitcnt lgkmcnt(2)\n\t" \
  "v_smfmac_f32_32x32x32_bf16 v[208:223], v[232:235], v[200:207], v243\n\t" \
  "ds_read_b128 v[232:235], v242 offset:14448\n\t" \
  "v_exp_f32 v197, v181\n\t" \
  "v_exp_f32 v198, v182\n\t" \
  "v_exp_f32 v199, v183\n\t" \
  "v_exp_f32 v200, v184\n\t" \
  "v_exp_f32 v201, v185\n\t" \
  "v_exp_f32 v202, v186\n\t" \
  "s_waitcnt lgkmcnt(2)\n\t" \
  "v_smfmac_f32_32x32x32_bf16 v[208:223], v[228:231], v[192:199], v243\n\t" \
  "ds_read_b128 v[228:231], v241 offset:16512\n\t" \
  "v_exp_f32 v203, v187\n\t" \
  "v_exp_f32 v204, v188\n\t" \
  "v_exp_f32 v205, v189\n\t" \
  "v_exp_f32 v206, v190\n\t" \
  "v_exp_f32 v207, v191\n\t" \
  "v_add_u32 v240, 16512, v240\n\t" \
  "v_add_u32 v241, 16512, v241\n\t" \
  "v_add_u32 v242, 16512, v242\n\t" \
  "s_add_u32 %9, %9, 1\n\t" \
  "s_cmp_lg_u32 %9, 1\n\t" \
  "s_cbranch_scc1 2f\n\t" \
  "s_waitcnt vmcnt(0)\n\t" \
  "s_barrier\n\t" \
  "2:\n\t" \
  "s_cmp_lg_u32 %9, 8\n\t" \
  "s_cbranch_scc1 1b\n\t" \
  "s_waitcnt lgkmcnt(0)\n\t" \
  "s_nop 1\n\t" \
  "v_smfmac_f32_32x32x32_bf16 v[208:223], v[232:235], v[200:207], v243\n\t" \
  "s_nop 15\n\t" \
  "v_mov_b32 %0, v208\n\t" \
  "v_mov_b32 %1, v209\n\t" \
  "v_mov_b32 %2, v210\n\t" \
  "v_mov_b32 %3, v211\n\t" \
  "v_mov_b32 %4, v212\n\t" \
  "v_mov_b32 %5, v213\n\t" \
  "v_mov_b32 %6, v214\n\t" \
  "v_mov_b32 %7, v215\n\t" \
  "v_mov_b32 %8, v216\n\t"
#define ATTN_FAST_CLOBBERS "v160", "v161", "v162", "v163", "v164", "v165", "v166", "v167", "v168", "v169", "v170", "v171", "v172", "v173", "v174", "v175", "v176", "v177", "v178", "v179", "v180", "v181", "v182", "v183", "v184", "v185", "v186", "v187", "v188", "v189", "v190", "v191", "v192", "v193", "v194", "v195", "v196", "v197", "v198", "v199", "v200", "v201", "v202", "v203", "v204", "v205", "v206", "v207", "v208", "v209", "v210", "v211", "v212", "v213", "v214", "v215", "v216", "v217", "v218", "v219", "v220", "v221", "v222", "v223", "v224", "v225", "v226", "v227", "v228", "v229", "v230", "v231", "v232", "v233", "v234", "v235", "v236", "v237", "v238", "v239", "v240", "v241", "v242", "v243"

__device__ __forceinline__ void phase_attn(const Params &p, const Ctx &c, int layer) {
  const int head = 4 * c.bx + (c.bj >> 3), qb = c.bj & 7, l = lane_id(), h = l >> 5, tt = qb * 8 + c.w;
  STAMP(c, layer, PH_ATT, 0);
  if (c.w == 7) *(LAS v4u *)(c.lds + l * KVP + 2048) = v4u{ONES2, ONES2, ONES2, ONES2};
  if (c.w == 0) {
    const int hp = head >> 1, cq = hp / 3, ck = (16 + hp) / 3, cv = (32 + hp) / 3;
    poll(p, c, layer, PH_QKV, l < 16 ? ck * 16 + l : l < 32 ? cv * 16 + (l & 15) : cq * 16 + 2 * qb + (l & 1));
  }
  BAR();
  STAMP(c, layer, PH_ATT, 1);
  v4u q; float kn;
  {
    const unsigned sq = OFF_QF + layer * 2 * MB + (head * 64 + tt) * 1024, sk = OFF_KN + layer * 8192 + head * 256;
    asm volatile("s_nop 4\n\tbuffer_load_dwordx4 %0, %2, %4, %5 offen sc1\n\tbuffer_load_dword %1, %3, %4, %6 offen sc1"
                 : "=&v"(q), "=&v"(kn) : "v"(l * 16), "v"(l * 4), "s"(c.rws), "s"(sq), "s"(sk) : "memory");
    const unsigned skv = OFF_KV + layer * 4 * MB + head * 131072;
#pragma unroll
    for (int i = 0; i < 8; ++i) {
      const int t = 8 * i + c.w;
      glds<true>(c.rws, l * 16, skv + t * 2048, c.lds0 + t * KVP);
      glds<true>(c.rws, l * 16, skv + t * 2048 + 1024, c.lds0 + t * KVP + 1024);
    }
    asm volatile("s_waitcnt vmcnt(12)\n\ts_barrier" : "+v"(q), "+v"(kn) :: "memory");
  }
  float km = max32(kn);
  km = __builtin_fmaxf(__builtin_bit_cast(float, __builtin_amdgcn_readlane(__builtin_bit_cast(int, km), 31)),
                       __builtin_bit_cast(float, __builtin_amdgcn_readlane(__builtin_bit_cast(int, km), 63)));
  const float qp = dot2(q.w, q.w, dot2(q.z, q.z, dot2(q.y, q.y, dot2(q.x, q.x, 0.f))));
  const float qn = halfsum2(qp, qp);
  const unsigned ka = c.lds0 + l * 16;
  const unsigned va0 = c.lds0 + ((l & 16) ? 2048 : 1024 + h * 256 + (l & 15) * 16), va1 = va0 + ((l & 16) ? 0 : 512);
  float o[9];
  if (__all(qn * km <= 4096.f)) {
    unsigned cnt;
    asm volatile(ATTN_FAST_ASM
                 : "=v"(o[0]), "=v"(o[1]), "=v"(o[2]), "=v"(o[3]), "=v"(o[4]), "=v"(o[5]), "=v"(o[6]), "=v"(o[7]), "=v"(o[8]), "=&s"(cnt)
                 : "v"(q.x), "v"(q.y), "v"(q.z), "v"(q.w), "v"(ka), "v"(va0), "v"(va1)
                 : "memory", "scc", ATTN_FAST_CLOBBERS);
  } else {
    asm volatile("s_waitcnt vmcnt(0)\n\ts_barrier" ::: "memory");
    f32x16 acc = {};
    float m = -__builtin_inff();
    for (int t = 0; t < 64; ++t) {
      const v4u kf = *(const LAS v4u *)(uintptr_t)(ka + t * KVP);
      const v4u v0 = *(const LAS v4u *)(uintptr_t)(va0 + t * KVP), v1 = *(const LAS v4u *)(uintptr_t)(va1 + t * KVP);
      f32x16 s = mfma(kf, q, f32x16{});
      float tm = s[0];
#pragma unroll
      for (int r = 1; r < 16; ++r) tm = __builtin_fmaxf(tm, s[r]);
      auto sw = __builtin_amdgcn_permlane32_swap(__builtin_bit_cast(unsigned, tm), __builtin_bit_cast(unsigned, tm), false, false);
      const unsigned sw0 = sw[0], sw1 = sw[1];
      const float mn = __builtin_fmaxf(m, __builtin_fmaxf(__uint_as_float(sw0), __uint_as_float(sw1)));
      const float al = __builtin_amdgcn_exp2f(m - mn);
      m = mn;
      v8u p0, p1;
#pragma unroll
      for (int r = 0; r < 8; ++r) {
        p0[r] = __builtin_bit_cast(unsigned, __builtin_amdgcn_exp2f(s[r] - mn));
        p1[r] = __builtin_bit_cast(unsigned, __builtin_amdgcn_exp2f(s[8 + r] - mn));
      }
#pragma unroll
      for (int r = 0; r < 16; ++r) acc[r] *= al;
      acc = __builtin_amdgcn_smfmac_f32_32x32x32_bf16(__builtin_bit_cast(bf16x8, v0), __builtin_bit_cast(bf16x16, p0), acc, 0xdddd, 0, 0);
      acc = __builtin_amdgcn_smfmac_f32_32x32x32_bf16(__builtin_bit_cast(bf16x8, v1), __builtin_bit_cast(bf16x16, p1), acc, 0xdddd, 0, 0);
    }
#pragma unroll
    for (int r = 0; r < 9; ++r) o[r] = acc[r];
  }
  STAMP(c, layer, PH_ATT, 2);
  const float inv = __builtin_amdgcn_rcpf(o[8]);
  st16(v4u{pk2(o[0] * inv, o[1] * inv), pk2(o[2] * inv, o[3] * inv), pk2(o[4] * inv, o[5] * inv), pk2(o[6] * inv, o[7] * inv)},
       c.rws, l * 16, OFF_OF + layer * 2 * MB + (tt * 32 + head) * 1024);
  BAR();
  if (PERSIST && c.w >= 4) { stage_w<PH_WO>(p, c); VMWAIT(16); } else VMWAIT(0);
  BAR();
  publish(p, c, layer, PH_ATT, qb * 32 + head);
  STAMP(c, layer, PH_ATT, 3);
}

__device__ __forceinline__ Ctx make_ctx(const Params &p) {
  extern __shared__ __attribute__((aligned(16))) char smem[];
  Ctx c;
  c.rws = mkrsrc(p.ws, WS_BYTES);
  c.lds = (LAS char *)smem;
  c.lds0 = (unsigned)(uintptr_t)c.lds;
  c.w = __builtin_amdgcn_readfirstlane(threadIdx.x >> 6);
  c.bx = blockIdx.x & 7; c.bj = blockIdx.x >> 3;
  c.tag = 0;
  return c;
}

template <int PH> __global__ __launch_bounds__(512, 2) void phase_kernel(Params p, int layer, int pad) {
  Ctx c = make_ctx(p);
  if constexpr (PH == PH_ATT) phase_attn(p, c, layer);
  else if constexpr (PH == PH_QKV || PH == PH_UP) phase_ln_gemm<PH>(p, c, layer);
  else phase_res_gemm<PH>(p, c, layer);
}

extern "C" void kernel_launch(void *const *d_in, const int *in_sizes, int n_in, void *d_out, int out_size, void *d_ws, size_t ws_size,
                              hipStream_t stream) {
  Params p;
  p.x = d_in[0]; p.wqkv = d_in[1]; p.wo = d_in[2]; p.w1 = d_in[4]; p.b1 = d_in[5]; p.w2 = d_in[6];
  p.out = d_out; p.ws = (char *)d_ws;
#define LAUNCH(PH) do { (void)hipFuncSetAttribute((const void *)phase_kernel<PH>, hipFuncAttributeMaxDynamicSharedMemorySize, LDS_BYTES); \
    phase_kernel<PH><<<256, 512, LDS_BYTES, stream>>>(p, layer, 0); } while (0)
  for (int layer = 0; layer < 2; ++layer) { LAUNCH(PH_QKV); LAUNCH(PH_ATT); LAUNCH(PH_WO); LAUNCH(PH_UP); LAUNCH(PH_DOWN); }
}
```

```cpp
#include <hip/hip_runtime.h>
#include <hip/hip_bf16.h>
#include <stdint.h>
#include <type_traits>

#define STAMPS 0

#define LAS __attribute__((address_space(3)))
#define GAS __attribute__((address_space(1)))
typedef unsigned v4u __attribute__((ext_vector_type(4)));
typedef unsigned v2u __attribute__((ext_vector_type(2)));
typedef float f32x2 __attribute__((ext_vector_type(2)));
typedef float f32x4 __attribute__((ext_vector_type(4)));
typedef float f32x16 __attribute__((ext_vector_type(16)));
typedef __bf16 bf16x2 __attribute__((ext_vector_type(2)));
typedef __bf16 bf16x8 __attribute__((ext_vector_type(8)));
typedef __bf16 bf16x16 __attribute__((ext_vector_type(16)));
typedef short s16x4 __attribute__((ext_vector_type(4)));
typedef unsigned v8u __attribute__((ext_vector_type(8)));
typedef unsigned v16u __attribute__((ext_vector_type(16)));
typedef __amdgpu_buffer_rsrc_t rsrc_t;
typedef unsigned long long u64;

constexpr unsigned MB = 1u << 20;
constexpr unsigned OFF_EPOCH = 0;
constexpr unsigned OFF_FLAGS = 65536;
constexpr unsigned OFF_KN = 32768;
constexpr unsigned OFF_QF = MB;
constexpr unsigned OFF_KV = OFF_QF + 4 * MB;
constexpr unsigned OFF_OF = OFF_KV + 8 * MB;
constexpr unsigned OFF_X1 = OFF_OF + 4 * MB;
constexpr unsigned OFF_X2 = OFF_X1 + 4 * MB;
constexpr unsigned OFF_HF = OFF_X2 + 2 * MB;
constexpr unsigned HF_LAYER = 6 * MB;
constexpr unsigned OFF_ST = OFF_HF + 2 * HF_LAYER;
constexpr unsigned OFF_WT1 = OFF_ST + MB;
constexpr unsigned OFF_WTO = OFF_WT1 + 43 * 33 * 1024, OFF_WT2 = OFF_WTO + 16 * 32 * 1024, OFF_WTQ = OFF_WT2 + 16 * 86 * 1024;
constexpr unsigned WS_BYTES = OFF_WTQ + 48 * 33 * 1024;
constexpr unsigned LDS_BYTES = 163840;
constexpr unsigned NTP = 33 * 1024;
constexpr unsigned L_PREP = 3 * NTP;
constexpr unsigned L_VSCR = L_PREP + 32768;
constexpr unsigned L_XCH_WO = L_PREP, L_XCH_DN = 65536;
constexpr unsigned L_APART = 136 * 1024;
constexpr unsigned L_DUMMY = 135168;
constexpr unsigned L_CNT = 159744;
constexpr unsigned L_JUNK = L_CNT + 1024;
constexpr unsigned KVP = 2080;
constexpr int SC1 = 16;
#define NW_WO 16
#define REWARM 0
#define FSTR 128
#define PSLEEP 1
enum { PH_QKV = 0, PH_ATT = 1, PH_WO = 2, PH_UP = 3, PH_DOWN = 4, SLOT_PREP = 10 };

struct Params {
  const void *x, *wqkv, *wo, *w1, *b1, *w2;
  void *out;
  char *ws;
};

struct Ctx {
  rsrc_t rws;
  LAS char *lds;
  unsigned lds0;
  int w;
  int bx, bj;
  u64 tag;
};

#define SB() __builtin_amdgcn_sched_barrier(0)
#define OPAQUE_V(x) asm volatile("" : "+v"(x))
__device__ __forceinline__ Ctx fresh(const Ctx &c0) { Ctx c = c0; asm volatile("" : "+s"(c.w), "+s"(c.bx), "+s"(c.bj), "+s"(c.lds0), "+s"(c.tag)); return c; }
#define BAR() asm volatile("s_waitcnt lgkmcnt(0)\n\ts_barrier" ::: "memory")
#define VMWAIT(n) asm volatile("s_waitcnt vmcnt(" #n ")" ::: "memory")
__device__ __forceinline__ unsigned rfl(unsigned v) { return (unsigned)__builtin_amdgcn_readfirstlane((int)v); }
__device__ __forceinline__ u64 rfl64(u64 v) { return (u64)rfl((unsigned)v) | ((u64)rfl((unsigned)(v >> 32)) << 32); }
__device__ __forceinline__ rsrc_t mkrsrc(const void *p, unsigned bytes) { return __builtin_amdgcn_make_buffer_rsrc((void *)p, (short)0, (int)bytes, 0x00020000); }
__device__ __forceinline__ int lane_id() { int l; asm volatile("v_mbcnt_lo_u32_b32 %0, -1, 0\n\tv_mbcnt_hi_u32_b32 %0, -1, %0" : "=v"(l)); return l; }
__device__ __forceinline__ f32x16 mfma(v4u a, v4u b, f32x16 c) {
  return __builtin_amdgcn_mfma_f32_32x32x16_bf16(__builtin_bit_cast(bf16x8, a), __builtin_bit_cast(bf16x8, b), c, 0, 0, 0);
}
__device__ __forceinline__ unsigned pk2(float lo, float hi) { bf16x2 v = {(__bf16)lo, (__bf16)hi}; return __builtin_bit_cast(unsigned, v); }
__device__ __forceinline__ float dot2(unsigned a, unsigned b, float c) {
  return __builtin_amdgcn_fdot2_f32_bf16(__builtin_bit_cast(bf16x2, a), __builtin_bit_cast(bf16x2, b), c, false);
}
constexpr unsigned ONES2 = 0x3f803f80u;
__device__ __forceinline__ float bf_lo(unsigned u) { return __builtin_bit_cast(float, u << 16); }
__device__ __forceinline__ float bf_hi(unsigned u) { return __builtin_bit_cast(float, u & 0xffff0000u); }
__device__ __forceinline__ float halfsum2(float a, float b) {
  auto r = __builtin_amdgcn_permlane32_swap(__builtin_bit_cast(unsigned, a), __builtin_bit_cast(unsigned, b), false, false);
  const unsigned r0 = r[0], r1 = r[1];
  return __uint_as_float(r0) + __uint_as_float(r1);
}
template <int CTRL, int RMASK> __device__ __forceinline__ float dppmax(float v) {
  const int o = __builtin_amdgcn_update_dpp(0, __builtin_bit_cast(int, v), CTRL, RMASK, 0xf, true);
  return __builtin_fmaxf(v, __builtin_bit_cast(float, o));
}
__device__ __forceinline__ float max32(float v) {
  v = dppmax<0x111, 0xf>(v); v = dppmax<0x112, 0xf>(v); v = dppmax<0x114, 0xf>(v); v = dppmax<0x118, 0xf>(v);
  return dppmax<0x142, 0xa>(v);
}
template <bool DEV> __device__ __forceinline__ void glds(rsrc_t rs, unsigned voff, unsigned soff, unsigned ldsaddr) {
  unsigned keep;
  if constexpr (DEV)
    asm volatile("s_mov_b32 %0, m0\n\ts_mov_b32 m0, %1\n\ts_nop 2\n\tbuffer_load_dwordx4 %2, %3, %4 offen sc1 lds\n\ts_mov_b32 m0, %0"
                 : "=&s"(keep) : "s"(ldsaddr), "v"(voff), "s"(rs), "s"(soff) : "memory");
  else
    asm volatile("s_mov_b32 %0, m0\n\ts_mov_b32 m0, %1\n\ts_nop 2\n\tbuffer_load_dwordx4 %2, %3, %4 offen lds\n\ts_mov_b32 m0, %0"
                 : "=&s"(keep) : "s"(ldsaddr), "v"(voff), "s"(rs), "s"(soff) : "memory");
}
__device__ __forceinline__ v2u trd(const LAS char *p) {
  return __builtin_bit_cast(v2u, __builtin_amdgcn_ds_read_tr16_b64_v4i16((LAS s16x4 *)p));
}
__device__ __forceinline__ v4u trfrag(const LAS char *p0, const LAS char *p1) { const v2u a = trd(p0), b = trd(p1); return v4u{a.x, a.y, b.x, b.y}; }
__device__ __forceinline__ v4u ld16(rsrc_t rs, unsigned voff, unsigned soff) { return __builtin_amdgcn_raw_buffer_load_b128(rs, voff, soff, SC1); }
__device__ __forceinline__ void st16(v4u v, rsrc_t rs, unsigned voff, unsigned soff) { __builtin_amdgcn_raw_buffer_store_b128(v, rs, voff, soff, SC1); }

#define STAMP(layer, ph, i) do {} while (0)
#define STAMP_ARGS
#define STAMP_PASS

__device__ __forceinline__ GAS u64 *flag_ptr(const Params &p, int slot, int idx) { return (GAS u64 *)(p.ws + OFF_FLAGS + (slot * 256 + idx) * FSTR); }
__device__ __forceinline__ LAS unsigned *cnt_ptr(const Ctx &c, int i) { return (LAS unsigned *)(c.lds + L_CNT) + i; }
__device__ __forceinline__ void publish(const Params &p, const Ctx &c, int slot, int idx) {
  VMWAIT(0);
  if (lane_id() == 0) {
    const unsigned old = __hip_atomic_fetch_add(cnt_ptr(c, slot), 1u, __ATOMIC_RELAXED, __HIP_MEMORY_SCOPE_WORKGROUP);
    if (old == 3) __hip_atomic_store(flag_ptr(p, slot, idx), c.tag, __ATOMIC_RELAXED, __HIP_MEMORY_SCOPE_AGENT);
  }
}
__device__ __forceinline__ void poll(const Params &p, const Ctx &c, int slot, int idx  ) {
  GAS u64 *f = flag_ptr(p, slot, idx);
  bool wait = idx >= 0;
#pragma unroll 1
  for (unsigned spins = 0; spins < (1u << 20); ++spins) {
    if (wait) wait = __hip_atomic_load(f, __ATOMIC_RELAXED, __HIP_MEMORY_SCOPE_AGENT) != c.tag;
    if (!__any(wait)) break;
    __builtin_amdgcn_s_sleep(PSLEEP);
  }
  __builtin_amdgcn_fence(__ATOMIC_ACQUIRE, "wavefront");
}
__device__ __forceinline__ void lds_arrive(const Ctx &c, int i) {
  asm volatile("" ::: "memory");
  if (lane_id() == 0) __hip_atomic_fetch_add(cnt_ptr(c, i), 1u, __ATOMIC_RELAXED, __HIP_MEMORY_SCOPE_WORKGROUP);
}
__device__ __forceinline__ void lds_wait(const Ctx &c, int i, unsigned n) {
#pragma unroll 1
  while (__hip_atomic_load(cnt_ptr(c, i), __ATOMIC_RELAXED, __HIP_MEMORY_SCOPE_WORKGROUP) < n) __builtin_amdgcn_s_sleep(1);
  asm volatile("" ::: "memory");
}

__device__ __forceinline__ int up_nt0(int cb) { return cb < 11 ? 3 * cb : 33 + 2 * (cb - 11); }
__device__ __forceinline__ float colsum(v4u f, float cs) { return dot2(f.w, ONES2, dot2(f.z, ONES2, dot2(f.y, ONES2, dot2(f.x, ONES2, cs)))); }
__device__ __forceinline__ v2u aug_quad(float cs, unsigned bias_bits) {
  const unsigned hi = pk2(cs, 0.f) & 0xffffu;
  return v2u{pk2(cs, cs - bf_lo(hi)), hi | (bias_bits << 16)};
}
template <int I> __device__ __forceinline__ v4u frag(const v16u (&pre)[8]) {
  return __builtin_shufflevector(pre[I >> 2], pre[I >> 2], 4 * (I & 3), 4 * (I & 3) + 1, 4 * (I & 3) + 2, 4 * (I & 3) + 3);
}
template <int N, class F> __device__ __forceinline__ void static_for(F f) {
  if constexpr (N > 0) { static_for<N - 1>(f); f(std::integral_constant<int, N - 1>{}); }
}
template <bool FIRST> __device__ __forceinline__ void ln_tile(const Ctx &c, int &rb, int &cb) {
  if (FIRST) { rb = 4 * (c.bx >> 1) + (c.bj >> 3); cb = 8 * (c.bx & 1) + (c.bj & 7); }
  else { rb = 2 * c.bx + (c.bj >> 4); cb = c.bj & 15; }
}

__device__ __forceinline__ void helpers_start(const Params &p, Ctx &c STAMP_ARGS) {
  const int l = lane_id(), hj = c.w - 4, id = blockIdx.x, h = l >> 5;
  int rb, cb;
  ln_tile<true>(c, rb, cb);
  {
    const rsrc_t rw = mkrsrc(p.wqkv, 512 * 3072);
    unsigned voff[3];
#pragma unroll
    for (int i = 0; i < 3; ++i) { const int u = 64 * i + l, row = u / 12; voff[i] = row * 3072 + (u - 12 * row) * 16; }
#pragma unroll
    for (int i = 0; i < 8; ++i)
#pragma unroll
      for (int t = 0; t < 3; ++t) glds<false>(rw, voff[t], (4 * i + hj) * 16 * 3072 + cb * 192, c.lds0 + (4 * i + hj) * 3072 + t * 1024);
  }
  const u64 e = __hip_atomic_load((GAS u64 *)(p.ws + OFF_EPOCH), __ATOMIC_RELAXED, __HIP_MEMORY_SCOPE_AGENT) + 1;
  {
    LAS char *blk = c.lds + hj * 3072;
    const unsigned t0 = (8 * h + ((l & 15) >> 2)) * 192 + ((l >> 4) & 1) * 32 + (l & 3) * 8;
    float cs[3] = {0.f, 0.f, 0.f};
    static_for<4>([&](auto I) {
      constexpr int i2 = I.value;
      asm volatile("s_waitcnt vmcnt(%0)" :: "n"(19 - 6 * i2) : "memory");
      v4u f[6];
#pragma unroll
      for (int u = 0; u < 6; ++u) { const LAS char *q = blk + (2 * i2 + u / 3) * 12288 + (u % 3) * 64 + t0; f[u] = trfrag(q, q + 768); }
      asm volatile("s_waitcnt lgkmcnt(0)" ::: "memory");
#pragma unroll
      for (int u = 0; u < 6; ++u) { *(LAS v4u *)(blk + (2 * i2 + u / 3) * 12288 + (u % 3) * 1024 + l * 16) = f[u]; cs[u % 3] = colsum(f[u], cs[u % 3]); }
    });
#pragma unroll
    for (int t = 0; t < 3; ++t) {
      const float s = halfsum2(cs[t], cs[t]);
      if (l < 32) *(LAS v2u *)(c.lds + 32 * 3072 + t * 1024 + (l + 32 * (hj >> 1)) * 16 + (hj & 1) * 8) = aug_quad(s, 0);
    }
  }
  STAMP(0, PH_QKV, 1);
  BAR();
  c.tag = rfl64(e);
  if (id < 155) {
    const void *sw; unsigned pitch, rows, nt, kb0 = 0, dst;
    if (id < 48) { sw = p.wqkv; pitch = 3072; rows = 512; nt = id; dst = OFF_WTQ + nt * NTP; }
    else if (id < 91) { sw = p.w1; pitch = 2752; rows = 512; nt = id - 48; dst = OFF_WT1 + nt * NTP; }
    else if (id < 107) { sw = p.wo; pitch = 1024; rows = 512; nt = id - 91; dst = OFF_WTO + nt * 32768; }
    else { sw = p.w2; pitch = 1024; rows = 1376; nt = (id - 107) & 15; kb0 = ((id - 107) >> 4) * 32; dst = OFF_WT2 + nt * 86 * 1024; }
    sw = (const void *)rfl64((u64)sw); pitch = rfl(pitch); rows = rfl(rows); nt = rfl(nt); kb0 = rfl(kb0); dst = rfl(dst);
    const rsrc_t rw = mkrsrc(sw, rows * pitch);
    const unsigned voff = (l >> 2) * pitch + (l & 3) * 16;
#pragma unroll
    for (int i = 0; i < 8; ++i) glds<false>(rw, voff, (kb0 + 8 * hj + i) * 16 * pitch + nt * 64, c.lds0 + L_PREP + (8 * hj + i) * 1024);
    unsigned bias = 0;
    if (id >= 48 && id < 91 && hj < 2) bias = ((const GAS unsigned short *)p.b1)[nt * 32 + (l & 31)];
    VMWAIT(0);
    const LAS char *blk = c.lds + L_PREP + hj * 8192 + h * 256 + ((l & 15) >> 2) * 64 + ((l >> 4) & 1) * 32 + (l & 3) * 8;
    float cs = 0.f;
#pragma unroll
    for (int i = 0; i < 8; ++i) {
      const v4u f = trfrag(blk + i * 1024, blk + i * 1024 + 512);
      cs = colsum(f, cs);
      if (kb0 + 8 * hj + i < 86) st16(f, c.rws, l * 16, dst + (kb0 + 8 * hj + i) * 1024);
    }
    cs = halfsum2(cs, cs);
    if (id < 91 && l < 32) __builtin_amdgcn_raw_buffer_store_b64(aug_quad(cs, bias), c.rws, (l + 32 * (hj >> 1)) * 16 + (hj & 1) * 8, dst + 32768, SC1);
    publish(p, c, SLOT_PREP, id);
  }
  STAMP(0, PH_QKV, 3);
}

__device__ __forceinline__ void store_stats(const Ctx &c, const unsigned (&o)[8], int l, unsigned st_tt, int g) {
  float s1 = 0.f, s2 = 0.f, s1b = 0.f, s2b = 0.f;
#pragma unroll
  for (int i = 0; i < 8; i += 2) { s1 = dot2(o[i], ONES2, s1); s2 = dot2(o[i], o[i], s2); s1b = dot2(o[i + 1], ONES2, s1b); s2b = dot2(o[i + 1], o[i + 1], s2b); }
  __builtin_amdgcn_raw_buffer_store_b32(__float_as_uint(halfsum2(s1 + s1b, s2 + s2b)), c.rws, l * 4, st_tt + g * 256, SC1);
}

template <int PH, bool FIRST> __device__ __forceinline__ void ln_gemm(const Params &p, Ctx &c0, int layer STAMP_ARGS) {
  Ctx c = fresh(c0);
  int rb, cb;
  ln_tile<FIRST>(c, rb, cb);
  const int nt0 = PH == PH_QKV ? 3 * cb : up_nt0(cb);
  const int l = lane_id(), h = l >> 5, tt = rb * 4 + c.w;
  constexpr int PD = 11;
  v4u a[32];
  float st[16], rstd = 0.f, sp = 0.f, nm = 0.f;
  const unsigned base = (PH == PH_QKV ? OFF_X2 : OFF_X1 + layer * 2 * MB) + tt * 32768;
#define LDA(ks) a[ks] = ld16(c.rws, l * 16 + ((ks) & 3) * 1024, base + ((ks) >> 2) * 4096)
  STAMP(layer, PH, 0);
  if constexpr (FIRST) {
    const rsrc_t rx = mkrsrc(p.x, 2048 * 1024);
#pragma unroll
    for (int ks = 0; ks < 32; ++ks) a[ks] = __builtin_amdgcn_raw_buffer_load_b128(rx, (l >> 3) * 1024 + (l & 7) * 16 + (ks >> 2) * 128, tt * 32768 + (ks & 3) * 8192, 0);
    const u64 e = __hip_atomic_load((GAS u64 *)(p.ws + OFF_EPOCH), __ATOMIC_RELAXED, __HIP_MEMORY_SCOPE_AGENT) + 1;
    SB();
    LAS char *sb = c.lds + L_PREP + c.w * 8192;
    const unsigned w0 = (l >> 3) * 128 + ((l & 7) ^ (l >> 4)) * 16, r0 = (l & 31) * 128 + (h ^ ((l >> 1) & 7)) * 16;
    float s1 = 0.f, s2 = 0.f, s1b = 0.f, s2b = 0.f;
#pragma unroll
    for (int ch = 0; ch < 8; ++ch) {
#pragma unroll
      for (int i = 0; i < 4; ++i) *(LAS v4u *)(sb + (ch & 1) * 4096 + i * 1024 + (w0 ^ ((i & 1) * 64))) = a[4 * ch + i];
      asm volatile("" ::: "memory");
#pragma unroll
      for (int i = 0; i < 4; ++i) a[4 * ch + i] = *(const LAS v4u *)(sb + (ch & 1) * 4096 + (r0 ^ (i * 32)));
      asm volatile("" ::: "memory");
#pragma unroll
      for (int i = 0; i < 4; ++i) {
        const v4u f = a[4 * ch + i];
        s1 = dot2(f.x, ONES2, s1); s2 = dot2(f.x, f.x, s2); s1b = dot2(f.y, ONES2, s1b); s2b = dot2(f.y, f.y, s2b);
        s1 = dot2(f.z, ONES2, s1); s2 = dot2(f.z, f.z, s2); s1b = dot2(f.w, ONES2, s1b); s2b = dot2(f.w, f.w, s2b);
      }
    }
    sp = halfsum2(s1 + s1b, s2 + s2b);
    c0.tag = c.tag = rfl64(e);
    STAMP(layer, PH, 4);
    BAR();
    STAMP(layer, PH, 1);
  } else {
    if (c.w == 0) poll(p, c, PH == PH_QKV ? PH_DOWN : layer * 5 + PH_WO, l < 16 ? rb * 16 + l : -1);
    STAMP(layer, PH, 4);
    BAR();
    STAMP(layer, PH, 1);
#pragma unroll
    for (int ks = 0; ks < PD; ++ks) LDA(ks);
  }
  SB();
  const LAS char *b0 = c.lds + l * 16, *b2 = b0 + (FIRST ? 20 * 3072 : 2 * NTP - 4096);
  OPAQUE_V(b2);
  f32x16 acc[3] = {};
  v4u wf[3][3], aug = {};
#define WF(ks, nt) (*(const LAS v4u *)(FIRST ? ((ks) >= 20 ? b2 + ((ks) - 20) * 3072 + (nt) * 1024 : b0 + (ks) * 3072 + (nt) * 1024) \
                                             : (nt) == 2 ? b2 + ((ks) + 4) * 1024 : (nt) == 1 && (ks) > 30 ? b2 + ((ks) - 29) * 1024 : b0 + (nt) * NTP + (ks) * 1024))
#pragma unroll
  for (int ks = 0; ks < 2; ++ks)
#pragma unroll
    for (int nt = 0; nt < 3; ++nt) wf[ks][nt] = WF(ks, nt);
  SB();
#pragma unroll
  for (int ks = 0; ks < 33; ++ks) {
#pragma unroll
    for (int nt = 0; nt < 3; ++nt) {
      if (ks + 2 < 33) wf[(ks + 2) % 3][nt] = WF(ks + 2, nt);
      acc[nt] = mfma(wf[ks % 3][nt], ks < 32 ? a[ks] : aug, acc[nt]);
      if (!FIRST) {
        if (nt == 1 && ks + PD < 32) LDA(ks + PD);
        if (PH == PH_UP && nt == 2 && ks + PD == 31) lds_arrive(c, 40 + layer * 5 + PH);
        if (nt == 2 && ks >= 8 && ks < 16) {
#pragma unroll
          for (int g = 2 * (ks - 8); g < 2 * (ks - 7); ++g)
            st[g] = __uint_as_float(__builtin_amdgcn_raw_buffer_load_b32(c.rws, l * 4 + g * 256, OFF_ST + (PH == PH_UP ? layer : 2) * 262144 + tt * 4096, SC1));
        }
        if (ks == 27) { if (nt == 0) sp = (st[0] + st[1]) + (st[2] + st[3]); else sp += (st[4 * nt] + st[4 * nt + 1]) + (st[4 * nt + 2] + st[4 * nt + 3]); OPAQUE_V(sp); }
        if (ks == 28 && nt == 0) { sp += (st[12] + st[13]) + (st[14] + st[15]); OPAQUE_V(sp); }
      }
      if (ks == 28 && nt == 1) {
        auto sw = __builtin_amdgcn_permlane32_swap(__float_as_uint(sp), __float_as_uint(sp), false, false);
        const unsigned u1 = sw[0], u2 = sw[1];
        nm = __uint_as_float(u1) * (-1.f / 512.f); sp = __uint_as_float(u2) * (1.f / 512.f);
        OPAQUE_V(nm); OPAQUE_V(sp);
      }
      if (ks == 28 && nt == 2) { sp = __builtin_fmaxf(sp - nm * nm, 0.f) + 1e-5f; rstd = __builtin_amdgcn_rsqf(sp); OPAQUE_V(sp); OPAQUE_V(rstd); }
      if (ks == 29 && nt == 0) { sp *= rstd; aug.x = pk2(nm, nm); aug.y = pk2(0.f, sp); OPAQUE_V(sp); OPAQUE_V(aug); }
      if (ks == 29 && nt == 1) { aug.w = pk2(nm - bf_lo(aug.x), sp - bf_hi(aug.y)); aug.y |= aug.w & 0xffffu; aug.z = aug.x; OPAQUE_V(aug); }
      SB();
    }
  }
#undef WF
#undef LDA
  if (PH == PH_UP) lds_arrive(c, 16 + layer * 5 + PH);
  STAMP(layer, PH, 2);
#pragma unroll
  for (int nt = 0; nt < 3; ++nt) {
    const int NT = nt0 + nt;
    unsigned o[8];
    if constexpr (PH == PH_QKV) {
      const int type = NT >> 4, head = 2 * (NT & 15);
      const float rs = type == 0 ? rstd * (0.25f * 1.4426950408889634f) : rstd;
#pragma unroll
      for (int i = 0; i < 8; ++i) { const f32x2 v = f32x2{acc[nt][2 * i], acc[nt][2 * i + 1]} * rs; o[i] = pk2(v.x, v.y); }
      const v4u wa = {o[0], o[1], o[2], o[3]}, wb = {o[4], o[5], o[6], o[7]};
      if (type == 0) {
        const unsigned so = OFF_QF + layer * 2 * MB + (head * 64 + tt) * 1024;
        st16(wa, c.rws, l * 16, so); st16(wb, c.rws, l * 16, so + 65536);
      } else if (type == 1) {
        const unsigned so = OFF_KV + layer * 4 * MB + (head * 64 + tt) * 2048;
        st16(wa, c.rws, l * 16, so); st16(wb, c.rws, l * 16, so + 131072);
        const float na = dot2(wa.w, wa.w, dot2(wa.z, wa.z, dot2(wa.y, wa.y, dot2(wa.x, wa.x, 0.f))));
        const float nb = dot2(wb.w, wb.w, dot2(wb.z, wb.z, dot2(wb.y, wb.y, dot2(wb.x, wb.x, 0.f))));
        const float m = max32(halfsum2(na, nb));
        if ((l & 31) == 31)
          __builtin_amdgcn_raw_buffer_store_b32(__float_as_uint(m), c.rws, h * 256, OFF_KN + layer * 8192 + (head * 64 + tt) * 4, SC1);
      } else {
        LAS char *sb = c.lds + L_VSCR + (c.w * 2 + (nt & 1)) * 2304;
        *(LAS v4u *)(sb + (l & 31) * 32 + h * 16) = wa;
        *(LAS v4u *)(sb + 1152 + (l & 31) * 32 + h * 16) = wb;
        asm volatile("" ::: "memory");
        const LAS char *rp = sb + ((l >> 4) & 1) * 1152 + (8 * h + ((l & 15) >> 2)) * 32 + (l & 3) * 8;
        const v4u f0 = trfrag(rp, rp + 128), f1 = trfrag(rp + 512, rp + 640);
        const int i = l & 15, d = (i & 3) | ((i & 4) << 1) | ((i & 8) >> 1);
        const unsigned vo = ((l >> 4) & 1) * 131072 + h * 256 + d * 16, so = OFF_KV + layer * 4 * MB + (head * 64 + tt) * 2048 + 1024;
        st16(f0, c.rws, vo, so); st16(f1, c.rws, vo, so + 512);
      }
    } else {
      const float rl = rstd * 1.4426950408889634f;
      const f32x2 one = {1.f, 1.f}, rs2 = {rstd, rstd}, rm2 = {-2.f * rstd, -2.f * rstd};
#pragma unroll
      for (int i = 0; i < 8; ++i) {
        const f32x2 x = {acc[nt][2 * i], acc[nt][2 * i + 1]}, hl = x * rl;
        const f32x2 t = f32x2{__builtin_amdgcn_exp2f(hl.x), __builtin_amdgcn_exp2f(hl.y)} + one;
        const f32x2 d = __builtin_elementwise_fma(t, t, one);
        const f32x2 v = x * __builtin_elementwise_fma(f32x2{__builtin_amdgcn_rcpf(d.x), __builtin_amdgcn_rcpf(d.y)}, rm2, rs2);
        o[i] = pk2(v.x, v.y);
      }
      if (nt < 2 || cb < 11) {
        const unsigned so = OFF_HF + layer * HF_LAYER + (tt * 86 + 2 * NT) * 1024;
        st16(v4u{o[0], o[1], o[2], o[3]}, c.rws, l * 16, so); st16(v4u{o[4], o[5], o[6], o[7]}, c.rws, l * 16, so + 1024);
      }
    }
  }
  STAMP(layer, PH, 5);
  publish(p, c, layer * 5 + PH, PH == PH_QKV ? cb * 16 + rb : rb * 16 + cb);
  if (REWARM && PH == PH_UP) {
#pragma unroll
    for (int i = 0; i < 6; ++i)
      if (i < 4 || cb < 11) glds<true>(c.rws, l * 16, OFF_HF + layer * HF_LAYER + (tt * 86 + 2 * nt0 + i) * 1024, c.lds0 + L_JUNK);
  }
  STAMP(layer, PH, 3);
}

template <int PH> __device__ __forceinline__ void res_gemm(const Params &p, const Ctx &c0, int layer, const v16u (&pre)[8] STAMP_ARGS) {
  const Ctx c = fresh(c0);
  constexpr int KB = PH == PH_WO ? 32 : 86, KH = KB / 2;
  const int rb = 4 * c.bx + (c.bj >> 3), cb = c.bj & 7;
  const int l = lane_id(), h = l >> 5, kh = c.w >> 1, tt = rb * 2 + (c.w & 1);
  const unsigned fm = (tt * 32 + cb * 4 + kh * 2) * 1024;
  STAMP(layer, PH, 0);
  v4u res[2];
  if (PH == PH_WO && layer == 0) {
    const rsrc_t rx = mkrsrc(p.x, 2048 * 1024);
    const unsigned voff = (l & 31) * 1024 + h * 8, so = tt * 32768 + cb * 128 + kh * 64;
    const v2u r0 = __builtin_amdgcn_raw_buffer_load_b64(rx, voff, so, 0), r1 = __builtin_amdgcn_raw_buffer_load_b64(rx, voff + 16, so, 0);
    const v2u r2 = __builtin_amdgcn_raw_buffer_load_b64(rx, voff + 32, so, 0), r3 = __builtin_amdgcn_raw_buffer_load_b64(rx, voff + 48, so, 0);
    res[0] = v4u{r0.x, r0.y, r1.x, r1.y}; res[1] = v4u{r2.x, r2.y, r3.x, r3.y};
  } else {
    const unsigned so = (PH == PH_WO ? OFF_X2 : OFF_X1 + layer * 2 * MB) + fm;
    res[0] = ld16(c.rws, l * 16, so); res[1] = ld16(c.rws, l * 16 + 1024, so);
  }
  if (c.w == 0) { if (PH == PH_WO) poll(p, c, layer * 5 + PH_ATT, l < 32 ? (rb >> 2) * 32 + l : -1); else poll(p, c, layer * 5 + PH_UP, l < 16 ? (rb >> 1) * 16 + l : -1); }
  STAMP(layer, PH, 4);
  BAR();
  STAMP(layer, PH, 1);
  v4u a[KH];
  const unsigned base = (PH == PH_WO ? OFF_OF + layer * 2 * MB : OFF_HF + layer * HF_LAYER) + (tt * KB + kh * KH) * 1024;
#define LDA(ks) a[ks] = ld16(c.rws, l * 16 + ((ks) & 3) * 1024, base + ((ks) >> 2) * 4096)
  constexpr int PD = PH == PH_WO ? 16 : 13;
#pragma unroll
  for (int ks = 0; ks < PD; ++ks) LDA(ks);
  if (PH == PH_WO) lds_arrive(c, 40 + layer * 5 + PH);
  SB();
  f32x16 acc[2] = {};
  if constexpr (PH == PH_WO) {
    static_for<16>([&](auto I) {
      constexpr int ks = I.value;
      acc[0] = mfma(frag<ks>(pre), a[ks], acc[0]);
      if constexpr (PD + 2 * ks < KH) LDA(PD + 2 * ks);
      SB();
      acc[1] = mfma(frag<16 + ks>(pre), a[ks], acc[1]);
      if constexpr (PD + 2 * ks + 1 < KH) LDA(PD + 2 * ks + 1);
      SB();
    });
  } else {
    const LAS char *b0 = c.lds + kh * 2048 + l * 16, *b1 = b0 + 65536, *b2 = b0 + 131072;
    OPAQUE_V(b1); OPAQUE_V(b2);
    v4u wf[4][2];
#define SLOT(ks) ((ks) < 39 ? (ks) : (ks) - 39)
#define WF(ks, nt) (*(const LAS v4u *)((SLOT(ks) >= 32 ? b2 : SLOT(ks) >= 16 ? b1 : b0) + (SLOT(ks) & 15) * 4096 + (nt) * 1024))
#pragma unroll
    for (int ks = 0; ks < 3; ++ks)
#pragma unroll
      for (int nt = 0; nt < 2; ++nt) wf[ks][nt] = WF(ks, nt);
    SB();
#pragma unroll
    for (int ks = 0; ks < KH; ++ks) {
      if (ks == 4 || ks == 36) { asm volatile("s_barrier" ::: "memory"); SB(); }
#pragma unroll
      for (int nt = 0; nt < 2; ++nt) {
        if (ks + 3 < KH) wf[(ks + 3) % 4][nt] = WF(ks + 3, nt);
        acc[nt] = mfma(wf[ks % 4][nt], a[ks], acc[nt]);
        if (nt == 1 && PD + ks < KH) LDA(PD + ks);
        SB();
      }
    }
#undef WF
#undef SLOT
  }
#undef LDA
  constexpr unsigned XCH = PH == PH_WO ? L_XCH_WO : L_XCH_DN;
  const unsigned tok = layer * 2 + (PH == PH_WO ? 1 : 2);
  {
    const f32x16 s = kh ? acc[0] : acc[1];
    LAS f32x4 *xw = (LAS f32x4 *)(c.lds + XCH + c.w * 4096 + l * 16);
#pragma unroll
    for (int g = 0; g < 4; ++g) xw[g * 64] = f32x4{s[4 * g], s[4 * g + 1], s[4 * g + 2], s[4 * g + 3]};
  }
  lds_arrive(c, 32 + c.w);
  const f32x16 m = kh ? acc[1] : acc[0];
  f32x2 own[8];
#pragma unroll
  for (int i = 0; i < 8; ++i) { const unsigned r = res[i >> 2][i & 3]; own[i] = f32x2{m[2 * i], m[2 * i + 1]} + f32x2{bf_lo(r), bf_hi(r)}; OPAQUE_V(own[i]); }
  lds_wait(c, 32 + (c.w ^ 2), tok);
  STAMP(layer, PH, 2);
  const LAS f32x4 *xr = (const LAS f32x4 *)(c.lds + XCH + (c.w ^ 2) * 4096 + l * 16);
  unsigned o[8];
#pragma unroll
  for (int g = 0; g < 4; ++g) {
    const f32x4 q = xr[g * 64];
    const f32x2 u = own[2 * g] + f32x2{q[0], q[1]}, v = own[2 * g + 1] + f32x2{q[2], q[3]};
    o[2 * g] = pk2(u.x, u.y); o[2 * g + 1] = pk2(v.x, v.y);
  }
  if (PH == PH_DOWN && layer == 1) {
    const rsrc_t ro = mkrsrc(p.out, 2048 * 1024);
#pragma unroll
    for (int t = 0; t < 2; ++t) {
      auto s0 = __builtin_amdgcn_permlane32_swap(o[4 * t], o[4 * t + 2], false, false);
      auto s1 = __builtin_amdgcn_permlane32_swap(o[4 * t + 1], o[4 * t + 3], false, false);
      __builtin_amdgcn_raw_buffer_store_b128(v4u{s0[0], s1[0], s0[1], s1[1]}, ro, (l & 31) * 1024 + h * 16 + t * 32, tt * 32768 + cb * 128 + kh * 64, 0);
    }
  } else {
    const unsigned so = (PH == PH_WO ? OFF_X1 + layer * 2 * MB : OFF_X2) + fm;
    st16(v4u{o[0], o[1], o[2], o[3]}, c.rws, l * 16, so); st16(v4u{o[4], o[5], o[6], o[7]}, c.rws, l * 16 + 1024, so);
    store_stats(c, o, l, OFF_ST + (PH == PH_WO ? layer : 2) * 262144 + tt * 4096, 2 * cb + kh);
    STAMP(layer, PH, 5);
    publish(p, c, layer * 5 + PH, rb * 8 + cb);
    if (REWARM) { glds<true>(c.rws, l * 16, so, c.lds0 + L_JUNK); glds<true>(c.rws, l * 16, so + 1024, c.lds0 + L_JUNK); }
  }
  STAMP(layer, PH, 3);
}

__device__ __forceinline__ void helpers_chain(const Params &p, const Ctx &c0, int layer, const v16u (&pre)[8] STAMP_ARGS) {
  const Ctx c = fresh(c0);
  const int l = lane_id(), hj = c.w - 4;
  LAS char *lw = c.lds + l * 16;
  static_for<25>([&](auto I) { *(LAS v4u *)(lw + (25 * hj + I.value) * 1024) = frag<I.value>(pre); });
  const unsigned src = OFF_WT2 + ((2 * (c.bj & 7) + (hj & 1)) * 86 + (hj >> 1) * 43) * 1024;
  v4u w2[22];
#define LDW(i) w2[i] = ld16(c.rws, l * 16, src + ((i) < 18 ? 7 + (i) : 21 + (i)) * 1024)
  STAMP(layer, PH_WO, 0);
  BAR();
  lds_wait(c, 40 + layer * 5 + PH_WO, 4);
#pragma unroll
  for (int i = 0; i < NW_WO; ++i) LDW(i);
  STAMP(layer, PH_WO, 3);
  BAR();
  lds_wait(c, 40 + layer * 5 + PH_UP, 4);
#pragma unroll
  for (int i = NW_WO; i < 22; ++i) LDW(i);
#pragma unroll
  for (int ks = 25; ks < 39; ++ks) glds<true>(c.rws, l * 16, src + ks * 1024, c.lds0 + ks * 4096 + hj * 1024);
#undef LDW
  STAMP(layer, PH_UP, 3);
  lds_wait(c, 16 + layer * 5 + PH_UP, 4);
  static_for<7>([&](auto I) { *(LAS v4u *)(lw + I.value * 4096 + hj * 1024) = frag<25 + I.value>(pre); });
#pragma unroll
  for (int i = 0; i < 18; ++i) *(LAS v4u *)(lw + (7 + i) * 4096 + hj * 1024) = w2[i];
  VMWAIT(0);
  STAMP(layer, PH_DOWN, 1);
  BAR();
  BAR();
#pragma unroll
  for (int i = 0; i < 4; ++i) *(LAS v4u *)(lw + i * 4096 + hj * 1024) = w2[18 + i];
  BAR();
  if (layer == 0) {
    lds_wait(c, PH_DOWN, 4);
    const unsigned sq = OFF_WTQ + 3 * (c.bj & 15) * NTP + 25 * hj * 1024;
#pragma unroll
    for (int i = 0; i < 25; ++i) glds<true>(c.rws, l * 16, sq + i * 1024, c.lds0 + (25 * hj + i) * 1024);
    STAMP(1, PH_QKV, 0);
    VMWAIT(0);
    STAMP(1, PH_QKV, 1);
    BAR();
  }
}

#define ATTN_FAST_ASM \
  "s_mov_b32 %[keep], m0\n\t" \
  "v_mov_b32 v248, 0xdddd\n\t" \
  "ds_read_b128 v[224:227], %[ka]\n\t" \
  "ds_read_b128 v[232:235], %[va0]\n\t" \
  "ds_read_b128 v[240:243], %[va1]\n\t" \
  "ds_read_b128 v[228:231], %[ka] offset:4160\n\t" \
  "v_mov_b32 v244, 0\n\t" \
  "v_mov_b32 v245, 0\n\t" \
  "v_mov_b32 v246, 0\n\t" \
  "v_mov_b32 v247, 0\n\t" \
  "s_mov_b32 %[cnt], 0\n\t" \
  "s_nop 0\n\t" \
  "v_mfma_f32_32x32x16_bf16 %[oa], v[244:247], v[244:247], 0\n\t" \
  "v_mfma_f32_32x32x16_bf16 %[ob], v[244:247], v[244:247], 0\n\t" \
  "v_mfma_f32_32x32x16_bf16 v[208:223], v[244:247], v[244:247], 0\n\t" \
  "s_waitcnt lgkmcnt(3)\n\t" \
  "v_mfma_f32_32x32x16_bf16 v[192:207], v[224:227], %[qa], 0\n\t" \
  "s_nop 11\n\t" \
  "s_waitcnt lgkmcnt(0)\n\t" \
  "v_exp_f32 v192, v192\n\t" \
  "v_exp_f32 v193, v193\n\t" \
  "v_smfmac_f32_32x32x32_bf16 %[ob], v[244:247], v[216:223], v248\n\t" \
  "v_exp_f32 v194, v194\n\t" \
  "v_exp_f32 v195, v195\n\t" \
  "v_exp_f32 v196, v196\n\t" \
  "v_mfma_f32_32x32x16_bf16 v[208:223], v[224:227], %[qb], 0\n\t" \
  "ds_read_b128 v[224:227], %[ka] offset:8320\n\t" \
  "ds_read_b128 v[236:239], %[va0] offset:4160\n\t" \
  "ds_read_b128 v[244:247], %[va1] offset:4160\n\t" \
  "v_exp_f32 v197, v197\n\t" \
  "v_exp_f32 v198, v198\n\t" \
  "v_exp_f32 v199, v199\n\t" \
  "s_mov_b32 m0, %[m0v]\n\t" \
  "s_add_u32 %[m0v], %[m0v], 16640\n\t" \
  "buffer_load_dwordx4 %[vo], %[rs], %[so] offen sc1 lds\n\t" \
  "v_exp_f32 v200, v200\n\t" \
  "v_exp_f32 v201, v201\n\t" \
  "v_exp_f32 v202, v202\n\t" \
  "v_smfmac_f32_32x32x32_bf16 %[oa], v[232:235], v[192:199], v248\n\t" \
  "v_exp_f32 v203, v203\n\t" \
  "v_exp_f32 v204, v204\n\t" \
  "v_exp_f32 v205, v205\n\t" \
  "buffer_load_dwordx4 %[vo], %[rs], %[so] offen offset:1024 sc1 lds\n\t" \
  "s_add_u32 %[so], %[so], 16384\n\t" \
  "v_exp_f32 v206, v206\n\t" \
  "v_exp_f32 v207, v207\n\t" \
  "v_exp_f32 v208, v208\n\t" \
  "v_exp_f32 v209, v209\n\t" \
  "v_smfmac_f32_32x32x32_bf16 %[oa], v[240:243], v[200:207], v248\n\t" \
  "v_exp_f32 v210, v210\n\t" \
  "v_exp_f32 v211, v211\n\t" \
  "v_exp_f32 v212, v212\n\t" \
  "v_mfma_f32_32x32x16_bf16 v[192:207], v[228:231], %[qa], 0\n\t" \
  "v_exp_f32 v213, v213\n\t" \
  "v_exp_f32 v214, v214\n\t" \
  "v_exp_f32 v215, v215\n\t" \
  "s_mov_b32 m0, %[m0v]\n\t" \
  "s_add_u32 %[m0v], %[m0v], 16640\n\t" \
  "buffer_load_dwordx4 %[vo], %[rs], %[so] offen sc1 lds\n\t" \
  "v_exp_f32 v216, v216\n\t" \
  "v_exp_f32 v217, v217\n\t" \
  "v_exp_f32 v218, v218\n\t" \
  "v_smfmac_f32_32x32x32_bf16 %[ob], v[232:235], v[208:215], v248\n\t" \
  "v_exp_f32 v219, v219\n\t" \
  "v_exp_f32 v220, v220\n\t" \
  "v_exp_f32 v221, v221\n\t" \
  "buffer_load_dwordx4 %[vo], %[rs], %[so] offen offset:1024 sc1 lds\n\t" \
  "s_add_u32 %[so], %[so], 16384\n\t" \
  "v_exp_f32 v222, v222\n\t" \
  "v_exp_f32 v223, v223\n\t" \
  "s_waitcnt lgkmcnt(0)\n\t" \
  "v_exp_f32 v192, v192\n\t" \
  "v_exp_f32 v193, v193\n\t" \
  "v_smfmac_f32_32x32x32_bf16 %[ob], v[240:243], v[216:223], v248\n\t" \
  "v_exp_f32 v194, v194\n\t" \
  "v_exp_f32 v195, v195\n\t" \
  "v_exp_f32 v196, v196\n\t" \
  "v_mfma_f32_32x32x16_bf16 v[208:223], v[228:231], %[qb], 0\n\t" \
  "ds_read_b128 v[228:231], %[ka] offset:12480\n\t" \
  "ds_read_b128 v[232:235], %[va0] offset:8320\n\t" \
  "ds_read_b128 v[240:243], %[va1] offset:8320\n\t" \
  "v_exp_f32 v197, v197\n\t" \
  "v_exp_f32 v198, v198\n\t" \
  "v_exp_f32 v199, v199\n\t" \
  "s_mov_b32 m0, %[m0v]\n\t" \
  "s_add_u32 %[m0v], %[m0v], 16640\n\t" \
  "buffer_load_dwordx4 %[vo], %[rs], %[so] offen sc1 lds\n\t" \
  "v_exp_f32 v200, v200\n\t" \
  "v_exp_f32 v201, v201\n\t" \
  "v_exp_f32 v202, v202\n\t" \
  "v_smfmac_f32_32x32x32_bf16 %[oa], v[236:239], v[192:199], v248\n\t" \
  "v_exp_f32 v203, v203\n\t" \
  "v_exp_f32 v204, v204\n\t" \
  "v_exp_f32 v205, v205\n\t" \
  "buffer_load_dwordx4 %[vo], %[rs], %[so] offen offset:1024 sc1 lds\n\t" \
  "s_add_u32 %[so], %[so], 16384\n\t" \
  "v_exp_f32 v206, v206\n\t" \
  "v_exp_f32 v207, v207\n\t" \
  "v_exp_f32 v208, v208\n\t" \
  "v_exp_f32 v209, v209\n\t" \
  "v_smfmac_f32_32x32x32_bf16 %[oa], v[244:247], v[200:207], v248\n\t" \
  "v_exp_f32 v210, v210\n\t" \
  "v_exp_f32 v211, v211\n\t" \
  "v_exp_f32 v212, v212\n\t" \
  "v_mfma_f32_32x32x16_bf16 v[192:207], v[224:227], %[qa], 0\n\t" \
  "v_exp_f32 v213, v213\n\t" \
  "v_exp_f32 v214, v214\n\t" \
  "v_exp_f32 v215, v215\n\t" \
  "s_mov_b32 m0, %[m0v]\n\t" \
  "s_add_u32 %[m0v], %[m0v], 16640\n\t" \
  "buffer_load_dwordx4 %[vo], %[rs], %[so] offen sc1 lds\n\t" \
  "v_exp_f32 v216, v216\n\t" \
  "v_exp_f32 v217, v217\n\t" \
  "v_exp_f32 v218, v218\n\t" \
  "v_smfmac_f32_32x32x32_bf16 %[ob], v[236:239], v[208:215], v248\n\t" \
  "v_exp_f32 v219, v219\n\t" \
  "v_exp_f32 v220, v220\n\t" \
  "v_exp_f32 v221, v221\n\t" \
  "buffer_load_dwordx4 %[vo], %[rs], %[so] offen offset:1024 sc1 lds\n\t" \
  "s_add_u32 %[so], %[so], 16384\n\t" \
  "v_exp_f32 v222, v222\n\t" \
  "v_exp_f32 v223, v223\n\t" \
  "s_waitcnt lgkmcnt(0)\n\t" \
  "v_exp_f32 v192, v192\n\t" \
  "v_exp_f32 v193, v193\n\t" \
  "v_smfmac_f32_32x32x32_bf16 %[ob], v[244:247], v[216:223], v248\n\t" \
  "v_exp_f32 v194, v194\n\t" \
  "v_exp_f32 v195, v195\n\t" \
  "v_exp_f32 v196, v196\n\t" \
  "v_mfma_f32_32x32x16_bf16 v[208:223], v[224:227], %[qb], 0\n\t" \
  "ds_read_b128 v[224:227], %[ka] offset:16640\n\t" \
  "ds_read_b128 v[236:239], %[va0] offset:12480\n\t" \
  "ds_read_b128 v[244:247], %[va1] offset:12480\n\t" \
  "v_exp_f32 v197, v197\n\t" \
  "v_exp_f32 v198, v198\n\t" \
  "v_exp_f32 v199, v199\n\t" \
  "s_mov_b32 m0, %[m0v]\n\t" \
  "s_add_u32 %[m0v], %[m0v], 16640\n\t" \
  "buffer_load_dwordx4 %[vo], %[rs], %[so] offen sc1 lds\n\t" \
  "v_exp_f32 v200, v200\n\t" \
  "v_exp_f32 v201, v201\n\t" \
  "v_exp_f32 v202, v202\n\t" \
  "v_smfmac_f32_32x32x32_bf16 %[oa], v[232:235], v[192:199], v248\n\t" \
  "v_exp_f32 v203, v203\n\t" \
  "v_exp_f32 v204, v204\n\t" \
  "v_exp_f32 v205, v205\n\t" \
  "buffer_load_dwordx4 %[vo], %[rs], %[so] offen offset:1024 sc1 lds\n\t" \
  "s_add_u32 %[so], %[so], 16384\n\t" \
  "v_exp_f32 v206, v206\n\t" \
  "v_exp_f32 v207, v207\n\t" \
  "v_exp_f32 v208, v208\n\t" \
  "v_exp_f32 v209, v209\n\t" \
  "v_smfmac_f32_32x32x32_bf16 %[oa], v[240:243], v[200:207], v248\n\t" \
  "v_exp_f32 v210, v210\n\t" \
  "v_exp_f32 v211, v211\n\t" \
  "v_exp_f32 v212, v212\n\t" \
  "v_mfma_f32_32x32x16_bf16 v[192:207], v[228:231], %[qa], 0\n\t" \
  "v_exp_f32 v213, v213\n\t" \
  "v_exp_f32 v214, v214\n\t" \
  "v_exp_f32 v215, v215\n\t" \
  "s_mov_b32 m0, %[m0v]\n\t" \
  "s_add_u32 %[m0v], %[m0v], 16640\n\t" \
  "buffer_load_dwordx4 %[vo], %[rs], %[so] offen sc1 lds\n\t" \
  "v_exp_f32 v216, v216\n\t" \
  "v_exp_f32 v217, v217\n\t" \
  "v_exp_f32 v218, v218\n\t" \
  "v_smfmac_f32_32x32x32_bf16 %[ob], v[232:235], v[208:215], v248\n\t" \
  "v_exp_f32 v219, v219\n\t" \
  "v_exp_f32 v220, v220\n\t" \
  "v_exp_f32 v221, v221\n\t" \
  "buffer_load_dwordx4 %[vo], %[rs], %[so] offen offset:1024 sc1 lds\n\t" \
  "s_add_u32 %[so], %[so], 16384\n\t" \
  "v_exp_f32 v222, v222\n\t" \
  "v_exp_f32 v223, v223\n\t" \
  "s_waitcnt lgkmcnt(0)\n\t" \
  "v_exp_f32 v192, v192\n\t" \
  "v_exp_f32 v193, v193\n\t" \
  "v_smfmac_f32_32x32x32_bf16 %[ob], v[240:243], v[216:223], v248\n\t" \
  "v_exp_f32 v194, v194\n\t" \
  "v_exp_f32 v195, v195\n\t" \
  "v_exp_f32 v196, v196\n\t" \
  "v_mfma_f32_32x32x16_bf16 v[208:223], v[228:231], %[qb], 0\n\t" \
  "ds_read_b128 v[228:231], %[ka] offset:20800\n\t" \
  "ds_read_b128 v[232:235], %[va0] offset:16640\n\t" \
  "ds_read_b128 v[240:243], %[va1] offset:16640\n\t" \
  "v_exp_f32 v197, v197\n\t" \
  "v_exp_f32 v198, v198\n\t" \
  "v_exp_f32 v199, v199\n\t" \
  "v_exp_f32 v200, v200\n\t" \
  "v_exp_f32 v201, v201\n\t" \
  "v_exp_f32 v202, v202\n\t" \
  "v_smfmac_f32_32x32x32_bf16 %[oa], v[236:239], v[192:199], v248\n\t" \
  "v_exp_f32 v203, v203\n\t" \
  "v_exp_f32 v204, v204\n\t" \
  "v_exp_f32 v205, v205\n\t" \
  "v_exp_f32 v206, v206\n\t" \
  "v_exp_f32 v207, v207\n\t" \
  "v_exp_f32 v208, v208\n\t" \
  "v_exp_f32 v209, v209\n\t" \
  "v_smfmac_f32_32x32x32_bf16 %[oa], v[244:247], v[200:207], v248\n\t" \
  "v_exp_f32 v210, v210\n\t" \
  "v_exp_f32 v211, v211\n\t" \
  "v_exp_f32 v212, v212\n\t" \
  "v_mfma_f32_32x32x16_bf16 v[192:207], v[224:227], %[qa], 0\n\t" \
  "v_exp_f32 v213, v213\n\t" \
  "v_exp_f32 v214, v214\n\t" \
  "v_exp_f32 v215, v215\n\t" \
  "v_exp_f32 v216, v216\n\t" \
  "v_exp_f32 v217, v217\n\t" \
  "v_exp_f32 v218, v218\n\t" \
  "v_smfmac_f32_32x32x32_bf16 %[ob], v[236:239], v[208:215], v248\n\t" \
  "v_exp_f32 v219, v219\n\t" \
  "v_exp_f32 v220, v220\n\t" \
  "v_exp_f32 v221, v221\n\t" \
  "v_exp_f32 v222, v222\n\t" \
  "v_exp_f32 v223, v223\n\t" \
  "s_waitcnt vmcnt(10)\n\t" \
  "s_barrier\n\t" \
  "s_waitcnt lgkmcnt(0)\n\t" \
  "v_exp_f32 v192, v192\n\t" \
  "v_exp_f32 v193, v193\n\t" \
  "v_smfmac_f32_32x32x32_bf16 %[ob], v[244:247], v[216:223], v248\n\t" \
  "v_exp_f32 v194, v194\n\t" \
  "v_exp_f32 v195, v195\n\t" \
  "v_exp_f32 v196, v196\n\t" \
  "v_mfma_f32_32x32x16_bf16 v[208:223], v[224:227], %[qb], 0\n\t" \
  "ds_read_b128 v[224:227], %[ka] offset:24960\n\t" \
  "ds_read_b128 v[236:239], %[va0] offset:20800\n\t" \
  "ds_read_b128 v[244:247], %[va1] offset:20800\n\t" \
  "v_exp_f32 v197, v197\n\t" \
  "v_exp_f32 v198, v198\n\t" \
  "v_exp_f32 v199, v199\n\t" \
  "v_exp_f32 v200, v200\n\t" \
  "v_exp_f32 v201, v201\n\t" \
  "v_exp_f32 v202, v202\n\t" \
  "v_smfmac_f32_32x32x32_bf16 %[oa], v[232:235], v[192:199], v248\n\t" \
  "v_exp_f32 v203, v203\n\t" \
  "v_exp_f32 v204, v204\n\t" \
  "v_exp_f32 v205, v205\n\t" \
  "v_exp_f32 v206, v206\n\t" \
  "v_exp_f32 v207, v207\n\t" \
  "v_exp_f32 v208, v208\n\t" \
  "v_exp_f32 v209, v209\n\t" \
  "v_smfmac_f32_32x32x32_bf16 %[oa], v[240:243], v[200:207], v248\n\t" \
  "v_exp_f32 v210, v210\n\t" \
  "v_exp_f32 v211, v211\n\t" \
  "v_exp_f32 v212, v212\n\t" \
  "v_mfma_f32_32x32x16_bf16 v[192:207], v[228:231], %[qa], 0\n\t" \
  "v_exp_f32 v213, v213\n\t" \
  "v_exp_f32 v214, v214\n\t" \
  "v_exp_f32 v215, v215\n\t" \
  "v_exp_f32 v216, v216\n\t" \
  "v_exp_f32 v217, v217\n\t" \
  "v_exp_f32 v218, v218\n\t" \
  "v_smfmac_f32_32x32x32_bf16 %[ob], v[232:235], v[208:215], v248\n\t" \
  "v_exp_f32 v219, v219\n\t" \
  "v_exp_f32 v220, v220\n\t" \
  "v_exp_f32 v221, v221\n\t" \
  "v_exp_f32 v222, v222\n\t" \
  "v_exp_f32 v223, v223\n\t" \
  "s_waitcnt lgkmcnt(0)\n\t" \
  "v_exp_f32 v192, v192\n\t" \
  "v_exp_f32 v193, v193\n\t" \
  "v_smfmac_f32_32x32x32_bf16 %[ob], v[240:243], v[216:223], v248\n\t" \
  "v_exp_f32 v194, v194\n\t" \
  "v_exp_f32 v195, v195\n\t" \
  "v_exp_f32 v196, v196\n\t" \
  "v_mfma_f32_32x32x16_bf16 v[208:223], v[228:231], %[qb], 0\n\t" \
  "ds_read_b128 v[228:231], %[ka] offset:29120\n\t" \
  "ds_read_b128 v[232:235], %[va0] offset:24960\n\t" \
  "ds_read_b128 v[240:243], %[va1] offset:24960\n\t" \
  "v_exp_f32 v197, v197\n\t" \
  "v_exp_f32 v198, v198\n\t" \
  "v_exp_f32 v199, v199\n\t" \
  "v_exp_f32 v200, v200\n\t" \
  "v_exp_f32 v201, v201\n\t" \
  "v_exp_f32 v202, v202\n\t" \
  "v_smfmac_f32_32x32x32_bf16 %[oa], v[236:239], v[192:199], v248\n\t" \
  "v_exp_f32 v203, v203\n\t" \
  "v_exp_f32 v204, v204\n\t" \
  "v_exp_f32 v205, v205\n\t" \
  "v_exp_f32 v206, v206\n\t" \
  "v_exp_f32 v207, v207\n\t" \
  "v_exp_f32 v208, v208\n\t" \
  "v_exp_f32 v209, v209\n\t" \
  "v_smfmac_f32_32x32x32_bf16 %[oa], v[244:247], v[200:207], v248\n\t" \
  "v_exp_f32 v210, v210\n\t" \
  "v_exp_f32 v211, v211\n\t" \
  "v_exp_f32 v212, v212\n\t" \
  "v_mfma_f32_32x32x16_bf16 v[192:207], v[224:227], %[qa], 0\n\t" \
  "v_exp_f32 v213, v213\n\t" \
  "v_exp_f32 v214, v214\n\t" \
  "v_exp_f32 v215, v215\n\t" \
  "v_exp_f32 v216, v216\n\t" \
  "v_exp_f32 v217, v217\n\t" \
  "v_exp_f32 v218, v218\n\t" \
  "v_smfmac_f32_32x32x32_bf16 %[ob], v[236:239], v[208:215], v248\n\t" \
  "v_exp_f32 v219, v219\n\t" \
  "v_exp_f32 v220, v220\n\t" \
  "v_exp_f32 v221, v221\n\t" \
  "v_exp_f32 v222, v222\n\t" \
  "v_exp_f32 v223, v223\n\t" \
  "s_waitcnt lgkmcnt(0)\n\t" \
  "v_exp_f32 v192, v192\n\t" \
  "v_exp_f32 v193, v193\n\t" \
  "v_smfmac_f32_32x32x32_bf16 %[ob], v[244:247], v[216:223], v248\n\t" \
  "v_exp_f32 v194, v194\n\t" \
  "v_exp_f32 v195, v195\n\t" \
  "v_exp_f32 v196, v196\n\t" \
  "v_mfma_f32_32x32x16_bf16 v[208:223], v[224:227], %[qb], 0\n\t" \
  "ds_read_b128 v[224:227], %[ka] offset:33280\n\t" \
  "ds_read_b128 v[236:239], %[va0] offset:29120\n\t" \
  "ds_read_b128 v[244:247], %[va1] offset:29120\n\t" \
  "v_exp_f32 v197, v197\n\t" \
  "v_exp_f32 v198, v198\n\t" \
  "v_exp_f32 v199, v199\n\t" \
  "v_exp_f32 v200, v200\n\t" \
  "v_exp_f32 v201, v201\n\t" \
  "v_exp_f32 v202, v202\n\t" \
  "v_smfmac_f32_32x32x32_bf16 %[oa], v[232:235], v[192:199], v248\n\t" \
  "v_exp_f32 v203, v203\n\t" \
  "v_exp_f32 v204, v204\n\t" \
  "v_exp_f32 v205, v205\n\t" \
  "v_exp_f32 v206, v206\n\t" \
  "v_exp_f32 v207, v207\n\t" \
  "v_exp_f32 v208, v208\n\t" \
  "v_exp_f32 v209, v209\n\t" \
  "v_smfmac_f32_32x32x32_bf16 %[oa], v[240:243], v[200:207], v248\n\t" \
  "v_exp_f32 v210, v210\n\t" \
  "v_exp_f32 v211, v211\n\t" \
  "v_exp_f32 v212, v212\n\t" \
  "v_mfma_f32_32x32x16_bf16 v[192:207], v[228:231], %[qa], 0\n\t" \
  "v_exp_f32 v213, v213\n\t" \
  "v_exp_f32 v214, v214\n\t" \
  "v_exp_f32 v215, v215\n\t" \
  "v_exp_f32 v216, v216\n\t" \
  "v_exp_f32 v217, v217\n\t" \
  "v_exp_f32 v218, v218\n\t" \
  "v_smfmac_f32_32x32x32_bf16 %[ob], v[232:235], v[208:215], v248\n\t" \
  "v_exp_f32 v219, v219\n\t" \
  "v_exp_f32 v220, v220\n\t" \
  "v_exp_f32 v221, v221\n\t" \
  "v_exp_f32 v222, v222\n\t" \
  "v_exp_f32 v223, v223\n\t" \
  "s_waitcnt lgkmcnt(0)\n\t" \
  "v_exp_f32 v192, v192\n\t" \
  "v_exp_f32 v193, v193\n\t" \
  "v_smfmac_f32_32x32x32_bf16 %[ob], v[240:243], v[216:223], v248\n\t" \
  "v_exp_f32 v194, v194\n\t" \
  "v_exp_f32 v195, v195\n\t" \
  "v_exp_f32 v196, v196\n\t" \
  "v_mfma_f32_32x32x16_bf16 v[208:223], v[228:231], %[qb], 0\n\t" \
  "ds_read_b128 v[228:231], %[ka] offset:37440\n\t" \
  "ds_read_b128 v[232:235], %[va0] offset:33280\n\t" \
  "ds_read_b128 v[240:243], %[va1] offset:33280\n\t" \
  "v_exp_f32 v197, v197\n\t" \
  "v_exp_f32 v198, v198\n\t" \
  "v_exp_f32 v199, v199\n\t" \
  "v_exp_f32 v200, v200\n\t" \
  "v_exp_f32 v201, v201\n\t" \
  "v_exp_f32 v202, v202\n\t" \
  "v_smfmac_f32_32x32x32_bf16 %[oa], v[236:239], v[192:199], v248\n\t" \
  "v_exp_f32 v203, v203\n\t" \
  "v_exp_f32 v204, v204\n\t" \
  "v_exp_f32 v205, v205\n\t" \
  "v_exp_f32 v206, v206\n\t" \
  "v_exp_f32 v207, v207\n\t" \
  "v_exp_f32 v208, v208\n\t" \
  "v_exp_f32 v209, v209\n\t" \
  "v_smfmac_f32_32x32x32_bf16 %[oa], v[244:247], v[200:207], v248\n\t" \
  "v_exp_f32 v210, v210\n\t" \
  "v_exp_f32 v211, v211\n\t" \
  "v_exp_f32 v212, v212\n\t" \
  "v_mfma_f32_32x32x16_bf16 v[192:207], v[224:227], %[qa], 0\n\t" \
  "v_exp_f32 v213, v213\n\t" \
  "v_exp_f32 v214, v214\n\t" \
  "v_exp_f32 v215, v215\n\t" \
  "v_exp_f32 v216, v216\n\t" \
  "v_exp_f32 v217, v217\n\t" \
  "v_exp_f32 v218, v218\n\t" \
  "v_smfmac_f32_32x32x32_bf16 %[ob], v[236:239], v[208:215], v248\n\t" \
  "v_exp_f32 v219, v219\n\t" \
  "v_exp_f32 v220, v220\n\t" \
  "v_exp_f32 v221, v221\n\t" \
  "v_exp_f32 v222, v222\n\t" \
  "v_exp_f32 v223, v223\n\t" \
  "v_add_u32 %[ka], 33280, %[ka]\n\t" \
  "v_add_u32 %[va0], 33280, %[va0]\n\t" \
  "v_add_u32 %[va1], 33280, %[va1]\n\t" \
  "s_mov_b32 m0, %[dummy]\n\t" \
  "s_waitcnt vmcnt(0)\n\t" \
  "s_barrier\n\t" \
  "s_waitcnt lgkmcnt(0)\n\t" \
  "v_exp_f32 v192, v192\n\t" \
  "v_exp_f32 v193, v193\n\t" \
  "v_smfmac_f32_32x32x32_bf16 %[ob], v[244:247], v[216:223], v248\n\t" \
  "v_exp_f32 v194, v194\n\t" \
  "v_exp_f32 v195, v195\n\t" \
  "v_exp_f32 v196, v196\n\t" \
  "v_mfma_f32_32x32x16_bf16 v[208:223], v[224:227], %[qb], 0\n\t" \
  "ds_read_b128 v[224:227], %[ka] offset:8320\n\t" \
  "ds_read_b128 v[236:239], %[va0] offset:4160\n\t" \
  "ds_read_b128 v[244:247], %[va1] offset:4160\n\t" \
  "v_exp_f32 v197, v197\n\t" \
  "v_exp_f32 v198, v198\n\t" \
  "v_exp_f32 v199, v199\n\t" \
  "s_mov_b32 %[so], %[p0]\n\t" \
  "buffer_load_dwordx4 v[64:67], %[vo], %[rs], %[so] offen sc1\n\t" \
  "s_add_u32 %[so], %[so], 1024\n\t" \
  "v_exp_f32 v200, v200\n\t" \
  "v_exp_f32 v201, v201\n\t" \
  "v_exp_f32 v202, v202\n\t" \
  "v_smfmac_f32_32x32x32_bf16 %[oa], v[232:235], v[192:199], v248\n\t" \
  "v_exp_f32 v203, v203\n\t" \
  "v_exp_f32 v204, v204\n\t" \
  "v_exp_f32 v205, v205\n\t" \
  "v_exp_f32 v206, v206\n\t" \
  "v_exp_f32 v207, v207\n\t" \
  "v_exp_f32 v208, v208\n\t" \
  "v_exp_f32 v209, v209\n\t" \
  "v_smfmac_f32_32x32x32_bf16 %[oa], v[240:243], v[200:207], v248\n\t" \
  "v_exp_f32 v210, v210\n\t" \
  "v_exp_f32 v211, v211\n\t" \
  "v_exp_f32 v212, v212\n\t" \
  "v_mfma_f32_32x32x16_bf16 v[192:207], v[228:231], %[qa], 0\n\t" \
  "v_exp_f32 v213, v213\n\t" \
  "v_exp_f32 v214, v214\n\t" \
  "v_exp_f32 v215, v215\n\t" \
  "buffer_load_dwordx4 v[68:71], %[vo], %[rs], %[so] offen sc1\n\t" \
  "s_add_u32 %[so], %[so], 1024\n\t" \
  "v_exp_f32 v216, v216\n\t" \
  "v_exp_f32 v217, v217\n\t" \
  "v_exp_f32 v218, v218\n\t" \
  "v_smfmac_f32_32x32x32_bf16 %[ob], v[232:235], v[208:215], v248\n\t" \
  "v_exp_f32 v219, v219\n\t" \
  "v_exp_f32 v220, v220\n\t" \
  "v_exp_f32 v221, v221\n\t" \
  "buffer_load_dwordx4 %[vo], %[rw], %[wo] offen lds\n\t" \
  "s_add_u32 %[wo], %[wo], 262144\n\t" \
  "v_exp_f32 v222, v222\n\t" \
  "v_exp_f32 v223, v223\n\t" \
  "s_waitcnt lgkmcnt(0)\n\t" \
  "v_exp_f32 v192, v192\n\t" \
  "v_exp_f32 v193, v193\n\t" \
  "v_smfmac_f32_32x32x32_bf16 %[ob], v[240:243], v[216:223], v248\n\t" \
  "v_exp_f32 v194, v194\n\t" \
  "v_exp_f32 v195, v195\n\t" \
  "v_exp_f32 v196, v196\n\t" \
  "v_mfma_f32_32x32x16_bf16 v[208:223], v[228:231], %[qb], 0\n\t" \
  "ds_read_b128 v[228:231], %[ka] offset:12480\n\t" \
  "ds_read_b128 v[232:235], %[va0] offset:8320\n\t" \
  "ds_read_b128 v[240:243], %[va1] offset:8320\n\t" \
  "v_exp_f32 v197, v197\n\t" \
  "v_exp_f32 v198, v198\n\t" \
  "v_exp_f32 v199, v199\n\t" \
  "buffer_load_dwordx4 v[72:75], %[vo], %[rs], %[so] offen sc1\n\t" \
  "s_add_u32 %[so], %[so], 1024\n\t" \
  "v_exp_f32 v200, v200\n\t" \
  "v_exp_f32 v201, v201\n\t" \
  "v_exp_f32 v202, v202\n\t" \
  "v_smfmac_f32_32x32x32_bf16 %[oa], v[236:239], v[192:199], v248\n\t" \
  "v_exp_f32 v203, v203\n\t" \
  "v_exp_f32 v204, v204\n\t" \
  "v_exp_f32 v205, v205\n\t" \
  "v_exp_f32 v206, v206\n\t" \
  "v_exp_f32 v207, v207\n\t" \
  "v_exp_f32 v208, v208\n\t" \
  "v_exp_f32 v209, v209\n\t" \
  "v_smfmac_f32_32x32x32_bf16 %[oa], v[244:247], v[200:207], v248\n\t" \
  "v_exp_f32 v210, v210\n\t" \
  "v_exp_f32 v211, v211\n\t" \
  "v_exp_f32 v212, v212\n\t" \
  "v_mfma_f32_32x32x16_bf16 v[192:207], v[224:227], %[qa], 0\n\t" \
  "v_exp_f32 v213, v213\n\t" \
  "v_exp_f32 v214, v214\n\t" \
  "v_exp_f32 v215, v215\n\t" \
  "buffer_load_dwordx4 v[76:79], %[vo], %[rs], %[so] offen sc1\n\t" \
  "s_add_u32 %[so], %[so], 1024\n\t" \
  "v_exp_f32 v216, v216\n\t" \
  "v_exp_f32 v217, v217\n\t" \
  "v_exp_f32 v218, v218\n\t" \
  "v_smfmac_f32_32x32x32_bf16 %[ob], v[236:239], v[208:215], v248\n\t" \
  "v_exp_f32 v219, v219\n\t" \
  "v_exp_f32 v220, v220\n\t" \
  "v_exp_f32 v221, v221\n\t" \
  "v_exp_f32 v222, v222\n\t" \
  "v_exp_f32 v223, v223\n\t" \
  "s_waitcnt lgkmcnt(0)\n\t" \
  "v_exp_f32 v192, v192\n\t" \
  "v_exp_f32 v193, v193\n\t" \
  "v_smfmac_f32_32x32x32_bf16 %[ob], v[244:247], v[216:223], v248\n\t" \
  "v_exp_f32 v194, v194\n\t" \
  "v_exp_f32 v195, v195\n\t" \
  "v_exp_f32 v196, v196\n\t" \
  "v_mfma_f32_32x32x16_bf16 v[208:223], v[224:227], %[qb], 0\n\t" \
  "ds_read_b128 v[224:227], %[ka] offset:16640\n\t" \
  "ds_read_b128 v[236:239], %[va0] offset:12480\n\t" \
  "ds_read_b128 v[244:247], %[va1] offset:12480\n\t" \
  "v_exp_f32 v197, v197\n\t" \
  "v_exp_f32 v198, v198\n\t" \
  "v_exp_f32 v199, v199\n\t" \
  "buffer_load_dwordx4 v[80:83], %[vo], %[rs], %[so] offen sc1\n\t" \
  "s_add_u32 %[so], %[so], 1024\n\t" \
  "v_exp_f32 v200, v200\n\t" \
  "v_exp_f32 v201, v201\n\t" \
  "v_exp_f32 v202, v202\n\t" \
  "v_smfmac_f32_32x32x32_bf16 %[oa], v[232:235], v[192:199], v248\n\t" \
  "v_exp_f32 v203, v203\n\t" \
  "v_exp_f32 v204, v204\n\t" \
  "v_exp_f32 v205, v205\n\t" \
  "buffer_load_dwordx4 %[vo], %[rw], %[wo] offen lds\n\t" \
  "s_add_u32 %[wo], %[wo], 262144\n\t" \
  "v_exp_f32 v206, v206\n\t" \
  "v_exp_f32 v207, v207\n\t" \
  "v_exp_f32 v208, v208\n\t" \
  "v_exp_f32 v209, v209\n\t" \
  "v_smfmac_f32_32x32x32_bf16 %[oa], v[240:243], v[200:207], v248\n\t" \
  "v_exp_f32 v210, v210\n\t" \
  "v_exp_f32 v211, v211\n\t" \
  "v_exp_f32 v212, v212\n\t" \
  "v_mfma_f32_32x32x16_bf16 v[192:207], v[228:231], %[qa], 0\n\t" \
  "v_exp_f32 v213, v213\n\t" \
  "v_exp_f32 v214, v214\n\t" \
  "v_exp_f32 v215, v215\n\t" \
  "buffer_load_dwordx4 v[84:87], %[vo], %[rs], %[so] offen sc1\n\t" \
  "s_add_u32 %[so], %[so], 1024\n\t" \
  "v_exp_f32 v216, v216\n\t" \
  "v_exp_f32 v217, v217\n\t" \
  "v_exp_f32 v218, v218\n\t" \
  "v_smfmac_f32_32x32x32_bf16 %[ob], v[232:235], v[208:215], v248\n\t" \
  "v_exp_f32 v219, v219\n\t" \
  "v_exp_f32 v220, v220\n\t" \
  "v_exp_f32 v221, v221\n\t" \
  "v_exp_f32 v222, v222\n\t" \
  "v_exp_f32 v223, v223\n\t" \
  "s_waitcnt lgkmcnt(0)\n\t" \
  "v_exp_f32 v192, v192\n\t" \
  "v_exp_f32 v193, v193\n\t" \
  "v_smfmac_f32_32x32x32_bf16 %[ob], v[240:243], v[216:223], v248\n\t" \
  "v_exp_f32 v194, v194\n\t" \
  "v_exp_f32 v195, v195\n\t" \
  "v_exp_f32 v196, v196\n\t" \
  "v_mfma_f32_32x32x16_bf16 v[208:223], v[228:231], %[qb], 0\n\t" \
  "ds_read_b128 v[228:231], %[ka] offset:20800\n\t" \
  "ds_read_b128 v[232:235], %[va0] offset:16640\n\t" \
  "ds_read_b128 v[240:243], %[va1] offset:16640\n\t" \
  "v_exp_f32 v197, v197\n\t" \
  "v_exp_f32 v198, v198\n\t" \
  "v_exp_f32 v199, v199\n\t" \
  "buffer_load_dwordx4 v[88:91], %[vo], %[rs], %[so] offen sc1\n\t" \
  "s_add_u32 %[so], %[so], 1024\n\t" \
  "v_exp_f32 v200, v200\n\t" \
  "v_exp_f32 v201, v201\n\t" \
  "v_exp_f32 v202, v202\n\t" \
  "v_smfmac_f32_32x32x32_bf16 %[oa], v[236:239], v[192:199], v248\n\t" \
  "v_exp_f32 v203, v203\n\t" \
  "v_exp_f32 v204, v204\n\t" \
  "v_exp_f32 v205, v205\n\t" \
  "v_exp_f32 v206, v206\n\t" \
  "v_exp_f32 v207, v207\n\t" \
  "v_exp_f32 v208, v208\n\t" \
  "v_exp_f32 v209, v209\n\t" \
  "v_smfmac_f32_32x32x32_bf16 %[oa], v[244:247], v[200:207], v248\n\t" \
  "v_exp_f32 v210, v210\n\t" \
  "v_exp_f32 v211, v211\n\t" \
  "v_exp_f32 v212, v212\n\t" \
  "v_mfma_f32_32x32x16_bf16 v[192:207], v[224:227], %[qa], 0\n\t" \
  "v_exp_f32 v213, v213\n\t" \
  "v_exp_f32 v214, v214\n\t" \
  "v_exp_f32 v215, v215\n\t" \
  "buffer_load_dwordx4 v[92:95], %[vo], %[rs], %[so] offen sc1\n\t" \
  "s_add_u32 %[so], %[so], 1024\n\t" \
  "v_exp_f32 v216, v216\n\t" \
  "v_exp_f32 v217, v217\n\t" \
  "v_exp_f32 v218, v218\n\t" \
  "v_smfmac_f32_32x32x32_bf16 %[ob], v[236:239], v[208:215], v248\n\t" \
  "v_exp_f32 v219, v219\n\t" \
  "v_exp_f32 v220, v220\n\t" \
  "v_exp_f32 v221, v221\n\t" \
  "buffer_load_dwordx4 %[vo], %[rw], %[wo] offen lds\n\t" \
  "s_add_u32 %[wo], %[wo], 262144\n\t" \
  "v_exp_f32 v222, v222\n\t" \
  "v_exp_f32 v223, v223\n\t" \
  "s_waitcnt lgkmcnt(0)\n\t" \
  "v_exp_f32 v192, v192\n\t" \
  "v_exp_f32 v193, v193\n\t" \
  "v_smfmac_f32_32x32x32_bf16 %[ob], v[244:247], v[216:223], v248\n\t" \
  "v_exp_f32 v194, v194\n\t" \
  "v_exp_f32 v195, v195\n\t" \
  "v_exp_f32 v196, v196\n\t" \
  "v_mfma_f32_32x32x16_bf16 v[208:223], v[224:227], %[qb], 0\n\t" \
  "ds_read_b128 v[224:227], %[ka] offset:24960\n\t" \
  "ds_read_b128 v[236:239], %[va0] offset:20800\n\t" \
  "ds_read_b128 v[244:247], %[va1] offset:20800\n\t" \
  "v_exp_f32 v197, v197\n\t" \
  "v_exp_f32 v198, v198\n\t" \
  "v_exp_f32 v199, v199\n\t" \
  "buffer_load_dwordx4 v[96:99], %[vo], %[rs], %[so] offen sc1\n\t" \
  "s_add_u32 %[so], %[so], 1024\n\t" \
  "v_exp_f32 v200, v200\n\t" \
  "v_exp_f32 v201, v201\n\t" \
  "v_exp_f32 v202, v202\n\t" \
  "v_smfmac_f32_32x32x32_bf16 %[oa], v[232:235], v[192:199], v248\n\t" \
  "v_exp_f32 v203, v203\n\t" \
  "v_exp_f32 v204, v204\n\t" \
  "v_exp_f32 v205, v205\n\t" \
  "v_exp_f32 v206, v206\n\t" \
  "v_exp_f32 v207, v207\n\t" \
  "v_exp_f32 v208, v208\n\t" \
  "v_exp_f32 v209, v209\n\t" \
  "v_smfmac_f32_32x32x32_bf16 %[oa], v[240:243], v[200:207], v248\n\t" \
  "v_exp_f32 v210, v210\n\t" \
  "v_exp_f32 v211, v211\n\t" \
  "v_exp_f32 v212, v212\n\t" \
  "v_mfma_f32_32x32x16_bf16 v[192:207], v[228:231], %[qa], 0\n\t" \
  "v_exp_f32 v213, v213\n\t" \
  "v_exp_f32 v214, v214\n\t" \
  "v_exp_f32 v215, v215\n\t" \
  "buffer_load_dwordx4 v[100:103], %[vo], %[rs], %[so] offen sc1\n\t" \
  "s_add_u32 %[so], %[so], 1024\n\t" \
  "v_exp_f32 v216, v216\n\t" \
  "v_exp_f32 v217, v217\n\t" \
  "v_exp_f32 v218, v218\n\t" \
  "v_smfmac_f32_32x32x32_bf16 %[ob], v[232:235], v[208:215], v248\n\t" \
  "v_exp_f32 v219, v219\n\t" \
  "v_exp_f32 v220, v220\n\t" \
  "v_exp_f32 v221, v221\n\t" \
  "v_exp_f32 v222, v222\n\t" \
  "v_exp_f32 v223, v223\n\t" \
  "s_waitcnt lgkmcnt(0)\n\t" \
  "v_exp_f32 v192, v192\n\t" \
  "v_exp_f32 v193, v193\n\t" \
  "v_smfmac_f32_32x32x32_bf16 %[ob], v[240:243], v[216:223], v248\n\t" \
  "v_exp_f32 v194, v194\n\t" \
  "v_exp_f32 v195, v195\n\t" \
  "v_exp_f32 v196, v196\n\t" \
  "v_mfma_f32_32x32x16_bf16 v[208:223], v[228:231], %[qb], 0\n\t" \
  "ds_read_b128 v[228:231], %[ka] offset:29120\n\t" \
  "ds_read_b128 v[232:235], %[va0] offset:24960\n\t" \
  "ds_read_b128 v[240:243], %[va1] offset:24960\n\t" \
  "v_exp_f32 v197, v197\n\t" \
  "v_exp_f32 v198, v198\n\t" \
  "v_exp_f32 v199, v199\n\t" \
  "buffer_load_dwordx4 v[104:107], %[vo], %[rs], %[so] offen sc1\n\t" \
  "s_add_u32 %[so], %[so], 1024\n\t" \
  "v_exp_f32 v200, v200\n\t" \
  "v_exp_f32 v201, v201\n\t" \
  "v_exp_f32 v202, v202\n\t" \
  "v_smfmac_f32_32x32x32_bf16 %[oa], v[236:239], v[192:199], v248\n\t" \
  "v_exp_f32 v203, v203\n\t" \
  "v_exp_f32 v204, v204\n\t" \
  "v_exp_f32 v205, v205\n\t" \
  "buffer_load_dwordx4 %[vo], %[rw], %[wo] offen lds\n\t" \
  "s_add_u32 %[wo], %[wo], 262144\n\t" \
  "v_exp_f32 v206, v206\n\t" \
  "v_exp_f32 v207, v207\n\t" \
  "v_exp_f32 v208, v208\n\t" \
  "v_exp_f32 v209, v209\n\t" \
  "v_smfmac_f32_32x32x32_bf16 %[oa], v[244:247], v[200:207], v248\n\t" \
  "v_exp_f32 v210, v210\n\t" \
  "v_exp_f32 v211, v211\n\t" \
  "v_exp_f32 v212, v212\n\t" \
  "v_mfma_f32_32x32x16_bf16 v[192:207], v[224:227], %[qa], 0\n\t" \
  "v_exp_f32 v213, v213\n\t" \
  "v_exp_f32 v214, v214\n\t" \
  "v_exp_f32 v215, v215\n\t" \
  "buffer_load_dwordx4 v[108:111], %[vo], %[rs], %[so] offen sc1\n\t" \
  "s_add_u32 %[so], %[so], 1024\n\t" \
  "v_exp_f32 v216, v216\n\t" \
  "v_exp_f32 v217, v217\n\t" \
  "v_exp_f32 v218, v218\n\t" \
  "v_smfmac_f32_32x32x32_bf16 %[ob], v[236:239], v[208:215], v248\n\t" \
  "v_exp_f32 v219, v219\n\t" \
  "v_exp_f32 v220, v220\n\t" \
  "v_exp_f32 v221, v221\n\t" \
  "v_exp_f32 v222, v222\n\t" \
  "v_exp_f32 v223, v223\n\t" \
  "s_waitcnt lgkmcnt(0)\n\t" \
  "v_exp_f32 v192, v192\n\t" \
  "v_exp_f32 v193, v193\n\t" \
  "v_smfmac_f32_32x32x32_bf16 %[ob], v[244:247], v[216:223], v248\n\t" \
  "v_exp_f32 v194, v194\n\t" \
  "v_exp_f32 v195, v195\n\t" \
  "v_exp_f32 v196, v196\n\t" \
  "v_mfma_f32_32x32x16_bf16 v[208:223], v[224:227], %[qb], 0\n\t" \
  "ds_read_b128 v[224:227], %[ka] offset:33280\n\t" \
  "ds_read_b128 v[236:239], %[va0] offset:29120\n\t" \
  "ds_read_b128 v[244:247], %[va1] offset:29120\n\t" \
  "v_exp_f32 v197, v197\n\t" \
  "v_exp_f32 v198, v198\n\t" \
  "v_exp_f32 v199, v199\n\t" \
  "buffer_load_dwordx4 v[112:115], %[vo], %[rs], %[so] offen sc1\n\t" \
  "s_add_u32 %[so], %[so], 1024\n\t" \
  "v_exp_f32 v200, v200\n\t" \
  "v_exp_f32 v201, v201\n\t" \
  "v_exp_f32 v202, v202\n\t" \
  "v_smfmac_f32_32x32x32_bf16 %[oa], v[232:235], v[192:199], v248\n\t" \
  "v_exp_f32 v203, v203\n\t" \
  "v_exp_f32 v204, v204\n\t" \
  "v_exp_f32 v205, v205\n\t" \
  "v_exp_f32 v206, v206\n\t" \
  "v_exp_f32 v207, v207\n\t" \
  "v_exp_f32 v208, v208\n\t" \
  "v_exp_f32 v209, v209\n\t" \
  "v_smfmac_f32_32x32x32_bf16 %[oa], v[240:243], v[200:207], v248\n\t" \
  "v_exp_f32 v210, v210\n\t" \
  "v_exp_f32 v211, v211\n\t" \
  "v_exp_f32 v212, v212\n\t" \
  "v_mfma_f32_32x32x16_bf16 v[192:207], v[228:231], %[qa], 0\n\t" \
  "v_exp_f32 v213, v213\n\t" \
  "v_exp_f32 v214, v214\n\t" \
  "v_exp_f32 v215, v215\n\t" \
  "buffer_load_dwordx4 v[116:119], %[vo], %[rs], %[so] offen sc1\n\t" \
  "s_add_u32 %[so], %[so], 1024\n\t" \
  "v_exp_f32 v216, v216\n\t" \
  "v_exp_f32 v217, v217\n\t" \
  "v_exp_f32 v218, v218\n\t" \
  "v_smfmac_f32_32x32x32_bf16 %[ob], v[232:235], v[208:215], v248\n\t" \
  "v_exp_f32 v219, v219\n\t" \
  "v_exp_f32 v220, v220\n\t" \
  "v_exp_f32 v221, v221\n\t" \
  "buffer_load_dwordx4 %[vo], %[rw], %[wo] offen lds\n\t" \
  "s_add_u32 %[wo], %[wo], 262144\n\t" \
  "v_exp_f32 v222, v222\n\t" \
  "v_exp_f32 v223, v223\n\t" \
  "s_waitcnt lgkmcnt(0)\n\t" \
  "v_exp_f32 v192, v192\n\t" \
  "v_exp_f32 v193, v193\n\t" \
  "v_smfmac_f32_32x32x32_bf16 %[ob], v[240:243], v[216:223], v248\n\t" \
  "v_exp_f32 v194, v194\n\t" \
  "v_exp_f32 v195, v195\n\t" \
  "v_exp_f32 v196, v196\n\t" \
  "v_mfma_f32_32x32x16_bf16 v[208:223], v[228:231], %[qb], 0\n\t" \
  "ds_read_b128 v[228:231], %[ka] offset:37440\n\t" \
  "ds_read_b128 v[232:235], %[va0] offset:33280\n\t" \
  "ds_read_b128 v[240:243], %[va1] offset:33280\n\t" \
  "v_exp_f32 v197, v197\n\t" \
  "v_exp_f32 v198, v198\n\t" \
  "v_exp_f32 v199, v199\n\t" \
  "buffer_load_dwordx4 v[120:123], %[vo], %[rs], %[so] offen sc1\n\t" \
  "s_add_u32 %[so], %[so], 1024\n\t" \
  "v_exp_f32 v200, v200\n\t" \
  "v_exp_f32 v201, v201\n\t" \
  "v_exp_f32 v202, v202\n\t" \
  "v_smfmac_f32_32x32x32_bf16 %[oa], v[236:239], v[192:199], v248\n\t" \
  "v_exp_f32 v203, v203\n\t" \
  "v_exp_f32 v204, v204\n\t" \
  "v_exp_f32 v205, v205\n\t" \
  "v_exp_f32 v206, v206\n\t" \
  "v_exp_f32 v207, v207\n\t" \
  "v_exp_f32 v208, v208\n\t" \
  "v_exp_f32 v209, v209\n\t" \
  "v_smfmac_f32_32x32x32_bf16 %[oa], v[244:247], v[200:207], v248\n\t" \
  "v_exp_f32 v210, v210\n\t" \
  "v_exp_f32 v211, v211\n\t" \
  "v_exp_f32 v212, v212\n\t" \
  "v_mfma_f32_32x32x16_bf16 v[192:207], v[224:227], %[qa], 0\n\t" \
  "v_exp_f32 v213, v213\n\t" \
  "v_exp_f32 v214, v214\n\t" \
  "v_exp_f32 v215, v215\n\t" \
  "buffer_load_dwordx4 v[124:127], %[vo], %[rs], %[so] offen sc1\n\t" \
  "s_add_u32 %[so], %[so], 1024\n\t" \
  "v_exp_f32 v216, v216\n\t" \
  "v_exp_f32 v217, v217\n\t" \
  "v_exp_f32 v218, v218\n\t" \
  "v_smfmac_f32_32x32x32_bf16 %[ob], v[236:239], v[208:215], v248\n\t" \
  "v_exp_f32 v219, v219\n\t" \
  "v_exp_f32 v220, v220\n\t" \
  "v_exp_f32 v221, v221\n\t" \
  "v_exp_f32 v222, v222\n\t" \
  "v_exp_f32 v223, v223\n\t" \
  "v_add_u32 %[ka], 33280, %[ka]\n\t" \
  "v_add_u32 %[va0], 33280, %[va0]\n\t" \
  "v_add_u32 %[va1], 33280, %[va1]\n\t" \
  "s_waitcnt lgkmcnt(0)\n\t" \
  "v_exp_f32 v192, v192\n\t" \
  "v_exp_f32 v193, v193\n\t" \
  "v_smfmac_f32_32x32x32_bf16 %[ob], v[244:247], v[216:223], v248\n\t" \
  "v_exp_f32 v194, v194\n\t" \
  "v_exp_f32 v195, v195\n\t" \
  "v_exp_f32 v196, v196\n\t" \
  "v_mfma_f32_32x32x16_bf16 v[208:223], v[224:227], %[qb], 0\n\t" \
  "ds_read_b128 v[224:227], %[ka] offset:8320\n\t" \
  "ds_read_b128 v[236:239], %[va0] offset:4160\n\t" \
  "ds_read_b128 v[244:247], %[va1] offset:4160\n\t" \
  "v_exp_f32 v197, v197\n\t" \
  "v_exp_f32 v198, v198\n\t" \
  "v_exp_f32 v199, v199\n\t" \
  "s_mov_b32 %[so], %[p1]\n\t" \
  "buffer_load_dwordx4 v[128:131], %[vo], %[rs], %[so] offen sc1\n\t" \
  "s_add_u32 %[so], %[so], 1024\n\t" \
  "v_exp_f32 v200, v200\n\t" \
  "v_exp_f32 v201, v201\n\t" \
  "v_exp_f32 v202, v202\n\t" \
  "v_smfmac_f32_32x32x32_bf16 %[oa], v[232:235], v[192:199], v248\n\t" \
  "v_exp_f32 v203, v203\n\t" \
  "v_exp_f32 v204, v204\n\t" \
  "v_exp_f32 v205, v205\n\t" \
  "v_exp_f32 v206, v206\n\t" \
  "v_exp_f32 v207, v207\n\t" \
  "v_exp_f32 v208, v208\n\t" \
  "v_exp_f32 v209, v209\n\t" \
  "v_smfmac_f32_32x32x32_bf16 %[oa], v[240:243], v[200:207], v248\n\t" \
  "v_exp_f32 v210, v210\n\t" \
  "v_exp_f32 v211, v211\n\t" \
  "v_exp_f32 v212, v212\n\t" \
  "v_mfma_f32_32x32x16_bf16 v[192:207], v[228:231], %[qa], 0\n\t" \
  "v_exp_f32 v213, v213\n\t" \
  "v_exp_f32 v214, v214\n\t" \
  "v_exp_f32 v215, v215\n\t" \
  "buffer_load_dwordx4 v[132:135], %[vo], %[rs], %[so] offen sc1\n\t" \
  "s_add_u32 %[so], %[so], 1024\n\t" \
  "v_exp_f32 v216, v216\n\t" \
  "v_exp_f32 v217, v217\n\t" \
  "v_exp_f32 v218, v218\n\t" \
  "v_smfmac_f32_32x32x32_bf16 %[ob], v[232:235], v[208:215], v248\n\t" \
  "v_exp_f32 v219, v219\n\t" \
  "v_exp_f32 v220, v220\n\t" \
  "v_exp_f32 v221, v221\n\t" \
  "buffer_load_dwordx4 %[vo], %[rw], %[wo] offen lds\n\t" \
  "s_add_u32 %[wo], %[wo], 262144\n\t" \
  "v_exp_f32 v222, v222\n\t" \
  "v_exp_f32 v223, v223\n\t" \
  "s_waitcnt lgkmcnt(0)\n\t" \
  "v_exp_f32 v192, v192\n\t" \
  "v_exp_f32 v193, v193\n\t" \
  "v_smfmac_f32_32x32x32_bf16 %[ob], v[240:243], v[216:223], v248\n\t" \
  "v_exp_f32 v194, v194\n\t" \
  "v_exp_f32 v195, v195\n\t" \
  "v_exp_f32 v196, v196\n\t" \
  "v_mfma_f32_32x32x16_bf16 v[208:223], v[228:231], %[qb], 0\n\t" \
  "ds_read_b128 v[228:231], %[ka] offset:12480\n\t" \
  "ds_read_b128 v[232:235], %[va0] offset:8320\n\t" \
  "ds_read_b128 v[240:243], %[va1] offset:8320\n\t" \
  "v_exp_f32 v197, v197\n\t" \
  "v_exp_f32 v198, v198\n\t" \
  "v_exp_f32 v199, v199\n\t" \
  "buffer_load_dwordx4 v[136:139], %[vo], %[rs], %[so] offen sc1\n\t" \
  "s_add_u32 %[so], %[so], 1024\n\t" \
  "v_exp_f32 v200, v200\n\t" \
  "v_exp_f32 v201, v201\n\t" \
  "v_exp_f32 v202, v202\n\t" \
  "v_smfmac_f32_32x32x32_bf16 %[oa], v[236:239], v[192:199], v248\n\t" \
  "v_exp_f32 v203, v203\n\t" \
  "v_exp_f32 v204, v204\n\t" \
  "v_exp_f32 v205, v205\n\t" \
  "v_exp_f32 v206, v206\n\t" \
  "v_exp_f32 v207, v207\n\t" \
  "v_exp_f32 v208, v208\n\t" \
  "v_exp_f32 v209, v209\n\t" \
  "v_smfmac_f32_32x32x32_bf16 %[oa], v[244:247], v[200:207], v248\n\t" \
  "v_exp_f32 v210, v210\n\t" \
  "v_exp_f32 v211, v211\n\t" \
  "v_exp_f32 v212, v212\n\t" \
  "v_mfma_f32_32x32x16_bf16 v[192:207], v[224:227], %[qa], 0\n\t" \
  "v_exp_f32 v213, v213\n\t" \
  "v_exp_f32 v214, v214\n\t" \
  "v_exp_f32 v215, v215\n\t" \
  "buffer_load_dwordx4 v[140:143], %[vo], %[rs], %[so] offen sc1\n\t" \
  "s_add_u32 %[so], %[so], 1024\n\t" \
  "v_exp_f32 v216, v216\n\t" \
  "v_exp_f32 v217, v217\n\t" \
  "v_exp_f32 v218, v218\n\t" \
  "v_smfmac_f32_32x32x32_bf16 %[ob], v[236:239], v[208:215], v248\n\t" \
  "v_exp_f32 v219, v219\n\t" \
  "v_exp_f32 v220, v220\n\t" \
  "v_exp_f32 v221, v221\n\t" \
  "buffer_load_dwordx4 %[vo], %[rw], %[wo] offen lds\n\t" \
  "s_add_u32 %[wo], %[wo], 262144\n\t" \
  "v_exp_f32 v222, v222\n\t" \
  "v_exp_f32 v223, v223\n\t" \
  "s_waitcnt lgkmcnt(0)\n\t" \
  "v_exp_f32 v192, v192\n\t" \
  "v_exp_f32 v193, v193\n\t" \
  "v_smfmac_f32_32x32x32_bf16 %[ob], v[244:247], v[216:223], v248\n\t" \
  "v_exp_f32 v194, v194\n\t" \
  "v_exp_f32 v195, v195\n\t" \
  "v_exp_f32 v196, v196\n\t" \
  "v_mfma_f32_32x32x16_bf16 v[208:223], v[224:227], %[qb], 0\n\t" \
  "ds_read_b128 v[224:227], %[ka] offset:16640\n\t" \
  "ds_read_b128 v[236:239], %[va0] offset:12480\n\t" \
  "ds_read_b128 v[244:247], %[va1] offset:12480\n\t" \
  "v_exp_f32 v197, v197\n\t" \
  "v_exp_f32 v198, v198\n\t" \
  "v_exp_f32 v199, v199\n\t" \
  "buffer_load_dwordx4 v[144:147], %[vo], %[rs], %[so] offen sc1\n\t" \
  "s_add_u32 %[so], %[so], 1024\n\t" \
  "v_exp_f32 v200, v200\n\t" \
  "v_exp_f32 v201, v201\n\t" \
  "v_exp_f32 v202, v202\n\t" \
  "v_smfmac_f32_32x32x32_bf16 %[oa], v[232:235], v[192:199], v248\n\t" \
  "v_exp_f32 v203, v203\n\t" \
  "v_exp_f32 v204, v204\n\t" \
  "v_exp_f32 v205, v205\n\t" \
  "v_exp_f32 v206, v206\n\t" \
  "v_exp_f32 v207, v207\n\t" \
  "v_exp_f32 v208, v208\n\t" \
  "v_exp_f32 v209, v209\n\t" \
  "v_smfmac_f32_32x32x32_bf16 %[oa], v[240:243], v[200:207], v248\n\t" \
  "v_exp_f32 v210, v210\n\t" \
  "v_exp_f32 v211, v211\n\t" \
  "v_exp_f32 v212, v212\n\t" \
  "v_mfma_f32_32x32x16_bf16 v[192:207], v[228:231], %[qa], 0\n\t" \
  "v_exp_f32 v213, v213\n\t" \
  "v_exp_f32 v214, v214\n\t" \
  "v_exp_f32 v215, v215\n\t" \
  "buffer_load_dwordx4 v[148:151], %[vo], %[rs], %[so] offen sc1\n\t" \
  "s_add_u32 %[so], %[so], 1024\n\t" \
  "v_exp_f32 v216, v216\n\t" \
  "v_exp_f32 v217, v217\n\t" \
  "v_exp_f32 v218, v218\n\t" \
  "v_smfmac_f32_32x32x32_bf16 %[ob], v[232:235], v[208:215], v248\n\t" \
  "v_exp_f32 v219, v219\n\t" \
  "v_exp_f32 v220, v220\n\t" \
  "v_exp_f32 v221, v221\n\t" \
  "buffer_load_dwordx4 %[vo], %[rw], %[wo] offen lds\n\t" \
  "s_add_u32 %[wo], %[wo], 262144\n\t" \
  "v_exp_f32 v222, v222\n\t" \
  "v_exp_f32 v223, v223\n\t" \
  "s_waitcnt lgkmcnt(0)\n\t" \
  "v_exp_f32 v192, v192\n\t" \
  "v_exp_f32 v193, v193\n\t" \
  "v_smfmac_f32_32x32x32_bf16 %[ob], v[240:243], v[216:223], v248\n\t" \
  "v_exp_f32 v194, v194\n\t" \
  "v_exp_f32 v195, v195\n\t" \
  "v_exp_f32 v196, v196\n\t" \
  "v_mfma_f32_32x32x16_bf16 v[208:223], v[228:231], %[qb], 0\n\t" \
  "ds_read_b128 v[228:231], %[ka] offset:20800\n\t" \
  "ds_read_b128 v[232:235], %[va0] offset:16640\n\t" \
  "ds_read_b128 v[240:243], %[va1] offset:16640\n\t" \
  "v_exp_f32 v197, v197\n\t" \
  "v_exp_f32 v198, v198\n\t" \
  "v_exp_f32 v199, v199\n\t" \
  "buffer_load_dwordx4 v[152:155], %[vo], %[rs], %[so] offen sc1\n\t" \
  "s_add_u32 %[so], %[so], 1024\n\t" \
  "v_exp_f32 v200, v200\n\t" \
  "v_exp_f32 v201, v201\n\t" \
  "v_exp_f32 v202, v202\n\t" \
  "v_smfmac_f32_32x32x32_bf16 %[oa], v[236:239], v[192:199], v248\n\t" \
  "v_exp_f32 v203, v203\n\t" \
  "v_exp_f32 v204, v204\n\t" \
  "v_exp_f32 v205, v205\n\t" \
  "v_exp_f32 v206, v206\n\t" \
  "v_exp_f32 v207, v207\n\t" \
  "v_exp_f32 v208, v208\n\t" \
  "v_exp_f32 v209, v209\n\t" \
  "v_smfmac_f32_32x32x32_bf16 %[oa], v[244:247], v[200:207], v248\n\t" \
  "v_exp_f32 v210, v210\n\t" \
  "v_exp_f32 v211, v211\n\t" \
  "v_exp_f32 v212, v212\n\t" \
  "v_mfma_f32_32x32x16_bf16 v[192:207], v[224:227], %[qa], 0\n\t" \
  "v_exp_f32 v213, v213\n\t" \
  "v_exp_f32 v214, v214\n\t" \
  "v_exp_f32 v215, v215\n\t" \
  "buffer_load_dwordx4 v[156:159], %[vo], %[rs], %[so] offen sc1\n\t" \
  "s_add_u32 %[so], %[so], 1024\n\t" \
  "v_exp_f32 v216, v216\n\t" \
  "v_exp_f32 v217, v217\n\t" \
  "v_exp_f32 v218, v218\n\t" \
  "v_smfmac_f32_32x32x32_bf16 %[ob], v[236:239], v[208:215], v248\n\t" \
  "v_exp_f32 v219, v219\n\t" \
  "v_exp_f32 v220, v220\n\t" \
  "v_exp_f32 v221, v221\n\t" \
  "buffer_load_dwordx4 %[vo], %[rw], %[wo] offen lds\n\t" \
  "s_add_u32 %[wo], %[wo], 262144\n\t" \
  "v_exp_f32 v222, v222\n\t" \
  "v_exp_f32 v223, v223\n\t" \
  "s_waitcnt lgkmcnt(0)\n\t" \
  "v_exp_f32 v192, v192\n\t" \
  "v_exp_f32 v193, v193\n\t" \
  "v_smfmac_f32_32x32x32_bf16 %[ob], v[244:247], v[216:223], v248\n\t" \
  "v_exp_f32 v194, v194\n\t" \
  "v_exp_f32 v195, v195\n\t" \
  "v_exp_f32 v196, v196\n\t" \
  "v_mfma_f32_32x32x16_bf16 v[208:223], v[224:227], %[qb], 0\n\t" \
  "ds_read_b128 v[224:227], %[ka] offset:24960\n\t" \
  "ds_read_b128 v[236:239], %[va0] offset:20800\n\t" \
  "ds_read_b128 v[244:247], %[va1] offset:20800\n\t" \
  "v_exp_f32 v197, v197\n\t" \
  "v_exp_f32 v198, v198\n\t" \
  "v_exp_f32 v199, v199\n\t" \
  "buffer_load_dwordx4 v[160:163], %[vo], %[rs], %[so] offen sc1\n\t" \
  "s_add_u32 %[so], %[so], 1024\n\t" \
  "v_exp_f32 v200, v200\n\t" \
  "v_exp_f32 v201, v201\n\t" \
  "v_exp_f32 v202, v202\n\t" \
  "v_smfmac_f32_32x32x32_bf16 %[oa], v[232:235], v[192:199], v248\n\t" \
  "v_exp_f32 v203, v203\n\t" \
  "v_exp_f32 v204, v204\n\t" \
  "v_exp_f32 v205, v205\n\t" \
  "v_exp_f32 v206, v206\n\t" \
  "v_exp_f32 v207, v207\n\t" \
  "v_exp_f32 v208, v208\n\t" \
  "v_exp_f32 v209, v209\n\t" \
  "v_smfmac_f32_32x32x32_bf16 %[oa], v[240:243], v[200:207], v248\n\t" \
  "v_exp_f32 v210, v210\n\t" \
  "v_exp_f32 v211, v211\n\t" \
  "v_exp_f32 v212, v212\n\t" \
  "v_mfma_f32_32x32x16_bf16 v[192:207], v[228:231], %[qa], 0\n\t" \
  "v_exp_f32 v213, v213\n\t" \
  "v_exp_f32 v214, v214\n\t" \
  "v_exp_f32 v215, v215\n\t" \
  "s_mov_b32 %[so], %[p2]\n\t" \
  "buffer_load_dwordx4 v[164:167], %[vo], %[rs], %[so] offen sc1\n\t" \
  "s_add_u32 %[so], %[so], 1024\n\t" \
  "v_exp_f32 v216, v216\n\t" \
  "v_exp_f32 v217, v217\n\t" \
  "v_exp_f32 v218, v218\n\t" \
  "v_smfmac_f32_32x32x32_bf16 %[ob], v[232:235], v[208:215], v248\n\t" \
  "v_exp_f32 v219, v219\n\t" \
  "v_exp_f32 v220, v220\n\t" \
  "v_exp_f32 v221, v221\n\t" \
  "buffer_load_dwordx4 %[vo], %[rw], %[wo] offen lds\n\t" \
  "s_add_u32 %[wo], %[wo], 262144\n\t" \
  "v_exp_f32 v222, v222\n\t" \
  "v_exp_f32 v223, v223\n\t" \
  "s_waitcnt lgkmcnt(0)\n\t" \
  "v_exp_f32 v192, v192\n\t" \
  "v_exp_f32 v193, v193\n\t" \
  "v_smfmac_f32_32x32x32_bf16 %[ob], v[240:243], v[216:223], v248\n\t" \
  "v_exp_f32 v194, v194\n\t" \
  "v_exp_f32 v195, v195\n\t" \
  "v_exp_f32 v196, v196\n\t" \
  "v_mfma_f32_32x32x16_bf16 v[208:223], v[228:231], %[qb], 0\n\t" \
  "ds_read_b128 v[228:231], %[ka] offset:29120\n\t" \
  "ds_read_b128 v[232:235], %[va0] offset:24960\n\t" \
  "ds_read_b128 v[240:243], %[va1] offset:24960\n\t" \
  "v_exp_f32 v197, v197\n\t" \
  "v_exp_f32 v198, v198\n\t" \
  "v_exp_f32 v199, v199\n\t" \
  "buffer_load_dwordx4 v[168:171], %[vo], %[rs], %[so] offen sc1\n\t" \
  "s_add_u32 %[so], %[so], 1024\n\t" \
  "v_exp_f32 v200, v200\n\t" \
  "v_exp_f32 v201, v201\n\t" \
  "v_exp_f32 v202, v202\n\t" \
  "v_smfmac_f32_32x32x32_bf16 %[oa], v[236:239], v[192:199], v248\n\t" \
  "v_exp_f32 v203, v203\n\t" \
  "v_exp_f32 v204, v204\n\t" \
  "v_exp_f32 v205, v205\n\t" \
  "v_exp_f32 v206, v206\n\t" \
  "v_exp_f32 v207, v207\n\t" \
  "v_exp_f32 v208, v208\n\t" \
  "v_exp_f32 v209, v209\n\t" \
  "v_smfmac_f32_32x32x32_bf16 %[oa], v[244:247], v[200:207], v248\n\t" \
  "v_exp_f32 v210, v210\n\t" \
  "v_exp_f32 v211, v211\n\t" \
  "v_exp_f32 v212, v212\n\t" \
  "v_mfma_f32_32x32x16_bf16 v[192:207], v[224:227], %[qa], 0\n\t" \
  "v_exp_f32 v213, v213\n\t" \
  "v_exp_f32 v214, v214\n\t" \
  "v_exp_f32 v215, v215\n\t" \
  "buffer_load_dwordx4 v[172:175], %[vo], %[rs], %[so] offen sc1\n\t" \
  "s_add_u32 %[so], %[so], 1024\n\t" \
  "v_exp_f32 v216, v216\n\t" \
  "v_exp_f32 v217, v217\n\t" \
  "v_exp_f32 v218, v218\n\t" \
  "v_smfmac_f32_32x32x32_bf16 %[ob], v[236:239], v[208:215], v248\n\t" \
  "v_exp_f32 v219, v219\n\t" \
  "v_exp_f32 v220, v220\n\t" \
  "v_exp_f32 v221, v221\n\t" \
  "buffer_load_dwordx4 %[vo], %[rw], %[wo] offen lds\n\t" \
  "s_add_u32 %[wo], %[wo], 262144\n\t" \
  "v_exp_f32 v222, v222\n\t" \
  "v_exp_f32 v223, v223\n\t" \
  "s_waitcnt lgkmcnt(0)\n\t" \
  "v_exp_f32 v192, v192\n\t" \
  "v_exp_f32 v193, v193\n\t" \
  "v_smfmac_f32_32x32x32_bf16 %[ob], v[244:247], v[216:223], v248\n\t" \
  "v_exp_f32 v194, v194\n\t" \
  "v_exp_f32 v195, v195\n\t" \
  "v_exp_f32 v196, v196\n\t" \
  "v_mfma_f32_32x32x16_bf16 v[208:223], v[224:227], %[qb], 0\n\t" \
  "ds_read_b128 v[224:227], %[ka] offset:33280\n\t" \
  "ds_read_b128 v[236:239], %[va0] offset:29120\n\t" \
  "ds_read_b128 v[244:247], %[va1] offset:29120\n\t" \
  "v_exp_f32 v197, v197\n\t" \
  "v_exp_f32 v198, v198\n\t" \
  "v_exp_f32 v199, v199\n\t" \
  "buffer_load_dwordx4 v[176:179], %[vo], %[rs], %[so] offen sc1\n\t" \
  "s_add_u32 %[so], %[so], 1024\n\t" \
  "v_exp_f32 v200, v200\n\t" \
  "v_exp_f32 v201, v201\n\t" \
  "v_exp_f32 v202, v202\n\t" \
  "v_smfmac_f32_32x32x32_bf16 %[oa], v[232:235], v[192:199], v248\n\t" \
  "v_exp_f32 v203, v203\n\t" \
  "v_exp_f32 v204, v204\n\t" \
  "v_exp_f32 v205, v205\n\t" \
  "v_exp_f32 v206, v206\n\t" \
  "v_exp_f32 v207, v207\n\t" \
  "v_exp_f32 v208, v208\n\t" \
  "v_exp_f32 v209, v209\n\t" \
  "v_smfmac_f32_32x32x32_bf16 %[oa], v[240:243], v[200:207], v248\n\t" \
  "v_exp_f32 v210, v210\n\t" \
  "v_exp_f32 v211, v211\n\t" \
  "v_exp_f32 v212, v212\n\t" \
  "v_mfma_f32_32x32x16_bf16 v[192:207], v[228:231], %[qa], 0\n\t" \
  "v_exp_f32 v213, v213\n\t" \
  "v_exp_f32 v214, v214\n\t" \
  "v_exp_f32 v215, v215\n\t" \
  "buffer_load_dwordx4 v[180:183], %[vo], %[rs], %[so] offen sc1\n\t" \
  "s_add_u32 %[so], %[so], 1024\n\t" \
  "v_exp_f32 v216, v216\n\t" \
  "v_exp_f32 v217, v217\n\t" \
  "v_exp_f32 v218, v218\n\t" \
  "v_smfmac_f32_32x32x32_bf16 %[ob], v[232:235], v[208:215], v248\n\t" \
  "v_exp_f32 v219, v219\n\t" \
  "v_exp_f32 v220, v220\n\t" \
  "v_exp_f32 v221, v221\n\t" \
  "buffer_load_dwordx4 %[vo], %[rw], %[wo] offen lds\n\t" \
  "s_add_u32 %[wo], %[wo], 262144\n\t" \
  "v_exp_f32 v222, v222\n\t" \
  "v_exp_f32 v223, v223\n\t" \
  "s_waitcnt lgkmcnt(0)\n\t" \
  "v_exp_f32 v192, v192\n\t" \
  "v_exp_f32 v193, v193\n\t" \
  "v_smfmac_f32_32x32x32_bf16 %[ob], v[240:243], v[216:223], v248\n\t" \
  "v_exp_f32 v194, v194\n\t" \
  "v_exp_f32 v195, v195\n\t" \
  "v_exp_f32 v196, v196\n\t" \
  "v_mfma_f32_32x32x16_bf16 v[208:223], v[228:231], %[qb], 0\n\t" \
  "ds_read_b128 v[228:231], %[ka] offset:37440\n\t" \
  "ds_read_b128 v[232:235], %[va0] offset:33280\n\t" \
  "ds_read_b128 v[240:243], %[va1] offset:33280\n\t" \
  "v_exp_f32 v197, v197\n\t" \
  "v_exp_f32 v198, v198\n\t" \
  "v_exp_f32 v199, v199\n\t" \
  "buffer_load_dwordx4 v[184:187], %[vo], %[rs], %[so] offen sc1\n\t" \
  "s_add_u32 %[so], %[so], 1024\n\t" \
  "v_exp_f32 v200, v200\n\t" \
  "v_exp_f32 v201, v201\n\t" \
  "v_exp_f32 v202, v202\n\t" \
  "v_smfmac_f32_32x32x32_bf16 %[oa], v[236:239], v[192:199], v248\n\t" \
  "v_exp_f32 v203, v203\n\t" \
  "v_exp_f32 v204, v204\n\t" \
  "v_exp_f32 v205, v205\n\t" \
  "v_exp_f32 v206, v206\n\t" \
  "v_exp_f32 v207, v207\n\t" \
  "v_exp_f32 v208, v208\n\t" \
  "v_exp_f32 v209, v209\n\t" \
  "v_smfmac_f32_32x32x32_bf16 %[oa], v[244:247], v[200:207], v248\n\t" \
  "v_exp_f32 v210, v210\n\t" \
  "v_exp_f32 v211, v211\n\t" \
  "v_exp_f32 v212, v212\n\t" \
  "v_mfma_f32_32x32x16_bf16 v[192:207], v[224:227], %[qa], 0\n\t" \
  "v_exp_f32 v213, v213\n\t" \
  "v_exp_f32 v214, v214\n\t" \
  "v_exp_f32 v215, v215\n\t" \
  "buffer_load_dwordx4 v[188:191], %[vo], %[rs], %[so] offen sc1\n\t" \
  "s_add_u32 %[so], %[so], 1024\n\t" \
  "v_exp_f32 v216, v216\n\t" \
  "v_exp_f32 v217, v217\n\t" \
  "v_exp_f32 v218, v218\n\t" \
  "v_smfmac_f32_32x32x32_bf16 %[ob], v[236:239], v[208:215], v248\n\t" \
  "v_exp_f32 v219, v219\n\t" \
  "v_exp_f32 v220, v220\n\t" \
  "v_exp_f32 v221, v221\n\t" \
  "v_exp_f32 v222, v222\n\t" \
  "v_exp_f32 v223, v223\n\t" \
  "v_add_u32 %[ka], 33280, %[ka]\n\t" \
  "v_add_u32 %[va0], 33280, %[va0]\n\t" \
  "v_add_u32 %[va1], 33280, %[va1]\n\t" \
  "1:\n\t" \
  "s_waitcnt lgkmcnt(0)\n\t" \
  "v_exp_f32 v192, v192\n\t" \
  "v_exp_f32 v193, v193\n\t" \
  "v_smfmac_f32_32x32x32_bf16 %[ob], v[244:247], v[216:223], v248\n\t" \
  "v_exp_f32 v194, v194\n\t" \
  "v_exp_f32 v195, v195\n\t" \
  "v_exp_f32 v196, v196\n\t" \
  "v_mfma_f32_32x32x16_bf16 v[208:223], v[224:227], %[qb], 0\n\t" \
  "ds_read_b128 v[224:227], %[ka] offset:8320\n\t" \
  "ds_read_b128 v[236:239], %[va0] offset:4160\n\t" \
  "ds_read_b128 v[244:247], %[va1] offset:4160\n\t" \
  "v_exp_f32 v197, v197\n\t" \
  "v_exp_f32 v198, v198\n\t" \
  "v_exp_f32 v199, v199\n\t" \
  "v_exp_f32 v200, v200\n\t" \
  "v_exp_f32 v201, v201\n\t" \
  "v_exp_f32 v202, v202\n\t" \
  "v_smfmac_f32_32x32x32_bf16 %[oa], v[232:235], v[192:199], v248\n\t" \
  "v_exp_f32 v203, v203\n\t" \
  "v_exp_f32 v204, v204\n\t" \
  "v_exp_f32 v205, v205\n\t" \
  "v_exp_f32 v206, v206\n\t" \
  "v_exp_f32 v207, v207\n\t" \
  "v_exp_f32 v208, v208\n\t" \
  "v_exp_f32 v209, v209\n\t" \
  "v_smfmac_f32_32x32x32_bf16 %[oa], v[240:243], v[200:207], v248\n\t" \
  "v_exp_f32 v210, v210\n\t" \
  "v_exp_f32 v211, v211\n\t" \
  "v_exp_f32 v212, v212\n\t" \
  "v_mfma_f32_32x32x16_bf16 v[192:207], v[228:231], %[qa], 0\n\t" \
  "v_exp_f32 v213, v213\n\t" \
  "v_exp_f32 v214, v214\n\t" \
  "v_exp_f32 v215, v215\n\t" \
  "v_exp_f32 v216, v216\n\t" \
  "v_exp_f32 v217, v217\n\t" \
  "v_exp_f32 v218, v218\n\t" \
  "v_smfmac_f32_32x32x32_bf16 %[ob], v[232:235], v[208:215], v248\n\t" \
  "v_exp_f32 v219, v219\n\t" \
  "v_exp_f32 v220, v220\n\t" \
  "v_exp_f32 v221, v221\n\t" \
  "v_exp_f32 v222, v222\n\t" \
  "v_exp_f32 v223, v223\n\t" \
  "s_waitcnt lgkmcnt(0)\n\t" \
  "v_exp_f32 v192, v192\n\t" \
  "v_exp_f32 v193, v193\n\t" \
  "v_smfmac_f32_32x32x32_bf16 %[ob], v[240:243], v[216:223], v248\n\t" \
  "v_exp_f32 v194, v194\n\t" \
  "v_exp_f32 v195, v195\n\t" \
  "v_exp_f32 v196, v196\n\t" \
  "v_mfma_f32_32x32x16_bf16 v[208:223], v[228:231], %[qb], 0\n\t" \
  "ds_read_b128 v[228:231], %[ka] offset:12480\n\t" \
  "ds_read_b128 v[232:235], %[va0] offset:8320\n\t" \
  "ds_read_b128 v[240:243], %[va1] offset:8320\n\t" \
  "v_exp_f32 v197, v197\n\t" \
  "v_exp_f32 v198, v198\n\t" \
  "v_exp_f32 v199, v199\n\t" \
  "v_exp_f32 v200, v200\n\t" \
  "v_exp_f32 v201, v201\n\t" \
  "v_exp_f32 v202, v202\n\t" \
  "v_smfmac_f32_32x32x32_bf16 %[oa], v[236:239], v[192:199], v248\n\t" \
  "v_exp_f32 v203, v203\n\t" \
  "v_exp_f32 v204, v204\n\t" \
  "v_exp_f32 v205, v205\n\t" \
  "v_exp_f32 v206, v206\n\t" \
  "v_exp_f32 v207, v207\n\t" \
  "v_exp_f32 v208, v208\n\t" \
  "v_exp_f32 v209, v209\n\t" \
  "v_smfmac_f32_32x32x32_bf16 %[oa], v[244:247], v[200:207], v248\n\t" \
  "v_exp_f32 v210, v210\n\t" \
  "v_exp_f32 v211, v211\n\t" \
  "v_exp_f32 v212, v212\n\t" \
  "v_mfma_f32_32x32x16_bf16 v[192:207], v[224:227], %[qa], 0\n\t" \
  "v_exp_f32 v213, v213\n\t" \
  "v_exp_f32 v214, v214\n\t" \
  "v_exp_f32 v215, v215\n\t" \
  "v_exp_f32 v216, v216\n\t" \
  "v_exp_f32 v217, v217\n\t" \
  "v_exp_f32 v218, v218\n\t" \
  "v_smfmac_f32_32x32x32_bf16 %[ob], v[236:239], v[208:215], v248\n\t" \
  "v_exp_f32 v219, v219\n\t" \
  "v_exp_f32 v220, v220\n\t" \
  "v_exp_f32 v221, v221\n\t" \
  "v_exp_f32 v222, v222\n\t" \
  "v_exp_f32 v223, v223\n\t" \
  "s_waitcnt lgkmcnt(0)\n\t" \
  "v_exp_f32 v192, v192\n\t" \
  "v_exp_f32 v193, v193\n\t" \
  "v_smfmac_f32_32x32x32_bf16 %[ob], v[244:247], v[216:223], v248\n\t" \
  "v_exp_f32 v194, v194\n\t" \
  "v_exp_f32 v195, v195\n\t" \
  "v_exp_f32 v196, v196\n\t" \
  "v_mfma_f32_32x32x16_bf16 v[208:223], v[224:227], %[qb], 0\n\t" \
  "ds_read_b128 v[224:227], %[ka] offset:16640\n\t" \
  "ds_read_b128 v[236:239], %[va0] offset:12480\n\t" \
  "ds_read_b128 v[244:247], %[va1] offset:12480\n\t" \
  "v_exp_f32 v197, v197\n\t" \
  "v_exp_f32 v198, v198\n\t" \
  "v_exp_f32 v199, v199\n\t" \
  "v_exp_f32 v200, v200\n\t" \
  "v_exp_f32 v201, v201\n\t" \
  "v_exp_f32 v202, v202\n\t" \
  "v_smfmac_f32_32x32x32_bf16 %[oa], v[232:235], v[192:199], v248\n\t" \
  "v_exp_f32 v203, v203\n\t" \
  "v_exp_f32 v204, v204\n\t" \
  "v_exp_f32 v205, v205\n\t" \
  "v_exp_f32 v206, v206\n\t" \
  "v_exp_f32 v207, v207\n\t" \
  "v_exp_f32 v208, v208\n\t" \
  "v_exp_f32 v209, v209\n\t" \
  "v_smfmac_f32_32x32x32_bf16 %[oa], v[240:243], v[200:207], v248\n\t" \
  "v_exp_f32 v210, v210\n\t" \
  "v_exp_f32 v211, v211\n\t" \
  "v_exp_f32 v212, v212\n\t" \
  "v_mfma_f32_32x32x16_bf16 v[192:207], v[228:231], %[qa], 0\n\t" \
  "v_exp_f32 v213, v213\n\t" \
  "v_exp_f32 v214, v214\n\t" \
  "v_exp_f32 v215, v215\n\t" \
  "v_exp_f32 v216, v216\n\t" \
  "v_exp_f32 v217, v217\n\t" \
  "v_exp_f32 v218, v218\n\t" \
  "v_smfmac_f32_32x32x32_bf16 %[ob], v[232:235], v[208:215], v248\n\t" \
  "v_exp_f32 v219, v219\n\t" \
  "v_exp_f32 v220, v220\n\t" \
  "v_exp_f32 v221, v221\n\t" \
  "v_exp_f32 v222, v222\n\t" \
  "v_exp_f32 v223, v223\n\t" \
  "s_waitcnt lgkmcnt(0)\n\t" \
  "v_exp_f32 v192, v192\n\t" \
  "v_exp_f32 v193, v193\n\t" \
  "v_smfmac_f32_32x32x32_bf16 %[ob], v[240:243], v[216:223], v248\n\t" \
  "v_exp_f32 v194, v194\n\t" \
  "v_exp_f32 v195, v195\n\t" \
  "v_exp_f32 v196, v196\n\t" \
  "v_mfma_f32_32x32x16_bf16 v[208:223], v[228:231], %[qb], 0\n\t" \
  "ds_read_b128 v[228:231], %[ka] offset:20800\n\t" \
  "ds_read_b128 v[232:235], %[va0] offset:16640\n\t" \
  "ds_read_b128 v[240:243], %[va1] offset:16640\n\t" \
  "v_exp_f32 v197, v197\n\t" \
  "v_exp_f32 v198, v198\n\t" \
  "v_exp_f32 v199, v199\n\t" \
  "v_exp_f32 v200, v200\n\t" \
  "v_exp_f32 v201, v201\n\t" \
  "v_exp_f32 v202, v202\n\t" \
  "v_smfmac_f32_32x32x32_bf16 %[oa], v[236:239], v[192:199], v248\n\t" \
  "v_exp_f32 v203, v203\n\t" \
  "v_exp_f32 v204, v204\n\t" \
  "v_exp_f32 v205, v205\n\t" \
  "v_exp_f32 v206, v206\n\t" \
  "v_exp_f32 v207, v207\n\t" \
  "v_exp_f32 v208, v208\n\t" \
  "v_exp_f32 v209, v209\n\t" \
  "v_smfmac_f32_32x32x32_bf16 %[oa], v[244:247], v[200:207], v248\n\t" \
  "v_exp_f32 v210, v210\n\t" \
  "v_exp_f32 v211, v211\n\t" \
  "v_exp_f32 v212, v212\n\t" \
  "v_mfma_f32_32x32x16_bf16 v[192:207], v[224:227], %[qa], 0\n\t" \
  "v_exp_f32 v213, v213\n\t" \
  "v_exp_f32 v214, v214\n\t" \
  "v_exp_f32 v215, v215\n\t" \
  "v_exp_f32 v216, v216\n\t" \
  "v_exp_f32 v217, v217\n\t" \
  "v_exp_f32 v218, v218\n\t" \
  "v_smfmac_f32_32x32x32_bf16 %[ob], v[236:239], v[208:215], v248\n\t" \
  "v_exp_f32 v219, v219\n\t" \
  "v_exp_f32 v220, v220\n\t" \
  "v_exp_f32 v221, v221\n\t" \
  "v_exp_f32 v222, v222\n\t" \
  "v_exp_f32 v223, v223\n\t" \
  "v_add_u32 %[ka], 16640, %[ka]\n\t" \
  "v_add_u32 %[va0], 16640, %[va0]\n\t" \
  "v_add_u32 %[va1], 16640, %[va1]\n\t" \
  "s_add_u32 %[cnt], %[cnt], 1\n\t" \
  "s_cmp_lg_u32 %[cnt], 2\n\t" \
  "s_cbranch_scc1 1b\n\t" \
  "s_waitcnt vmcnt(0) lgkmcnt(0)\n\t" \
  "v_smfmac_f32_32x32x32_bf16 %[ob], v[244:247], v[216:223], v248\n\t" \
  "s_mov_b32 m0, %[keep]\n\t" \
  "s_nop 11\n\t"
#define ATTN_FAST_CLOBBERS "v192", "v193", "v194", "v195", "v196", "v197", "v198", "v199", "v200", "v201", "v202", "v203", "v204", "v205", "v206", "v207", "v208", "v209", "v210", "v211", "v212", "v213", "v214", "v215", "v216", "v217", "v218", "v219", "v220", "v221", "v222", "v223", "v224", "v225", "v226", "v227", "v228", "v229", "v230", "v231", "v232", "v233", "v234", "v235", "v236", "v237", "v238", "v239", "v240", "v241", "v242", "v243", "v244", "v245", "v246", "v247", "v248"

__device__ __forceinline__ void attn_main(const Params &p, const Ctx &c0, int layer, float (&o)[2][10], bool &fast, v16u (&pre)[8] STAMP_ARGS) {
  const Ctx c = fresh(c0);
  const int head = 4 * c.bx + (c.bj >> 3), qb = c.bj & 7, l = lane_id(), h = l >> 5, tt = qb * 8 + 2 * (c.w & 3);
  STAMP(layer, PH_ATT, 0);
  if (c.w == 0) {
    const int hp = head >> 1, cq = hp / 3, ck = (16 + hp) / 3, cv = (32 + hp) / 3;
    poll(p, c, layer * 5 + PH_QKV, l < 16 ? ck * 16 + l : l < 32 ? cv * 16 + (l & 15) : l < 34 ? cq * 16 + 2 * qb + (l & 1) : -1);
  }
  BAR();
  STAMP(layer, PH_ATT, 1);
  v4u qa, qb2; float kn;
  const unsigned skv = OFF_KV + layer * 4 * MB + head * 131072 + c.w * 2048;
  {
    const unsigned sq = OFF_QF + layer * 2 * MB + (head * 64 + tt) * 1024, sk = OFF_KN + layer * 8192 + head * 256;
#pragma unroll
    for (int i = 0; i < 2; ++i) {
      glds<true>(c.rws, l * 16, skv + i * 16384, c.lds0 + (8 * i + c.w) * KVP);
      glds<true>(c.rws, l * 16, skv + i * 16384 + 1024, c.lds0 + (8 * i + c.w) * KVP + 1024);
      if (i == 0)
        asm volatile("buffer_load_dwordx4 %0, %3, %5, %6 offen sc1\n\tbuffer_load_dwordx4 %1, %3, %5, %6 offen offset:1024 sc1\n\t"
                     "buffer_load_dword %2, %4, %5, %7 offen sc1"
                     : "=&v"(qa), "=&v"(qb2), "=&v"(kn) : "v"(l * 16), "v"(l * 4), "s"(c.rws), "s"(sq), "s"(sk) : "memory");
    }
    if (l < 16) { unsigned one = (l & 1) ? 0u : ONES2; OPAQUE_V(one); *(LAS v4u *)(c.lds + (8 * c.w + (l >> 1)) * KVP + 2048 + (l & 1) * 16) = v4u{one, one, one, one}; }
    asm volatile("s_waitcnt vmcnt(2)" : "+v"(qa), "+v"(qb2), "+v"(kn) :: "memory");
  }
  float km = max32(kn);
  km = __builtin_fmaxf(__builtin_bit_cast(float, __builtin_amdgcn_readlane(__builtin_bit_cast(int, km), 31)),
                       __builtin_bit_cast(float, __builtin_amdgcn_readlane(__builtin_bit_cast(int, km), 63)));
  const float qn = halfsum2(dot2(qa.w, qa.w, dot2(qa.z, qa.z, dot2(qa.y, qa.y, dot2(qa.x, qa.x, 0.f)))),
                            dot2(qb2.w, qb2.w, dot2(qb2.z, qb2.z, dot2(qb2.y, qb2.y, dot2(qb2.x, qb2.x, 0.f)))));
  const unsigned kb0 = c.lds0 + (c.w >> 2) * KVP;
  unsigned ka = kb0 + l * 16;
  unsigned va0 = kb0 + ((l & 16) ? ((l & 15) ? 2064 : 2048) : 1024 + h * 256 + (l & 15) * 16), va1 = va0 + ((l & 16) ? 0 : 512);
  fast = __all(qn * km <= 4096.f);
  unsigned p0, p1, p2;
  if (c.w < 4) { p0 = OFF_WTO + (2 * (c.bj & 7) * 32 + (c.w >> 1) * 16) * 1024; p1 = p0 + 32768; p2 = p1 + 9 * 1024; }
  else { p0 = OFF_WT1 + up_nt0(c.bj & 15) * NTP + 25 * (c.w - 4) * 1024; p1 = p0 + 16384; p2 = OFF_WT2 + ((2 * (c.bj & 7) + (c.w & 1)) * 86 + ((c.w - 4) >> 1) * 43) * 1024; }
  asm volatile("s_waitcnt vmcnt(0) lgkmcnt(0)\n\ts_barrier" ::: "memory");
  STAMP(layer, PH_ATT, 4);
  if (c.w >= 5 && layer == 0) { const int i = (c.w - 5) * 64 + l; poll(p, c, SLOT_PREP, i < 155 ? i : -1); VMWAIT(0); }
  if (fast) {
    unsigned cnt, keep, so = skv + 32768, m0v = c.lds0 + (16 + c.w) * KVP, wo = OFF_WT2 + (c.bj * 8 + c.w) * 1024;
    const rsrc_t rw = mkrsrc(p.ws, layer == 0 ? WS_BYTES : OFF_WTQ);
    f32x16 oa, ob;
    asm volatile(ATTN_FAST_ASM
                 : [oa] "=&v"(oa), [ob] "=&v"(ob), [cnt] "=&s"(cnt), [keep] "=&s"(keep), [so] "+s"(so), [m0v] "+s"(m0v), [wo] "+s"(wo),
                   [ka] "+v"(ka), [va0] "+v"(va0), [va1] "+v"(va1),
                   "=&{v[64:79]}"(pre[0]), "=&{v[80:95]}"(pre[1]), "=&{v[96:111]}"(pre[2]), "=&{v[112:127]}"(pre[3]),
                   "=&{v[128:143]}"(pre[4]), "=&{v[144:159]}"(pre[5]), "=&{v[160:175]}"(pre[6]), "=&{v[176:191]}"(pre[7])
                 : [qa] "v"(qa), [qb] "v"(qb2), [vo] "v"(l * 16), [rs] "s"(c.rws), [rw] "s"(rw), [dummy] "s"(c.lds0 + L_DUMMY),
                   [p0] "s"(p0), [p1] "s"(p1), [p2] "s"(p2)
                 : "memory", "scc", ATTN_FAST_CLOBBERS);
#pragma unroll
    for (int r = 0; r < 9; ++r) { o[0][r] = oa[r]; o[1][r] = ob[r]; }
    o[0][9] = o[1][9] = 0.f;
  } else {
#pragma unroll
    for (int i = 2; i < 8; ++i) {
      glds<true>(c.rws, l * 16, skv + i * 16384, c.lds0 + (8 * i + c.w) * KVP);
      glds<true>(c.rws, l * 16, skv + i * 16384 + 1024, c.lds0 + (8 * i + c.w) * KVP + 1024);
    }
    asm volatile("s_waitcnt vmcnt(0)\n\ts_barrier\n\ts_barrier" ::: "memory");
#pragma unroll
    for (int j = 0; j < 2; ++j) {
      const v4u q = j ? qb2 : qa;
      f32x16 acc = {};
      float m = -__builtin_inff();
#pragma unroll 1
      for (int t = 0; t < 32; ++t) {
        const v4u kf = *(const LAS v4u *)(uintptr_t)(ka + t * 2 * KVP);
        const v4u v0 = *(const LAS v4u *)(uintptr_t)(va0 + t * 2 * KVP), v1 = *(const LAS v4u *)(uintptr_t)(va1 + t * 2 * KVP);
        f32x16 s = mfma(kf, q, f32x16{});
        float tm = s[0];
#pragma unroll
        for (int r = 1; r < 16; ++r) tm = __builtin_fmaxf(tm, s[r]);
        auto sw = __builtin_amdgcn_permlane32_swap(__builtin_bit_cast(unsigned, tm), __builtin_bit_cast(unsigned, tm), false, false);
        const unsigned sw0 = sw[0], sw1 = sw[1];
        const float mn = __builtin_fmaxf(m, __builtin_fmaxf(__uint_as_float(sw0), __uint_as_float(sw1)));
        const float al = __builtin_amdgcn_exp2f(m - mn);
        m = mn;
        v8u e0, e1;
#pragma unroll
        for (int r = 0; r < 8; ++r) {
          e0[r] = __float_as_uint(__builtin_amdgcn_exp2f(s[r] - mn));
          e1[r] = __float_as_uint(__builtin_amdgcn_exp2f(s[8 + r] - mn));
        }
#pragma unroll
        for (int r = 0; r < 16; ++r) acc[r] *= al;
        acc = __builtin_amdgcn_smfmac_f32_32x32x32_bf16(__builtin_bit_cast(bf16x8, v0), __builtin_bit_cast(bf16x16, e0), acc, 0xdddd, 0, 0);
        acc = __builtin_amdgcn_smfmac_f32_32x32x32_bf16(__builtin_bit_cast(bf16x8, v1), __builtin_bit_cast(bf16x16, e1), acc, 0xdddd, 0, 0);
      }
#pragma unroll
      for (int r = 0; r < 9; ++r) o[j][r] = acc[r];
      o[j][9] = m;
    }
#pragma unroll
    for (int i = 0; i < 32; ++i) {
      const v4u f = ld16(c.rws, l * 16, i < 16 ? p0 + i * 1024 : i < 25 ? p1 + (i - 16) * 1024 : p2 + (i - 25) * 1024);
#pragma unroll
      for (int e = 0; e < 4; ++e) pre[i >> 2][4 * (i & 3) + e] = f[e];
    }
  }
  STAMP(layer, PH_ATT, 2);
}
__device__ __forceinline__ void attn_send(const Ctx &c0, const float (&o)[2][10]) {
  const Ctx c = fresh(c0);
  const int l = lane_id();
  LAS float *xp = (LAS float *)(c.lds + L_APART + (c.w & 3) * 5120);
#pragma unroll
  for (int j = 0; j < 2; ++j) {
    *(LAS f32x4 *)(xp + j * 640 + l * 4) = f32x4{o[j][0], o[j][1], o[j][2], o[j][3]};
    *(LAS f32x4 *)(xp + j * 640 + 256 + l * 4) = f32x4{o[j][4], o[j][5], o[j][6], o[j][7]};
    *(LAS v2u *)(xp + j * 640 + 512 + l * 2) = v2u{__float_as_uint(o[j][8]), __float_as_uint(o[j][9])};
  }
  BAR();
}
__device__ __forceinline__ void attn_finish(const Params &p, const Ctx &c0, int layer, const float (&o)[2][10], bool fast STAMP_ARGS) {
  const Ctx c = fresh(c0);
  const int head = 4 * c.bx + (c.bj >> 3), qb = c.bj & 7, l = lane_id(), tt = qb * 8 + 2 * c.w;
  const LAS float *xp = (const LAS float *)(c.lds + L_APART + c.w * 5120);
  BAR();
#pragma unroll
  for (int j = 0; j < 2; ++j) {
    const f32x4 a0 = *(const LAS f32x4 *)(xp + j * 640 + l * 4), a1 = *(const LAS f32x4 *)(xp + j * 640 + 256 + l * 4);
    const v2u a2 = *(const LAS v2u *)(xp + j * 640 + 512 + l * 2);
    float f0 = 1.f, f1 = 1.f;
    if (!fast) {
      const float m1 = __uint_as_float(a2.y), mn = __builtin_fmaxf(o[j][9], m1);
      f0 = __builtin_amdgcn_exp2f(o[j][9] - mn); f1 = __builtin_amdgcn_exp2f(m1 - mn);
    }
    const float lp = o[j][8] * f0 + __uint_as_float(a2.x) * f1;
    const float inv = __builtin_amdgcn_rcpf(halfsum2(lp, lp));
    f0 *= inv; f1 *= inv;
    st16(v4u{pk2(o[j][0] * f0 + a0[0] * f1, o[j][1] * f0 + a0[1] * f1), pk2(o[j][2] * f0 + a0[2] * f1, o[j][3] * f0 + a0[3] * f1),
             pk2(o[j][4] * f0 + a1[0] * f1, o[j][5] * f0 + a1[1] * f1), pk2(o[j][6] * f0 + a1[2] * f1, o[j][7] * f0 + a1[3] * f1)},
         c.rws, l * 16, OFF_OF + layer * 2 * MB + ((tt + j) * 32 + head) * 1024);
  }
  STAMP(layer, PH_ATT, 5);
  publish(p, c, layer * 5 + PH_ATT, qb * 32 + head);
  STAMP(layer, PH_ATT, 3);
}

__global__ __launch_bounds__(512, 2) void fused_kernel(Params p) {
  extern __shared__ __attribute__((aligned(16))) char smem[];
  Ctx c;
  c.rws = mkrsrc(p.ws, WS_BYTES);
  c.lds = (LAS char *)smem;
  c.lds0 = (unsigned)(uintptr_t)c.lds;
  c.w = __builtin_amdgcn_readfirstlane(threadIdx.x >> 6);
  c.bx = blockIdx.x & 7; c.bj = blockIdx.x >> 3;
  c.tag = 0;
  if (c.w == 0) *cnt_ptr(c, lane_id()) = 0;
  if (c.w >= 4) helpers_start(p, c STAMP_PASS); else ln_gemm<PH_QKV, true>(p, c, 0 STAMP_PASS);
  c.tag = rfl64(c.tag);
#pragma unroll 1
  for (int layer = 0; layer < 2; ++layer) {
    float o[2][10];
    bool fast;
    v16u pre[8];
    attn_main(p, c, layer, o, fast, pre STAMP_PASS);
    if (c.w < 4) {
      attn_finish(p, c, layer, o, fast STAMP_PASS);
      res_gemm<PH_WO>(p, c, layer, pre STAMP_PASS);
      ln_gemm<PH_UP, false>(p, c, layer STAMP_PASS);
      res_gemm<PH_DOWN>(p, c, layer, pre STAMP_PASS);
      if (layer == 0) ln_gemm<PH_QKV, false>(p, c, 1 STAMP_PASS);
    } else {
      attn_send(c, o);
      helpers_chain(p, c, layer, pre STAMP_PASS);
    }
  }
  if (blockIdx.x == 0 && c.w == 0 && lane_id() == 0) __hip_atomic_store((GAS u64 *)(p.ws + OFF_EPOCH), c.tag, __ATOMIC_RELAXED, __HIP_MEMORY_SCOPE_AGENT);
}

extern "C" void kernel_launch(void *const *d_in, const int *in_sizes, int n_in, void *d_out, int out_size, void *d_ws, size_t ws_size,
                              hipStream_t stream) {
  Params p;
  p.x = d_in[0]; p.wqkv = d_in[1]; p.wo = d_in[2]; p.w1 = d_in[4]; p.b1 = d_in[5]; p.w2 = d_in[6];
  p.out = d_out; p.ws = (char *)d_ws;
  (void)hipFuncSetAttribute((const void *)fused_kernel, hipFuncAttributeMaxDynamicSharedMemorySize, LDS_BYTES);
  fused_kernel<<<256, 512, LDS_BYTES, stream>>>(p);
}
```

```cpp
#include <hip/hip_runtime.h>
#include <hip/hip_bf16.h>
#include <stdint.h>
#include <type_traits>

#define STAMPS 0

#define LAS __attribute__((address_space(3)))
#define GAS __attribute__((address_space(1)))
typedef unsigned v4u __attribute__((ext_vector_type(4)));
typedef unsigned v2u __attribute__((ext_vector_type(2)));
typedef float f32x2 __attribute__((ext_vector_type(2)));
typedef float f32x4 __attribute__((ext_vector_type(4)));
typedef float f32x16 __attribute__((ext_vector_type(16)));
typedef __bf16 bf16x2 __attribute__((ext_vector_type(2)));
typedef __bf16 bf16x8 __attribute__((ext_vector_type(8)));
typedef __bf16 bf16x16 __attribute__((ext_vector_type(16)));
typedef short s16x4 __attribute__((ext_vector_type(4)));
typedef unsigned v8u __attribute__((ext_vector_type(8)));
typedef unsigned v16u __attribute__((ext_vector_type(16)));
typedef __amdgpu_buffer_rsrc_t rsrc_t;
typedef unsigned long long u64;

constexpr unsigned MB = 1u << 20;
constexpr unsigned OFF_EPOCH = 0;
constexpr unsigned OFF_FLAGS = 65536;
constexpr unsigned OFF_KN = 32768;
constexpr unsigned OFF_QF = MB;
constexpr unsigned OFF_KV = OFF_QF + 4 * MB;
constexpr unsigned OFF_OF = OFF_KV + 8 * MB;
constexpr unsigned OFF_X1 = OFF_OF + 4 * MB;
constexpr unsigned OFF_X2 = OFF_X1 + 4 * MB;
constexpr unsigned OFF_HF = OFF_X2 + 2 * MB;
constexpr unsigned HF_LAYER = 6 * MB;
constexpr unsigned OFF_ST = OFF_HF + 2 * HF_LAYER;
constexpr unsigned OFF_ST0 = OFF_ST + 3 * 262144;
constexpr unsigned OFF_WT1 = OFF_ST + MB;
constexpr unsigned OFF_WTO = OFF_WT1 + 43 * 33 * 1024, OFF_WT2 = OFF_WTO + 16 * 32 * 1024, OFF_WTQ = OFF_WT2 + 16 * 86 * 1024;
constexpr unsigned WS_BYTES = OFF_WTQ + 48 * 33 * 1024;
constexpr unsigned LDS_BYTES = 163840;
constexpr unsigned NTP = 33 * 1024;
constexpr unsigned L_VSCR = 3 * NTP;
constexpr unsigned L_PREP = L_VSCR + 4 * 4608;
constexpr unsigned L_XCH_WO = L_VSCR, L_XCH_DN = 25 * 4096;
constexpr unsigned L_APART = 136 * 1024;
constexpr unsigned L_DUMMY = 135168;
constexpr unsigned L_CNT = 159744;
constexpr unsigned L_ST0 = L_CNT + 2048;
constexpr unsigned L_JUNK = L_CNT + 1024;
constexpr unsigned KVP = 2080;
constexpr int SC1 = 16;
#define NW_WO 16
#define REWARM 0
#define XPD 32
#define XUP 8
#define CODEWARM 1
#define FSTR 128
#define PSLEEP 1
enum { PH_QKV = 0, PH_ATT = 1, PH_WO = 2, PH_UP = 3, PH_DOWN = 4, SLOT_PREP = 10 };

struct Params {
  const void *x, *wqkv, *wo, *w1, *b1, *w2;
  void *out;
  char *ws;
};

struct Ctx {
  rsrc_t rws;
  LAS char *lds;
  unsigned lds0;
  int w;
  int bx, bj;
  u64 tag;
};

#define SB() __builtin_amdgcn_sched_barrier(0)
#define OPAQUE_V(x) asm volatile("" : "+v"(x))
__device__ __forceinline__ Ctx fresh(const Ctx &c0) { Ctx c = c0; asm volatile("" : "+s"(c.w), "+s"(c.bx), "+s"(c.bj), "+s"(c.lds0), "+s"(c.tag)); return c; }
#define BAR() asm volatile("s_waitcnt lgkmcnt(0)\n\ts_barrier" ::: "memory")
#define VMWAIT(n) asm volatile("s_waitcnt vmcnt(" #n ")" ::: "memory")
__device__ __forceinline__ unsigned rfl(unsigned v) { return (unsigned)__builtin_amdgcn_readfirstlane((int)v); }
__device__ __forceinline__ u64 rfl64(u64 v) { return (u64)rfl((unsigned)v) | ((u64)rfl((unsigned)(v >> 32)) << 32); }
__device__ __forceinline__ rsrc_t mkrsrc(const void *p, unsigned bytes) { return __builtin_amdgcn_make_buffer_rsrc((void *)p, (short)0, (int)bytes, 0x00020000); }
__device__ __forceinline__ int lane_id() { int l; asm volatile("v_mbcnt_lo_u32_b32 %0, -1, 0\n\tv_mbcnt_hi_u32_b32 %0, -1, %0" : "=v"(l)); return l; }
__device__ __forceinline__ f32x16 mfma(v4u a, v4u b, f32x16 c) {
  return __builtin_amdgcn_mfma_f32_32x32x16_bf16(__builtin_bit_cast(bf16x8, a), __builtin_bit_cast(bf16x8, b), c, 0, 0, 0);
}
__device__ __forceinline__ unsigned pk2(float lo, float hi) { bf16x2 v = {(__bf16)lo, (__bf16)hi}; return __builtin_bit_cast(unsigned, v); }
__device__ __forceinline__ float dot2(unsigned a, unsigned b, float c) {
  return __builtin_amdgcn_fdot2_f32_bf16(__builtin_bit_cast(bf16x2, a), __builtin_bit_cast(bf16x2, b), c, false);
}
constexpr unsigned ONES2 = 0x3f803f80u;
__device__ __forceinline__ float bf_lo(unsigned u) { return __builtin_bit_cast(float, u << 16); }
__device__ __forceinline__ float bf_hi(unsigned u) { return __builtin_bit_cast(float, u & 0xffff0000u); }
__device__ __forceinline__ float halfsum2(float a, float b) {
  auto r = __builtin_amdgcn_permlane32_swap(__builtin_bit_cast(unsigned, a), __builtin_bit_cast(unsigned, b), false, false);
  const unsigned r0 = r[0], r1 = r[1];
  return __uint_as_float(r0) + __uint_as_float(r1);
}
template <int CTRL, int RMASK> __device__ __forceinline__ float dppmax(float v) {
  const int o = __builtin_amdgcn_update_dpp(0, __builtin_bit_cast(int, v), CTRL, RMASK, 0xf, true);
  return __builtin_fmaxf(v, __builtin_bit_cast(float, o));
}
template <int CTRL, int RMASK> __device__ __forceinline__ float dppadd(float v) {
  return v + __builtin_bit_cast(float, __builtin_amdgcn_update_dpp(0, __builtin_bit_cast(int, v), CTRL, RMASK, 0xf, true));
}
__device__ __forceinline__ float sum32(float v) {
  v = dppadd<0x111, 0xf>(v); v = dppadd<0x112, 0xf>(v); v = dppadd<0x114, 0xf>(v); v = dppadd<0x118, 0xf>(v);
  return dppadd<0x142, 0xa>(v);
}
__device__ __forceinline__ float max32(float v) {
  v = dppmax<0x111, 0xf>(v); v = dppmax<0x112, 0xf>(v); v = dppmax<0x114, 0xf>(v); v = dppmax<0x118, 0xf>(v);
  return dppmax<0x142, 0xa>(v);
}
template <bool DEV> __device__ __forceinline__ void glds(rsrc_t rs, unsigned voff, unsigned soff, unsigned ldsaddr) {
  unsigned keep;
  if constexpr (DEV)
    asm volatile("s_mov_b32 %0, m0\n\ts_mov_b32 m0, %1\n\ts_nop 2\n\tbuffer_load_dwordx4 %2, %3, %4 offen sc1 lds\n\ts_mov_b32 m0, %0"
                 : "=&s"(keep) : "s"(ldsaddr), "v"(voff), "s"(rs), "s"(soff) : "memory");
  else
    asm volatile("s_mov_b32 %0, m0\n\ts_mov_b32 m0, %1\n\ts_nop 2\n\tbuffer_load_dwordx4 %2, %3, %4 offen lds\n\ts_mov_b32 m0, %0"
                 : "=&s"(keep) : "s"(ldsaddr), "v"(voff), "s"(rs), "s"(soff) : "memory");
}
__device__ __forceinline__ v2u trd(const LAS char *p) {
  return __builtin_bit_cast(v2u, __builtin_amdgcn_ds_read_tr16_b64_v4i16((LAS s16x4 *)p));
}
__device__ __forceinline__ v4u trfrag(const LAS char *p0, const LAS char *p1) { const v2u a = trd(p0), b = trd(p1); return v4u{a.x, a.y, b.x, b.y}; }
__device__ __forceinline__ v4u ld16(rsrc_t rs, unsigned voff, unsigned soff) { return __builtin_amdgcn_raw_buffer_load_b128(rs, voff, soff, SC1); }
__device__ __forceinline__ void st16(v4u v, rsrc_t rs, unsigned voff, unsigned soff) { __builtin_amdgcn_raw_buffer_store_b128(v, rs, voff, soff, SC1); }

#define STAMP(layer, ph, i) do {} while (0)
#define STAMP_ARGS
#define STAMP_PASS

__device__ __forceinline__ GAS u64 *flag_ptr(const Params &p, int slot, int idx) { return (GAS u64 *)(p.ws + OFF_FLAGS + (slot * 256 + idx) * FSTR); }
__device__ __forceinline__ LAS unsigned *cnt_ptr(const Ctx &c, int i) { return (LAS unsigned *)(c.lds + L_CNT) + i; }
__device__ __forceinline__ void publish(const Params &p, const Ctx &c, int slot, int idx) {
  unsigned old, t1;
  asm volatile("v_mov_b32 %0, %2\n\tv_mov_b32 %1, 1\n\ts_waitcnt vmcnt(0)\n\ts_mov_b64 exec, 1\n\tds_add_rtn_u32 %0, %0, %1\n\t"
               "s_mov_b64 exec, -1\n\ts_waitcnt lgkmcnt(0)" : "=&v"(old), "=&v"(t1) : "s"(c.lds0 + L_CNT + 4 * slot) : "memory");
  if (rfl(old) == 3)
    __builtin_amdgcn_raw_buffer_store_b64(v2u{(unsigned)c.tag, (unsigned)(c.tag >> 32)}, c.rws, (unsigned)lane_id() << 26,
                                          OFF_FLAGS + (slot * 256 + idx) * FSTR, SC1);
}
__device__ __forceinline__ void poll(const Params &p, const Ctx &c, int slot, int idx  ) {
  GAS u64 *f = flag_ptr(p, slot, idx);
  bool wait = idx >= 0;
#pragma unroll 1
  for (unsigned spins = 0; spins < (1u << 20); ++spins) {
    if (wait) wait = __hip_atomic_load(f, __ATOMIC_RELAXED, __HIP_MEMORY_SCOPE_AGENT) != c.tag;
    if (!__any(wait)) break;
    __builtin_amdgcn_s_sleep(PSLEEP);
  }
  __builtin_amdgcn_fence(__ATOMIC_ACQUIRE, "wavefront");
}
__device__ __forceinline__ void lds_arrive(const Ctx &c, int i) {
  unsigned t0, t1;
  asm volatile("v_mov_b32 %0, %2\n\tv_mov_b32 %1, 1\n\ts_mov_b64 exec, 1\n\tds_add_u32 %0, %1\n\ts_mov_b64 exec, -1"
               : "=&v"(t0), "=&v"(t1) : "s"(c.lds0 + L_CNT + 4 * i) : "memory");
}
__device__ __forceinline__ void lds_wait(const Ctx &c, int i, unsigned n) {
#pragma unroll 1
  while (rfl(__hip_atomic_load(cnt_ptr(c, i), __ATOMIC_RELAXED, __HIP_MEMORY_SCOPE_WORKGROUP)) < n) __builtin_amdgcn_s_sleep(1);
  asm volatile("" ::: "memory");
}

__device__ __forceinline__ int up_nt0(int cb) { return cb < 11 ? 3 * cb : 33 + 2 * (cb - 11); }
__device__ __forceinline__ v2u aug_quad(float cs, unsigned bias_bits) {
  const unsigned hi = pk2(cs, 0.f) & 0xffffu;
  return v2u{pk2(cs, cs - bf_lo(hi)), hi | (bias_bits << 16)};
}
template <int I> __device__ __forceinline__ v4u frag(const v16u (&pre)[8]) {
  return __builtin_shufflevector(pre[I >> 2], pre[I >> 2], 4 * (I & 3), 4 * (I & 3) + 1, 4 * (I & 3) + 2, 4 * (I & 3) + 3);
}
template <int N, class F> __device__ __forceinline__ void static_for(F f) {
  if constexpr (N > 0) { static_for<N - 1>(f); f(std::integral_constant<int, N - 1>{}); }
}
template <bool FIRST> __device__ __forceinline__ void ln_tile(const Ctx &c, int &rb, int &cb) {
  if (FIRST) { rb = 4 * (c.bx >> 1) + (c.bj >> 3); cb = 8 * (c.bx & 1) + (c.bj & 7); }
  else { rb = 2 * c.bx + (c.bj >> 4); cb = c.bj & 15; }
}

__device__ __forceinline__ void helpers_start(const Params &p, Ctx &c STAMP_ARGS) {
  const int l = lane_id(), hj = c.w - 4, id = blockIdx.x, h = l >> 5;
  int rb, cb;
  ln_tile<true>(c, rb, cb);
  v4u r0, r1; v2u ep;
  asm volatile("s_nop 4\n\tbuffer_load_dwordx4 %0, %3, %4, %5 offen\n\tbuffer_load_dwordx4 %1, %3, %4, %5 offen offset:1024\n\t"
               "buffer_load_dwordx2 %2, off, %6, 0 sc1"
               : "=&v"(r0), "=&v"(r1), "=&v"(ep) : "v"(l * 16), "s"(mkrsrc(p.x, 2048 * 1024)), "s"((id * 8 + 2 * hj) * 1024), "s"(c.rws) : "memory");
  const rsrc_t rq = mkrsrc(p.wqkv, 512 * 3072);
  unsigned voff[3];
#pragma unroll
  for (int i = 0; i < 3; ++i) { const int u = 64 * i + l, row = (u * 43691) >> 19; voff[i] = row * 3072 + (u - 12 * row) * 16; }
#define STG(i) _Pragma("unroll") for (int t = 0; t < 3; ++t) glds<false>(rq, voff[t], (4 * (i) + hj) * 16 * 3072 + cb * 192, c.lds0 + (4 * (i) + hj) * 3072 + t * 1024)
  STG(0); STG(1); STG(2); STG(3);
  asm volatile("s_waitcnt vmcnt(12)" : "+v"(r0), "+v"(r1), "+v"(ep) :: "memory");
  c.tag = rfl64(((u64)ep.y << 32 | ep.x) + 1);
  const float t1 = sum32(halfsum2(dot2(r0.w, ONES2, dot2(r0.z, ONES2, dot2(r0.y, ONES2, dot2(r0.x, ONES2, 0.f)))),
                                  dot2(r1.w, ONES2, dot2(r1.z, ONES2, dot2(r1.y, ONES2, dot2(r1.x, ONES2, 0.f))))));
  const float t2 = sum32(halfsum2(dot2(r0.w, r0.w, dot2(r0.z, r0.z, dot2(r0.y, r0.y, dot2(r0.x, r0.x, 0.f)))),
                                  dot2(r1.w, r1.w, dot2(r1.z, r1.z, dot2(r1.y, r1.y, dot2(r1.x, r1.x, 0.f))))));
  st16(v4u{__float_as_uint(t1), (unsigned)c.tag, __float_as_uint(t2), (unsigned)(c.tag >> 32)}, c.rws, (unsigned)(~l & 31) << 26 | h * 16,
       OFF_ST0 + (id * 8 + 2 * hj) * 16);
  const void *sw; unsigned pitch, rows, nt, kb0 = 0, dst;
  if (id < 48) { sw = p.wqkv; pitch = 3072; rows = 512; nt = id; dst = OFF_WTQ + nt * NTP; }
  else if (id < 91) { sw = p.w1; pitch = 2752; rows = 512; nt = id - 48; dst = OFF_WT1 + nt * NTP; }
  else if (id < 107) { sw = p.wo; pitch = 1024; rows = 512; nt = id - 91; dst = OFF_WTO + nt * 32768; }
  else { sw = p.w2; pitch = 1024; rows = id < 155 ? 1376 : 0; nt = (id - 107) & 15; kb0 = ((id - 107) >> 4) * 32; dst = OFF_WT2 + nt * 86 * 1024; }
  sw = (const void *)rfl64((u64)sw); pitch = rfl(pitch); rows = rfl(rows); nt = rfl(nt); kb0 = rfl(kb0); dst = rfl(dst);
  const v4u ones = {ONES2, ONES2, ONES2, ONES2};
  const LAS char *blk = c.lds + hj * 3072 + (8 * h + ((l & 15) >> 2)) * 192 + ((l >> 4) & 1) * 32 + (l & 3) * 8;
  f32x16 cs[3] = {};
#define CSUM(i) _Pragma("unroll") for (int u = 0; u < 6; ++u) { const LAS char *q = blk + (2 * (i) + u / 3) * 12288 + (u % 3) * 64; cs[u % 3] = mfma(ones, trfrag(q, q + 768), cs[u % 3]); }
  VMWAIT(7); lds_arrive(c, 52);
  STG(4); STG(5);
  CSUM(0);
  VMWAIT(6); lds_arrive(c, 53);
  STG(6); STG(7);
  v4u g;
  asm volatile("buffer_load_dwordx4 %0, %1, %2, %3 offen sc1" : "=&v"(g) : "v"((l & 31) * 16), "s"(c.rws), "s"(OFF_ST0 + (rb * 4 + hj) * 512) : "memory");
  {
    const rsrc_t rw = mkrsrc(sw, rows * pitch);
    const unsigned vo = (l >> 2) * pitch + (l & 3) * 16;
#pragma unroll
    for (int i = 0; i < 8; ++i) glds<false>(rw, vo, (kb0 + 8 * hj + i) * 16 * pitch + nt * 64, c.lds0 + L_PREP + (8 * hj + i) * 1024);
  }
  __builtin_amdgcn_s_setprio(2);
  CSUM(1);
  VMWAIT(15); lds_arrive(c, 54);
  CSUM(2);
  asm volatile("s_waitcnt vmcnt(8)" : "+v"(g) :: "memory");
  lds_arrive(c, 55);
  STAMP(0, PH_QKV, 0);
  CSUM(3);
#undef CSUM
#undef STG
#pragma unroll
  for (int t = 0; t < 3; ++t)
    if (l < 32) *(LAS v2u *)(c.lds + 32 * 3072 + t * 1024 + (l + 32 * (hj >> 1)) * 16 + (hj & 1) * 8) = aug_quad(cs[t][0], 0);
#pragma unroll 1
  for (unsigned spins = 0; spins < (1u << 20); ++spins) {
    if (__all(g.y == (unsigned)c.tag && g.w == (unsigned)(c.tag >> 32))) break;
    __builtin_amdgcn_s_sleep(1);
    g = ld16(c.rws, (l & 31) * 16, OFF_ST0 + (rb * 4 + hj) * 512);
  }
  *(LAS unsigned *)(c.lds + L_ST0 + hj * 256 + l * 4) = h ? g.z : g.x;
  lds_arrive(c, 56);
  __builtin_amdgcn_s_setprio(0);
  STAMP(0, PH_QKV, 1);
  if (id < 155) {
    unsigned bias = 0;
    if (id >= 48 && id < 91 && hj < 2) bias = ((const GAS unsigned short *)p.b1)[nt * 32 + (l & 31)];
    VMWAIT(0);
    const LAS char *sp = c.lds + L_PREP + hj * 8192 + h * 256 + ((l & 15) >> 2) * 64 + ((l >> 4) & 1) * 32 + (l & 3) * 8;
    f32x16 ss = {};
    v4u f[8];
#pragma unroll
    for (int i = 0; i < 8; ++i) { f[i] = trfrag(sp + i * 1024, sp + i * 1024 + 512); ss = mfma(ones, f[i], ss); }
    v2u aq = aug_quad(ss[0], bias);
    OPAQUE_V(aq);
    lds_wait(c, PH_QKV, 4);
#pragma unroll
    for (int i = 0; i < 8; ++i) st16(f[i], c.rws, kb0 + 8 * hj + i < 86 ? l * 16 : 1u << 31, dst + (kb0 + 8 * hj + i) * 1024);
    if (id < 91) __builtin_amdgcn_raw_buffer_store_b64(aq, c.rws, (unsigned)h << 31 | ((l + 32 * (hj >> 1)) * 16 + (hj & 1) * 8), dst + 32768, SC1);
  }
  STAMP(0, PH_QKV, 3);
}

__device__ __forceinline__ void store_stats(const Ctx &c, const unsigned (&o)[8], int l, unsigned st_tt, int g) {
  float s1 = 0.f, s2 = 0.f, s1b = 0.f, s2b = 0.f;
#pragma unroll
  for (int i = 0; i < 8; i += 2) { s1 = dot2(o[i], ONES2, s1); s2 = dot2(o[i], o[i], s2); s1b = dot2(o[i + 1], ONES2, s1b); s2b = dot2(o[i + 1], o[i + 1], s2b); }
  __builtin_amdgcn_raw_buffer_store_b32(__float_as_uint(halfsum2(s1 + s1b, s2 + s2b)), c.rws, l * 4, st_tt + g * 256, SC1);
}

template <int PH, bool FIRST> __device__ __forceinline__ void ln_gemm(const Params &p, Ctx &c0, int layer STAMP_ARGS) {
  Ctx c = fresh(c0);
  int rb, cb;
  ln_tile<FIRST>(c, rb, cb);
  const int nt0 = PH == PH_QKV ? 3 * cb : up_nt0(cb);
  const int l = lane_id(), h = l >> 5, tt = rb * 4 + c.w;
  constexpr int PD = 11;
  v4u a[32];
  float st[16], rstd = 0.f, sp = 0.f, nm = 0.f;
  const unsigned base = (PH == PH_QKV ? OFF_X2 : OFF_X1 + layer * 2 * MB) + tt * 32768;
#define LDA(ks) a[ks] = ld16(c.rws, l * 16 + ((ks) & 3) * 1024, base + ((ks) >> 2) * 4096)
  STAMP(layer, PH, 0);
  [[maybe_unused]] LAS char *sb = c.lds + L_VSCR + c.w * 4608;
  [[maybe_unused]] const unsigned w0 = (l >> 3) * 128 + ((l & 7) ^ (l >> 4)) * 16, r0 = (l & 31) * 128 + (h ^ ((l >> 1) & 7)) * 16;
  [[maybe_unused]] u64 e = 0;
  [[maybe_unused]] const rsrc_t rx = mkrsrc(p.x, 2048 * 1024);
#define LDX(ks) a[ks] = __builtin_amdgcn_raw_buffer_load_b128(rx, (l >> 3) * 1024 + (l & 7) * 16 + ((ks) >> 2) * 128, tt * 32768 + ((ks) & 3) * 8192, 0)
#define XW(ch, i) *(LAS v4u *)(sb + (i) * 1024 + (w0 ^ (((i) & 1) * 64))) = a[4 * (ch) + (i)]
#define XR(ch, i) a[4 * (ch) + (i)] = *(const LAS v4u *)(sb + (r0 ^ ((i) * 32)))
  if constexpr (FIRST) {
#pragma unroll
    for (int ks = 0; ks < XPD; ++ks) LDX(ks);
    e = __hip_atomic_load((GAS u64 *)(p.ws + OFF_EPOCH), __ATOMIC_RELAXED, __HIP_MEMORY_SCOPE_AGENT) + 1;
    SB();
#pragma unroll
    for (int ch = 0; ch < XUP; ++ch) {
#pragma unroll
      for (int i = 0; i < 4; ++i) XW(ch, i);
      asm volatile("" ::: "memory");
#pragma unroll
      for (int i = 0; i < 4; ++i) XR(ch, i);
      asm volatile("" ::: "memory");
    }
    STAMP(layer, PH, 4);
    lds_wait(c, 52, 4);
    STAMP(layer, PH, 1);
  } else {
    if (c.w == 0) poll(p, c, PH == PH_QKV ? PH_DOWN : layer * 5 + PH_WO, l < 16 ? rb * 16 + l : -1);
    STAMP(layer, PH, 4);
    BAR();
    STAMP(layer, PH, 1);
#pragma unroll
    for (int ks = 0; ks < PD; ++ks) LDA(ks);
  }
  SB();
  const LAS char *b0 = c.lds + (FIRST ? (8 * h + ((l & 15) >> 2)) * 192 + ((l >> 4) & 1) * 32 + (l & 3) * 8 : l * 16);
  const LAS char *b2 = b0 + (FIRST ? 20 * 3072 : 2 * NTP - 4096);
  OPAQUE_V(b2);
  f32x16 acc[3] = {};
  v4u wf[3][3], aug = {};
  auto WF = [&](int ks, int nt) -> v4u {
    if (FIRST) {
      if (ks == 32) return *(const LAS v4u *)(c.lds + 32 * 3072 + nt * 1024 + l * 16);
      const LAS char *q = (ks >= 20 ? b2 + (ks - 20) * 3072 : b0 + ks * 3072) + nt * 64;
      return trfrag(q, q + 768);
    }
    return *(const LAS v4u *)(nt == 2 ? b2 + (ks + 4) * 1024 : nt == 1 && ks > 30 ? b2 + (ks - 29) * 1024 : b0 + nt * NTP + ks * 1024);
  };
#pragma unroll
  for (int ks = 0; ks < 2; ++ks)
#pragma unroll
    for (int nt = 0; nt < 3; ++nt) wf[ks][nt] = WF(ks, nt);
  SB();
#pragma unroll
  for (int ks = 0; ks < 33; ++ks) {
#pragma unroll
    for (int nt = 0; nt < 3; ++nt) {
      if (FIRST && nt == 0 && (ks == 6 || ks == 14 || ks == 22)) { lds_wait(c, 53 + (ks >> 3), 4); SB(); }
      if (FIRST && nt == 0 && ks == 27) { lds_wait(c, 56, 4); SB(); }
      if (FIRST && nt == 0 && (ks == 7 || ks == 15 || ks == 23 || ks == 27)) { STAMP(2, 0, ks == 27 ? 3 : ks >> 3); SB(); }
      if (ks + 2 < 33) wf[(ks + 2) % 3][nt] = WF(ks + 2, nt);
      acc[nt] = mfma(wf[ks % 3][nt], ks < 32 ? a[ks] : aug, acc[nt]);
      if constexpr (FIRST) {
        const int ch = (ks >> 2) + XUP, u = 3 * (ks & 3) + nt;
        if (ch < 8 && u >= 2 && u < 6) { XW(ch, u - 2); asm volatile("" ::: "memory"); }
        if (ch < 8 && u >= 6 && u < 10) { XR(ch, u - 6); asm volatile("" ::: "memory"); }
        if (3 * ks + nt + XPD < 32) LDX(3 * ks + nt + XPD);
        if (ks == 27 && nt == 0) sp = *(const LAS float *)(c.lds + L_ST0 + c.w * 256 + l * 4);
      } else {
        if (nt == 1 && ks + PD < 32) LDA(ks + PD);
        if (PH == PH_UP && nt == 2 && ks + PD == 31) lds_arrive(c, 40 + layer * 5 + PH);
        if (nt == 2 && ks >= 8 && ks < 16) {
#pragma unroll
          for (int g = 2 * (ks - 8); g < 2 * (ks - 7); ++g)
            st[g] = __uint_as_float(__builtin_amdgcn_raw_buffer_load_b32(c.rws, l * 4 + g * 256, OFF_ST + (PH == PH_UP ? layer : 2) * 262144 + tt * 4096, SC1));
        }
        if (ks == 27) { if (nt == 0) sp = (st[0] + st[1]) + (st[2] + st[3]); else sp += (st[4 * nt] + st[4 * nt + 1]) + (st[4 * nt + 2] + st[4 * nt + 3]); OPAQUE_V(sp); }
        if (ks == 28 && nt == 0) { sp += (st[12] + st[13]) + (st[14] + st[15]); OPAQUE_V(sp); }
      }
      if (ks == 28 && nt == 1) {
        auto sw = __builtin_amdgcn_permlane32_swap(__float_as_uint(sp), __float_as_uint(sp), false, false);
        const unsigned u1 = sw[0], u2 = sw[1];
        nm = __uint_as_float(u1) * (-1.f / 512.f); sp = __uint_as_float(u2) * (1.f / 512.f);
        OPAQUE_V(nm); OPAQUE_V(sp);
      }
      if (ks == 28 && nt == 2) { sp = __builtin_fmaxf(sp - nm * nm, 0.f) + 1e-5f; rstd = __builtin_amdgcn_rsqf(sp); OPAQUE_V(sp); OPAQUE_V(rstd); }
      if (ks == 29 && nt == 0) { sp *= rstd; aug.x = pk2(nm, nm); aug.y = pk2(0.f, sp); OPAQUE_V(sp); OPAQUE_V(aug); }
      if (ks == 29 && nt == 1) { aug.w = pk2(nm - bf_lo(aug.x), sp - bf_hi(aug.y)); aug.y |= aug.w & 0xffffu; aug.z = aug.x; OPAQUE_V(aug); }
      SB();
    }
  }
#undef LDA
#undef XW
#undef XR
#undef LDX
  if constexpr (FIRST) c0.tag = c.tag = rfl64(e);
  if (PH == PH_UP) lds_arrive(c, 16 + layer * 5 + PH);
  STAMP(layer, PH, 2);
#pragma unroll
  for (int nt = 0; nt < 3; ++nt) {
    const int NT = nt0 + nt;
    unsigned o[8];
    if constexpr (PH == PH_QKV) {
      const int type = NT >> 4, head = 2 * (NT & 15);
      const float rs = type == 0 ? rstd * (0.25f * 1.4426950408889634f) : rstd;
#pragma unroll
      for (int i = 0; i < 8; ++i) { const f32x2 v = f32x2{acc[nt][2 * i], acc[nt][2 * i + 1]} * rs; o[i] = pk2(v.x, v.y); }
      const v4u wa = {o[0], o[1], o[2], o[3]}, wb = {o[4], o[5], o[6], o[7]};
      if (type == 0) {
        const unsigned so = OFF_QF + layer * 2 * MB + (head * 64 + tt) * 1024;
        st16(wa, c.rws, l * 16, so); st16(wb, c.rws, l * 16, so + 65536);
      } else if (type == 1) {
        const unsigned so = OFF_KV + layer * 4 * MB + (head * 64 + tt) * 2048;
        st16(wa, c.rws, l * 16, so); st16(wb, c.rws, l * 16, so + 131072);
        const float na = dot2(wa.w, wa.w, dot2(wa.z, wa.z, dot2(wa.y, wa.y, dot2(wa.x, wa.x, 0.f))));
        const float nb = dot2(wb.w, wb.w, dot2(wb.z, wb.z, dot2(wb.y, wb.y, dot2(wb.x, wb.x, 0.f))));
        const float m = max32(halfsum2(na, nb));
        if ((l & 31) == 31)
          __builtin_amdgcn_raw_buffer_store_b32(__float_as_uint(m), c.rws, h * 256, OFF_KN + layer * 8192 + (head * 64 + tt) * 4, SC1);
      } else {
        LAS char *sb = c.lds + L_VSCR + c.w * 4608 + (nt & 1) * 2304;
        *(LAS v4u *)(sb + (l & 31) * 32 + h * 16) = wa;
        *(LAS v4u *)(sb + 1152 + (l & 31) * 32 + h * 16) = wb;
        asm volatile("" ::: "memory");
        const LAS char *rp = sb + ((l >> 4) & 1) * 1152 + (8 * h + ((l & 15) >> 2)) * 32 + (l & 3) * 8;
        const v4u f0 = trfrag(rp, rp + 128), f1 = trfrag(rp + 512, rp + 640);
        const int i = l & 15, d = (i & 3) | ((i & 4) << 1) | ((i & 8) >> 1);
        const unsigned vo = ((l >> 4) & 1) * 131072 + h * 256 + d * 16, so = OFF_KV + layer * 4 * MB + (head * 64 + tt) * 2048 + 1024;
        st16(f0, c.rws, vo, so); st16(f1, c.rws, vo, so + 512);
      }
    } else {
      const float rl = rstd * 1.4426950408889634f;
      const f32x2 one = {1.f, 1.f}, rs2 = {rstd, rstd}, rm2 = {-2.f * rstd, -2.f * rstd};
#pragma unroll
      for (int i = 0; i < 8; ++i) {
        const f32x2 x = {acc[nt][2 * i], acc[nt][2 * i + 1]}, hl = x * rl;
        const f32x2 t = f32x2{__builtin_amdgcn_exp2f(hl.x), __builtin_amdgcn_exp2f(hl.y)} + one;
        const f32x2 d = __builtin_elementwise_fma(t, t, one);
        const f32x2 v = x * __builtin_elementwise_fma(f32x2{__builtin_amdgcn_rcpf(d.x), __builtin_amdgcn_rcpf(d.y)}, rm2, rs2);
        o[i] = pk2(v.x, v.y);
      }
      if (nt < 2 || cb < 11) {
        const unsigned so = OFF_HF + layer * HF_LAYER + (tt * 86 + 2 * NT) * 1024;
        st16(v4u{o[0], o[1], o[2], o[3]}, c.rws, l * 16, so); st16(v4u{o[4], o[5], o[6], o[7]}, c.rws, l * 16, so + 1024);
      }
    }
  }
  STAMP(layer, PH, 5);
  publish(p, c, layer * 5 + PH, PH == PH_QKV ? cb * 16 + rb : rb * 16 + cb);
  if (REWARM && PH == PH_UP) {
#pragma unroll
    for (int i = 0; i < 6; ++i)
      if (i < 4 || cb < 11) glds<true>(c.rws, l * 16, OFF_HF + layer * HF_LAYER + (tt * 86 + 2 * nt0 + i) * 1024, c.lds0 + L_JUNK);
  }
  STAMP(layer, PH, 3);
}

template <int PH> __device__ __forceinline__ void res_gemm(const Params &p, const Ctx &c0, int layer, const v16u (&pre)[8] STAMP_ARGS) {
  const Ctx c = fresh(c0);
  constexpr int KB = PH == PH_WO ? 32 : 86, KH = KB / 2;
  const int rb = 4 * c.bx + (c.bj >> 3), cb = c.bj & 7;
  const int l = lane_id(), h = l >> 5, kh = c.w >> 1, tt = rb * 2 + (c.w & 1);
  const unsigned fm = (tt * 32 + cb * 4 + kh * 2) * 1024;
  STAMP(layer, PH, 0);
  v4u res[2];
  if (PH == PH_WO && layer == 0) {
    const rsrc_t rx = mkrsrc((const void *)rfl64((u64)p.x), 2048 * 1024);
    const unsigned voff = (l & 31) * 1024 + h * 8, so = tt * 32768 + cb * 128 + kh * 64;
    const v2u r0 = __builtin_amdgcn_raw_buffer_load_b64(rx, voff, so, 0), r1 = __builtin_amdgcn_raw_buffer_load_b64(rx, voff + 16, so, 0);
    const v2u r2 = __builtin_amdgcn_raw_buffer_load_b64(rx, voff + 32, so, 0), r3 = __builtin_amdgcn_raw_buffer_load_b64(rx, voff + 48, so, 0);
    res[0] = v4u{r0.x, r0.y, r1.x, r1.y}; res[1] = v4u{r2.x, r2.y, r3.x, r3.y};
  } else {
    const unsigned so = (PH == PH_WO ? OFF_X2 : OFF_X1 + layer * 2 * MB) + fm;
    res[0] = ld16(c.rws, l * 16, so); res[1] = ld16(c.rws, l * 16 + 1024, so);
  }
  if (c.w == 0) { if (PH == PH_WO) poll(p, c, layer * 5 + PH_ATT, l < 32 ? (rb >> 2) * 32 + l : -1); else poll(p, c, layer * 5 + PH_UP, l < 16 ? (rb >> 1) * 16 + l : -1); }
  STAMP(layer, PH, 4);
  BAR();
  STAMP(layer, PH, 1);
  v4u a[KH];
  const unsigned base = (PH == PH_WO ? OFF_OF + layer * 2 * MB : OFF_HF + layer * HF_LAYER) + (tt * KB + kh * KH) * 1024;
#define LDA(ks) a[ks] = ld16(c.rws, l * 16 + ((ks) & 3) * 1024, base + ((ks) >> 2) * 4096)
  constexpr int PD = PH == PH_WO ? 16 : 13;
#pragma unroll
  for (int ks = 0; ks < PD; ++ks) LDA(ks);
  if (PH == PH_WO) lds_arrive(c, 40 + layer * 5 + PH);
  SB();
  f32x16 acc[2] = {};
  if constexpr (PH == PH_WO) {
    static_for<16>([&](auto I) {
      constexpr int ks = I.value;
      acc[0] = mfma(frag<ks>(pre), a[ks], acc[0]);
      if constexpr (PD + 2 * ks < KH) LDA(PD + 2 * ks);
      SB();
      acc[1] = mfma(frag<16 + ks>(pre), a[ks], acc[1]);
      if constexpr (PD + 2 * ks + 1 < KH) LDA(PD + 2 * ks + 1);
      SB();
    });
  } else {
    const LAS char *b0 = c.lds + kh * 2048 + l * 16, *b1 = b0 + 65536, *b2 = b0 + 131072;
    OPAQUE_V(b1); OPAQUE_V(b2);
    v4u wf[4][2];
#define SLOT(ks) ((ks) < 39 ? (ks) : (ks) - 39)
#define WF(ks, nt) (*(const LAS v4u *)((SLOT(ks) >= 32 ? b2 : SLOT(ks) >= 16 ? b1 : b0) + (SLOT(ks) & 15) * 4096 + (nt) * 1024))
#pragma unroll
    for (int ks = 0; ks < 3; ++ks)
#pragma unroll
      for (int nt = 0; nt < 2; ++nt) wf[ks][nt] = WF(ks, nt);
    SB();
#pragma unroll
    for (int ks = 0; ks < KH; ++ks) {
      if (ks == 4 || ks == 36) { asm volatile("s_barrier" ::: "memory"); SB(); }
#pragma unroll
      for (int nt = 0; nt < 2; ++nt) {
        if (ks + 3 < KH) wf[(ks + 3) % 4][nt] = WF(ks + 3, nt);
        acc[nt] = mfma(wf[ks % 4][nt], a[ks], acc[nt]);
        if (nt == 1 && PD + ks < KH) LDA(PD + ks);
        if (nt == 1 && PD + ks == KH - 1) lds_arrive(c, 40 + layer * 5 + PH);
        SB();
      }
    }
#undef WF
#undef SLOT
    lds_arrive(c, 16 + layer * 5 + PH);
  }
#undef LDA
  constexpr unsigned XCH = PH == PH_WO ? L_XCH_WO : L_XCH_DN;
  const unsigned tok = layer * 2 + (PH == PH_WO ? 1 : 2);
  {
    const f32x16 s = kh ? acc[0] : acc[1];
    LAS f32x4 *xw = (LAS f32x4 *)(c.lds + XCH + c.w * 4096 + l * 16);
#pragma unroll
    for (int g = 0; g < 4; ++g) xw[g * 64] = f32x4{s[4 * g], s[4 * g + 1], s[4 * g + 2], s[4 * g + 3]};
  }
  lds_arrive(c, 32 + c.w);
  const f32x16 m = kh ? acc[1] : acc[0];
  f32x2 own[8];
#pragma unroll
  for (int i = 0; i < 8; ++i) { const unsigned r = res[i >> 2][i & 3]; own[i] = f32x2{m[2 * i], m[2 * i + 1]} + f32x2{bf_lo(r), bf_hi(r)}; OPAQUE_V(own[i]); }
  lds_wait(c, 32 + (c.w ^ 2), tok);
  STAMP(layer, PH, 2);
  const LAS f32x4 *xr = (const LAS f32x4 *)(c.lds + XCH + (c.w ^ 2) * 4096 + l * 16);
  unsigned o[8];
#pragma unroll
  for (int g = 0; g < 4; ++g) {
    const f32x4 q = xr[g * 64];
    const f32x2 u = own[2 * g] + f32x2{q[0], q[1]}, v = own[2 * g + 1] + f32x2{q[2], q[3]};
    o[2 * g] = pk2(u.x, u.y); o[2 * g + 1] = pk2(v.x, v.y);
  }
  if (PH == PH_DOWN && layer == 1) {
    const rsrc_t ro = mkrsrc((const void *)rfl64((u64)p.out), 2048 * 1024);
#pragma unroll
    for (int t = 0; t < 2; ++t) {
      auto s0 = __builtin_amdgcn_permlane32_swap(o[4 * t], o[4 * t + 2], false, false);
      auto s1 = __builtin_amdgcn_permlane32_swap(o[4 * t + 1], o[4 * t + 3], false, false);
      __builtin_amdgcn_raw_buffer_store_b128(v4u{s0[0], s1[0], s0[1], s1[1]}, ro, (l & 31) * 1024 + h * 16 + t * 32, tt * 32768 + cb * 128 + kh * 64, 0);
    }
  } else {
    const unsigned so = (PH == PH_WO ? OFF_X1 + layer * 2 * MB : OFF_X2) + fm;
    st16(v4u{o[0], o[1], o[2], o[3]}, c.rws, l * 16, so); st16(v4u{o[4], o[5], o[6], o[7]}, c.rws, l * 16 + 1024, so);
    store_stats(c, o, l, OFF_ST + (PH == PH_WO ? layer : 2) * 262144 + tt * 4096, 2 * cb + kh);
    STAMP(layer, PH, 5);
    publish(p, c, layer * 5 + PH, rb * 8 + cb);
    if (REWARM) { glds<true>(c.rws, l * 16, so, c.lds0 + L_JUNK); glds<true>(c.rws, l * 16, so + 1024, c.lds0 + L_JUNK); }
  }
  STAMP(layer, PH, 3);
}

__device__ __forceinline__ void helpers_chain(const Params &p, const Ctx &c0, int layer, const v16u (&pre)[8] STAMP_ARGS) {
  const Ctx c = fresh(c0);
  const int l = lane_id(), hj = c.w - 4;
  LAS char *lw = c.lds + l * 16;
  static_for<25>([&](auto I) { *(LAS v4u *)(lw + (25 * hj + I.value) * 1024) = frag<I.value>(pre); });
  const unsigned src = OFF_WT2 + ((2 * (c.bj & 7) + (hj & 1)) * 86 + (hj >> 1) * 43) * 1024;
  v4u w2[22];
#define LDW(i) w2[i] = ld16(c.rws, l * 16, src + ((i) < 18 ? 7 + (i) : 21 + (i)) * 1024)
  STAMP(layer, PH_WO, 0);
  BAR();
  lds_wait(c, 40 + layer * 5 + PH_WO, 4);
#pragma unroll
  for (int i = 0; i < NW_WO; ++i) LDW(i);
  STAMP(layer, PH_WO, 3);
  BAR();
  lds_wait(c, 40 + layer * 5 + PH_UP, 4);
#pragma unroll
  for (int i = NW_WO; i < 22; ++i) LDW(i);
#pragma unroll
  for (int ks = 25; ks < 39; ++ks) glds<true>(c.rws, l * 16, src + ks * 1024, c.lds0 + ks * 4096 + hj * 1024);
#undef LDW
  STAMP(layer, PH_UP, 3);
  lds_wait(c, 16 + layer * 5 + PH_UP, 4);
  static_for<7>([&](auto I) { *(LAS v4u *)(lw + I.value * 4096 + hj * 1024) = frag<25 + I.value>(pre); });
#pragma unroll
  for (int i = 0; i < 18; ++i) *(LAS v4u *)(lw + (7 + i) * 4096 + hj * 1024) = w2[i];
  VMWAIT(0);
  STAMP(layer, PH_DOWN, 1);
  BAR();
  BAR();
#pragma unroll
  for (int i = 0; i < 4; ++i) *(LAS v4u *)(lw + i * 4096 + hj * 1024) = w2[18 + i];
  if (layer == 0) {
    v4u wq[25];
    const unsigned sq = OFF_WTQ + 3 * (c.bj & 15) * NTP + 25 * hj * 1024;
    asm volatile("s_waitcnt lgkmcnt(0)" ::: "memory");
    lds_wait(c, 40 + PH_DOWN, 4);
#pragma unroll
    for (int i = 0; i < 6; ++i) wq[i] = ld16(c.rws, l * 16, sq + i * 1024);
    BAR();
#pragma unroll
    for (int i = 6; i < 25; ++i) wq[i] = ld16(c.rws, l * 16, sq + i * 1024);
    STAMP(1, PH_QKV, 0);
    lds_wait(c, 16 + PH_DOWN, 4);
#pragma unroll
    for (int i = 0; i < 25; ++i) *(LAS v4u *)(lw + (25 * hj + i) * 1024) = wq[i];
    STAMP(1, PH_QKV, 1);
    BAR();
  } else BAR();
}

#define ATTN_FAST_ASM \
  "s_cmp_eq_u32 %[cnt], 0\n\t" \
  "s_cbranch_scc1 8f\n\t" \
  "s_mov_b32 %[keep], m0\n\t" \
  "v_mov_b32 v248, 0xdddd\n\t" \
  "ds_read_b128 v[224:227], %[ka]\n\t" \
  "ds_read_b128 v[232:235], %[va0]\n\t" \
  "ds_read_b128 v[240:243], %[va1]\n\t" \
  "ds_read_b128 v[228:231], %[ka] offset:4160\n\t" \
  "v_mov_b32 v244, 0\n\t" \
  "v_mov_b32 v245, 0\n\t" \
  "v_mov_b32 v246, 0\n\t" \
  "v_mov_b32 v247, 0\n\t" \
  "s_mov_b32 %[cnt], 0\n\t" \
  "s_nop 0\n\t" \
  "v_mfma_f32_32x32x16_bf16 %[oa], v[244:247], v[244:247], 0\n\t" \
  "v_mfma_f32_32x32x16_bf16 %[ob], v[244:247], v[244:247], 0\n\t" \
  "v_mfma_f32_32x32x16_bf16 v[208:223], v[244:247], v[244:247], 0\n\t" \
  "s_waitcnt lgkmcnt(3)\n\t" \
  "v_mfma_f32_32x32x16_bf16 v[192:207], v[224:227], %[qa], 0\n\t" \
  "s_nop 11\n\t" \
  "s_waitcnt lgkmcnt(0)\n\t" \
  "v_exp_f32 v192, v192\n\t" \
  "v_exp_f32 v193, v193\n\t" \
  "v_smfmac_f32_32x32x32_bf16 %[ob], v[244:247], v[216:223], v248\n\t" \
  "v_exp_f32 v194, v194\n\t" \
  "v_exp_f32 v195, v195\n\t" \
  "v_exp_f32 v196, v196\n\t" \
  "v_mfma_f32_32x32x16_bf16 v[208:223], v[224:227], %[qb], 0\n\t" \
  "ds_read_b128 v[224:227], %[ka] offset:8320\n\t" \
  "ds_read_b128 v[236:239], %[va0] offset:4160\n\t" \
  "ds_read_b128 v[244:247], %[va1] offset:4160\n\t" \
  "v_exp_f32 v197, v197\n\t" \
  "v_exp_f32 v198, v198\n\t" \
  "v_exp_f32 v199, v199\n\t" \
  "s_mov_b32 m0, %[m0v]\n\t" \
  "s_add_u32 %[m0v], %[m0v], 16640\n\t" \
  "buffer_load_dwordx4 %[vo], %[rs], %[so] offen sc1 lds\n\t" \
  "v_exp_f32 v200, v200\n\t" \
  "v_exp_f32 v201, v201\n\t" \
  "v_exp_f32 v202, v202\n\t" \
  "v_smfmac_f32_32x32x32_bf16 %[oa], v[232:235], v[192:199], v248\n\t" \
  "v_exp_f32 v203, v203\n\t" \
  "v_exp_f32 v204, v204\n\t" \
  "v_exp_f32 v205, v205\n\t" \
  "buffer_load_dwordx4 %[vo], %[rs], %[so] offen offset:1024 sc1 lds\n\t" \
  "s_add_u32 %[so], %[so], 16384\n\t" \
  "v_exp_f32 v206, v206\n\t" \
  "v_exp_f32 v207, v207\n\t" \
  "v_exp_f32 v208, v208\n\t" \
  "v_exp_f32 v209, v209\n\t" \
  "v_smfmac_f32_32x32x32_bf16 %[oa], v[240:243], v[200:207], v248\n\t" \
  "v_exp_f32 v210, v210\n\t" \
  "v_exp_f32 v211, v211\n\t" \
  "v_exp_f32 v212, v212\n\t" \
  "v_mfma_f32_32x32x16_bf16 v[192:207], v[228:231], %[qa], 0\n\t" \
  "v_exp_f32 v213, v213\n\t" \
  "v_exp_f32 v214, v214\n\t" \
  "v_exp_f32 v215, v215\n\t" \
  "s_mov_b32 m0, %[m0v]\n\t" \
  "s_add_u32 %[m0v], %[m0v], 16640\n\t" \
  "buffer_load_dwordx4 %[vo], %[rs], %[so] offen sc1 lds\n\t" \
  "v_exp_f32 v216, v216\n\t" \
  "v_exp_f32 v217, v217\n\t" \
  "v_exp_f32 v218, v218\n\t" \
  "v_smfmac_f32_32x32x32_bf16 %[ob], v[232:235], v[208:215], v248\n\t" \
  "v_exp_f32 v219, v219\n\t" \
  "v_exp_f32 v220, v220\n\t" \
  "v_exp_f32 v221, v221\n\t" \
  "buffer_load_dwordx4 %[vo], %[rs], %[so] offen offset:1024 sc1 lds\n\t" \
  "s_add_u32 %[so], %[so], 16384\n\t" \
  "v_exp_f32 v222, v222\n\t" \
  "v_exp_f32 v223, v223\n\t" \
  "s_waitcnt lgkmcnt(0)\n\t" \
  "v_exp_f32 v192, v192\n\t" \
  "v_exp_f32 v193, v193\n\t" \
  "v_smfmac_f32_32x32x32_bf16 %[ob], v[240:243], v[216:223], v248\n\t" \
  "v_exp_f32 v194, v194\n\t" \
  "v_exp_f32 v195, v195\n\t" \
  "v_exp_f32 v196, v196\n\t" \
  "v_mfma_f32_32x32x16_bf16 v[208:223], v[228:231], %[qb], 0\n\t" \
  "ds_read_b128 v[228:231], %[ka] offset:12480\n\t" \
  "ds_read_b128 v[232:235], %[va0] offset:8320\n\t" \
  "ds_read_b128 v[240:243], %[va1] offset:8320\n\t" \
  "v_exp_f32 v197, v197\n\t" \
  "v_exp_f32 v198, v198\n\t" \
  "v_exp_f32 v199, v199\n\t" \
  "s_mov_b32 m0, %[m0v]\n\t" \
  "s_add_u32 %[m0v], %[m0v], 16640\n\t" \
  "buffer_load_dwordx4 %[vo], %[rs], %[so] offen sc1 lds\n\t" \
  "v_exp_f32 v200, v200\n\t" \
  "v_exp_f32 v201, v201\n\t" \
  "v_exp_f32 v202, v202\n\t" \
  "v_smfmac_f32_32x32x32_bf16 %[oa], v[236:239], v[192:199], v248\n\t" \
  "v_exp_f32 v203, v203\n\t" \
  "v_exp_f32 v204, v204\n\t" \
  "v_exp_f32 v205, v205\n\t" \
  "buffer_load_dwordx4 %[vo], %[rs], %[so] offen offset:1024 sc1 lds\n\t" \
  "s_add_u32 %[so], %[so], 16384\n\t" \
  "v_exp_f32 v206, v206\n\t" \
  "v_exp_f32 v207, v207\n\t" \
  "v_exp_f32 v208, v208\n\t" \
  "v_exp_f32 v209, v209\n\t" \
  "v_smfmac_f32_32x32x32_bf16 %[oa], v[244:247], v[200:207], v248\n\t" \
  "v_exp_f32 v210, v210\n\t" \
  "v_exp_f32 v211, v211\n\t" \
  "v_exp_f32 v212, v212\n\t" \
  "v_mfma_f32_32x32x16_bf16 v[192:207], v[224:227], %[qa], 0\n\t" \
  "v_exp_f32 v213, v213\n\t" \
  "v_exp_f32 v214, v214\n\t" \
  "v_exp_f32 v215, v215\n\t" \
  "s_mov_b32 m0, %[m0v]\n\t" \
  "s_add_u32 %[m0v], %[m0v], 16640\n\t" \
  "buffer_load_dwordx4 %[vo], %[rs], %[so] offen sc1 lds\n\t" \
  "v_exp_f32 v216, v216\n\t" \
  "v_exp_f32 v217, v217\n\t" \
  "v_exp_f32 v218, v218\n\t" \
  "v_smfmac_f32_32x32x32_bf16 %[ob], v[236:239], v[208:215], v248\n\t" \
  "v_exp_f32 v219, v219\n\t" \
  "v_exp_f32 v220, v220\n\t" \
  "v_exp_f32 v221, v221\n\t" \
  "buffer_load_dwordx4 %[vo], %[rs], %[so] offen offset:1024 sc1 lds\n\t" \
  "s_add_u32 %[so], %[so], 16384\n\t" \
  "v_exp_f32 v222, v222\n\t" \
  "v_exp_f32 v223, v223\n\t" \
  "s_waitcnt lgkmcnt(0)\n\t" \
  "v_exp_f32 v192, v192\n\t" \
  "v_exp_f32 v193, v193\n\t" \
  "v_smfmac_f32_32x32x32_bf16 %[ob], v[244:247], v[216:223], v248\n\t" \
  "v_exp_f32 v194, v194\n\t" \
  "v_exp_f32 v195, v195\n\t" \
  "v_exp_f32 v196, v196\n\t" \
  "v_mfma_f32_32x32x16_bf16 v[208:223], v[224:227], %[qb], 0\n\t" \
  "ds_read_b128 v[224:227], %[ka] offset:16640\n\t" \
  "ds_read_b128 v[236:239], %[va0] offset:12480\n\t" \
  "ds_read_b128 v[244:247], %[va1] offset:12480\n\t" \
  "v_exp_f32 v197, v197\n\t" \
  "v_exp_f32 v198, v198\n\t" \
  "v_exp_f32 v199, v199\n\t" \
  "s_mov_b32 m0, %[m0v]\n\t" \
  "s_add_u32 %[m0v], %[m0v], 16640\n\t" \
  "buffer_load_dwordx4 %[vo], %[rs], %[so] offen sc1 lds\n\t" \
  "v_exp_f32 v200, v200\n\t" \
  "v_exp_f32 v201, v201\n\t" \
  "v_exp_f32 v202, v202\n\t" \
  "v_smfmac_f32_32x32x32_bf16 %[oa], v[232:235], v[192:199], v248\n\t" \
  "v_exp_f32 v203, v203\n\t" \
  "v_exp_f32 v204, v204\n\t" \
  "v_exp_f32 v205, v205\n\t" \
  "buffer_load_dwordx4 %[vo], %[rs], %[so] offen offset:1024 sc1 lds\n\t" \
  "s_add_u32 %[so], %[so], 16384\n\t" \
  "v_exp_f32 v206, v206\n\t" \
  "v_exp_f32 v207, v207\n\t" \
  "v_exp_f32 v208, v208\n\t" \
  "v_exp_f32 v209, v209\n\t" \
  "v_smfmac_f32_32x32x32_bf16 %[oa], v[240:243], v[200:207], v248\n\t" \
  "v_exp_f32 v210, v210\n\t" \
  "v_exp_f32 v211, v211\n\t" \
  "v_exp_f32 v212, v212\n\t" \
  "v_mfma_f32_32x32x16_bf16 v[192:207], v[228:231], %[qa], 0\n\t" \
  "v_exp_f32 v213, v213\n\t" \
  "v_exp_f32 v214, v214\n\t" \
  "v_exp_f32 v215, v215\n\t" \
  "s_mov_b32 m0, %[m0v]\n\t" \
  "s_add_u32 %[m0v], %[m0v], 16640\n\t" \
  "buffer_load_dwordx4 %[vo], %[rs], %[so] offen sc1 lds\n\t" \
  "v_exp_f32 v216, v216\n\t" \
  "v_exp_f32 v217, v217\n\t" \
  "v_exp_f32 v218, v218\n\t" \
  "v_smfmac_f32_32x32x32_bf16 %[ob], v[232:235], v[208:215], v248\n\t" \
  "v_exp_f32 v219, v219\n\t" \
  "v_exp_f32 v220, v220\n\t" \
  "v_exp_f32 v221, v221\n\t" \
  "buffer_load_dwordx4 %[vo], %[rs], %[so] offen offset:1024 sc1 lds\n\t" \
  "s_add_u32 %[so], %[so], 16384\n\t" \
  "v_exp_f32 v222, v222\n\t" \
  "v_exp_f32 v223, v223\n\t" \
  "s_waitcnt lgkmcnt(0)\n\t" \
  "v_exp_f32 v192, v192\n\t" \
  "v_exp_f32 v193, v193\n\t" \
  "v_smfmac_f32_32x32x32_bf16 %[ob], v[240:243], v[216:223], v248\n\t" \
  "v_exp_f32 v194, v194\n\t" \
  "v_exp_f32 v195, v195\n\t" \
  "v_exp_f32 v196, v196\n\t" \
  "v_mfma_f32_32x32x16_bf16 v[208:223], v[228:231], %[qb], 0\n\t" \
  "ds_read_b128 v[228:231], %[ka] offset:20800\n\t" \
  "ds_read_b128 v[232:235], %[va0] offset:16640\n\t" \
  "ds_read_b128 v[240:243], %[va1] offset:16640\n\t" \
  "v_exp_f32 v197, v197\n\t" \
  "v_exp_f32 v198, v198\n\t" \
  "v_exp_f32 v199, v199\n\t" \
  "v_exp_f32 v200, v200\n\t" \
  "v_exp_f32 v201, v201\n\t" \
  "v_exp_f32 v202, v202\n\t" \
  "v_smfmac_f32_32x32x32_bf16 %[oa], v[236:239], v[192:199], v248\n\t" \
  "v_exp_f32 v203, v203\n\t" \
  "v_exp_f32 v204, v204\n\t" \
  "v_exp_f32 v205, v205\n\t" \
  "v_exp_f32 v206, v206\n\t" \
  "v_exp_f32 v207, v207\n\t" \
  "v_exp_f32 v208, v208\n\t" \
  "v_exp_f32 v209, v209\n\t" \
  "v_smfmac_f32_32x32x32_bf16 %[oa], v[244:247], v[200:207], v248\n\t" \
  "v_exp_f32 v210, v210\n\t" \
  "v_exp_f32 v211, v211\n\t" \
  "v_exp_f32 v212, v212\n\t" \
  "v_mfma_f32_32x32x16_bf16 v[192:207], v[224:227], %[qa], 0\n\t" \
  "v_exp_f32 v213, v213\n\t" \
  "v_exp_f32 v214, v214\n\t" \
  "v_exp_f32 v215, v215\n\t" \
  "v_exp_f32 v216, v216\n\t" \
  "v_exp_f32 v217, v217\n\t" \
  "v_exp_f32 v218, v218\n\t" \
  "v_smfmac_f32_32x32x32_bf16 %[ob], v[236:239], v[208:215], v248\n\t" \
  "v_exp_f32 v219, v219\n\t" \
  "v_exp_f32 v220, v220\n\t" \
  "v_exp_f32 v221, v221\n\t" \
  "v_exp_f32 v222, v222\n\t" \
  "v_exp_f32 v223, v223\n\t" \
  "s_waitcnt vmcnt(10)\n\t" \
  "s_barrier\n\t" \
  "s_waitcnt lgkmcnt(0)\n\t" \
  "v_exp_f32 v192, v192\n\t" \
  "v_exp_f32 v193, v193\n\t" \
  "v_smfmac_f32_32x32x32_bf16 %[ob], v[244:247], v[216:223], v248\n\t" \
  "v_exp_f32 v194, v194\n\t" \
  "v_exp_f32 v195, v195\n\t" \
  "v_exp_f32 v196, v196\n\t" \
  "v_mfma_f32_32x32x16_bf16 v[208:223], v[224:227], %[qb], 0\n\t" \
  "ds_read_b128 v[224:227], %[ka] offset:24960\n\t" \
  "ds_read_b128 v[236:239], %[va0] offset:20800\n\t" \
  "ds_read_b128 v[244:247], %[va1] offset:20800\n\t" \
  "v_exp_f32 v197, v197\n\t" \
  "v_exp_f32 v198, v198\n\t" \
  "v_exp_f32 v199, v199\n\t" \
  "v_exp_f32 v200, v200\n\t" \
  "v_exp_f32 v201, v201\n\t" \
  "v_exp_f32 v202, v202\n\t" \
  "v_smfmac_f32_32x32x32_bf16 %[oa], v[232:235], v[192:199], v248\n\t" \
  "v_exp_f32 v203, v203\n\t" \
  "v_exp_f32 v204, v204\n\t" \
  "v_exp_f32 v205, v205\n\t" \
  "v_exp_f32 v206, v206\n\t" \
  "v_exp_f32 v207, v207\n\t" \
  "v_exp_f32 v208, v208\n\t" \
  "v_exp_f32 v209, v209\n\t" \
  "v_smfmac_f32_32x32x32_bf16 %[oa], v[240:243], v[200:207], v248\n\t" \
  "v_exp_f32 v210, v210\n\t" \
  "v_exp_f32 v211, v211\n\t" \
  "v_exp_f32 v212, v212\n\t" \
  "v_mfma_f32_32x32x16_bf16 v[192:207], v[228:231], %[qa], 0\n\t" \
  "v_exp_f32 v213, v213\n\t" \
  "v_exp_f32 v214, v214\n\t" \
  "v_exp_f32 v215, v215\n\t" \
  "v_exp_f32 v216, v216\n\t" \
  "v_exp_f32 v217, v217\n\t" \
  "v_exp_f32 v218, v218\n\t" \
  "v_smfmac_f32_32x32x32_bf16 %[ob], v[232:235], v[208:215], v248\n\t" \
  "v_exp_f32 v219, v219\n\t" \
  "v_exp_f32 v220, v220\n\t" \
  "v_exp_f32 v221, v221\n\t" \
  "v_exp_f32 v222, v222\n\t" \
  "v_exp_f32 v223, v223\n\t" \
  "s_waitcnt lgkmcnt(0)\n\t" \
  "v_exp_f32 v192, v192\n\t" \
  "v_exp_f32 v193, v193\n\t" \
  "v_smfmac_f32_32x32x32_bf16 %[ob], v[240:243], v[216:223], v248\n\t" \
  "v_exp_f32 v194, v194\n\t" \
  "v_exp_f32 v195, v195\n\t" \
  "v_exp_f32 v196, v196\n\t" \
  "v_mfma_f32_32x32x16_bf16 v[208:223], v[228:231], %[qb], 0\n\t" \
  "ds_read_b128 v[228:231], %[ka] offset:29120\n\t" \
  "ds_read_b128 v[232:235], %[va0] offset:24960\n\t" \
  "ds_read_b128 v[240:243], %[va1] offset:24960\n\t" \
  "v_exp_f32 v197, v197\n\t" \
  "v_exp_f32 v198, v198\n\t" \
  "v_exp_f32 v199, v199\n\t" \
  "v_exp_f32 v200, v200\n\t" \
  "v_exp_f32 v201, v201\n\t" \
  "v_exp_f32 v202, v202\n\t" \
  "v_smfmac_f32_32x32x32_bf16 %[oa], v[236:239], v[192:199], v248\n\t" \
  "v_exp_f32 v203, v203\n\t" \
  "v_exp_f32 v204, v204\n\t" \
  "v_exp_f32 v205, v205\n\t" \
  "v_exp_f32 v206, v206\n\t" \
  "v_exp_f32 v207, v207\n\t" \
  "v_exp_f32 v208, v208\n\t" \
  "v_exp_f32 v209, v209\n\t" \
  "v_smfmac_f32_32x32x32_bf16 %[oa], v[244:247], v[200:207], v248\n\t" \
  "v_exp_f32 v210, v210\n\t" \
  "v_exp_f32 v211, v211\n\t" \
  "v_exp_f32 v212, v212\n\t" \
  "v_mfma_f32_32x32x16_bf16 v[192:207], v[224:227], %[qa], 0\n\t" \
  "v_exp_f32 v213, v213\n\t" \
  "v_exp_f32 v214, v214\n\t" \
  "v_exp_f32 v215, v215\n\t" \
  "v_exp_f32 v216, v216\n\t" \
  "v_exp_f32 v217, v217\n\t" \
  "v_exp_f32 v218, v218\n\t" \
  "v_smfmac_f32_32x32x32_bf16 %[ob], v[236:239], v[208:215], v248\n\t" \
  "v_exp_f32 v219, v219\n\t" \
  "v_exp_f32 v220, v220\n\t" \
  "v_exp_f32 v221, v221\n\t" \
  "v_exp_f32 v222, v222\n\t" \
  "v_exp_f32 v223, v223\n\t" \
  "s_waitcnt lgkmcnt(0)\n\t" \
  "v_exp_f32 v192, v192\n\t" \
  "v_exp_f32 v193, v193\n\t" \
  "v_smfmac_f32_32x32x32_bf16 %[ob], v[244:247], v[216:223], v248\n\t" \
  "v_exp_f32 v194, v194\n\t" \
  "v_exp_f32 v195, v195\n\t" \
  "v_exp_f32 v196, v196\n\t" \
  "v_mfma_f32_32x32x16_bf16 v[208:223], v[224:227], %[qb], 0\n\t" \
  "ds_read_b128 v[224:227], %[ka] offset:33280\n\t" \
  "ds_read_b128 v[236:239], %[va0] offset:29120\n\t" \
  "ds_read_b128 v[244:247], %[va1] offset:29120\n\t" \
  "v_exp_f32 v197, v197\n\t" \
  "v_exp_f32 v198, v198\n\t" \
  "v_exp_f32 v199, v199\n\t" \
  "buffer_load_dwordx2 v[250:251], %[fo], %[rs], 0 offen sc1\n\t" \
  "v_exp_f32 v200, v200\n\t" \
  "v_exp_f32 v201, v201\n\t" \
  "v_exp_f32 v202, v202\n\t" \
  "v_smfmac_f32_32x32x32_bf16 %[oa], v[232:235], v[192:199], v248\n\t" \
  "v_exp_f32 v203, v203\n\t" \
  "v_exp_f32 v204, v204\n\t" \
  "v_exp_f32 v205, v205\n\t" \
  "v_exp_f32 v206, v206\n\t" \
  "v_exp_f32 v207, v207\n\t" \
  "v_exp_f32 v208, v208\n\t" \
  "v_exp_f32 v209, v209\n\t" \
  "v_smfmac_f32_32x32x32_bf16 %[oa], v[240:243], v[200:207], v248\n\t" \
  "v_exp_f32 v210, v210\n\t" \
  "v_exp_f32 v211, v211\n\t" \
  "v_exp_f32 v212, v212\n\t" \
  "v_mfma_f32_32x32x16_bf16 v[192:207], v[228:231], %[qa], 0\n\t" \
  "v_exp_f32 v213, v213\n\t" \
  "v_exp_f32 v214, v214\n\t" \
  "v_exp_f32 v215, v215\n\t" \
  "v_exp_f32 v216, v216\n\t" \
  "v_exp_f32 v217, v217\n\t" \
  "v_exp_f32 v218, v218\n\t" \
  "v_smfmac_f32_32x32x32_bf16 %[ob], v[232:235], v[208:215], v248\n\t" \
  "v_exp_f32 v219, v219\n\t" \
  "v_exp_f32 v220, v220\n\t" \
  "v_exp_f32 v221, v221\n\t" \
  "v_exp_f32 v222, v222\n\t" \
  "v_exp_f32 v223, v223\n\t" \
  "s_waitcnt lgkmcnt(0)\n\t" \
  "v_exp_f32 v192, v192\n\t" \
  "v_exp_f32 v193, v193\n\t" \
  "v_smfmac_f32_32x32x32_bf16 %[ob], v[240:243], v[216:223], v248\n\t" \
  "v_exp_f32 v194, v194\n\t" \
  "v_exp_f32 v195, v195\n\t" \
  "v_exp_f32 v196, v196\n\t" \
  "v_mfma_f32_32x32x16_bf16 v[208:223], v[228:231], %[qb], 0\n\t" \
  "ds_read_b128 v[228:231], %[ka] offset:37440\n\t" \
  "ds_read_b128 v[232:235], %[va0] offset:33280\n\t" \
  "ds_read_b128 v[240:243], %[va1] offset:33280\n\t" \
  "v_exp_f32 v197, v197\n\t" \
  "v_exp_f32 v198, v198\n\t" \
  "v_exp_f32 v199, v199\n\t" \
  "v_exp_f32 v200, v200\n\t" \
  "v_exp_f32 v201, v201\n\t" \
  "v_exp_f32 v202, v202\n\t" \
  "v_smfmac_f32_32x32x32_bf16 %[oa], v[236:239], v[192:199], v248\n\t" \
  "v_exp_f32 v203, v203\n\t" \
  "v_exp_f32 v204, v204\n\t" \
  "v_exp_f32 v205, v205\n\t" \
  "v_exp_f32 v206, v206\n\t" \
  "v_exp_f32 v207, v207\n\t" \
  "v_exp_f32 v208, v208\n\t" \
  "v_exp_f32 v209, v209\n\t" \
  "v_smfmac_f32_32x32x32_bf16 %[oa], v[244:247], v[200:207], v248\n\t" \
  "v_exp_f32 v210, v210\n\t" \
  "v_exp_f32 v211, v211\n\t" \
  "v_exp_f32 v212, v212\n\t" \
  "v_mfma_f32_32x32x16_bf16 v[192:207], v[224:227], %[qa], 0\n\t" \
  "v_exp_f32 v213, v213\n\t" \
  "v_exp_f32 v214, v214\n\t" \
  "v_exp_f32 v215, v215\n\t" \
  "v_exp_f32 v216, v216\n\t" \
  "v_exp_f32 v217, v217\n\t" \
  "v_exp_f32 v218, v218\n\t" \
  "v_smfmac_f32_32x32x32_bf16 %[ob], v[236:239], v[208:215], v248\n\t" \
  "v_exp_f32 v219, v219\n\t" \
  "v_exp_f32 v220, v220\n\t" \
  "v_exp_f32 v221, v221\n\t" \
  "v_exp_f32 v222, v222\n\t" \
  "v_exp_f32 v223, v223\n\t" \
  "v_add_u32 %[ka], 33280, %[ka]\n\t" \
  "v_add_u32 %[va0], 33280, %[va0]\n\t" \
  "v_add_u32 %[va1], 33280, %[va1]\n\t" \
  "s_mov_b32 m0, %[dummy]\n\t" \
  "s_mov_b32 %[so], 0x100000\n\t" \
  "5:\n\t" \
  "s_waitcnt vmcnt(0)\n\t" \
  "v_cmp_ne_u64 vcc, %[tag], v[250:251]\n\t" \
  "s_cbranch_vccz 6f\n\t" \
  "buffer_load_dwordx2 v[250:251], %[fo], %[rs], 0 offen sc1\n\t" \
  "s_sub_u32 %[so], %[so], 1\n\t" \
  "s_cbranch_scc0 5b\n\t" \
  "6:\n\t" \
  "s_barrier\n\t" \
  "s_waitcnt lgkmcnt(0)\n\t" \
  "v_exp_f32 v192, v192\n\t" \
  "v_exp_f32 v193, v193\n\t" \
  "v_smfmac_f32_32x32x32_bf16 %[ob], v[244:247], v[216:223], v248\n\t" \
  "v_exp_f32 v194, v194\n\t" \
  "v_exp_f32 v195, v195\n\t" \
  "v_exp_f32 v196, v196\n\t" \
  "v_mfma_f32_32x32x16_bf16 v[208:223], v[224:227], %[qb], 0\n\t" \
  "ds_read_b128 v[224:227], %[ka] offset:8320\n\t" \
  "ds_read_b128 v[236:239], %[va0] offset:4160\n\t" \
  "ds_read_b128 v[244:247], %[va1] offset:4160\n\t" \
  "v_exp_f32 v197, v197\n\t" \
  "v_exp_f32 v198, v198\n\t" \
  "v_exp_f32 v199, v199\n\t" \
  "s_mov_b32 %[so], %[p0]\n\t" \
  "buffer_load_dwordx4 v[64:67], %[vo], %[rs], %[so] offen sc1\n\t" \
  "s_add_u32 %[so], %[so], 1024\n\t" \
  "v_exp_f32 v200, v200\n\t" \
  "v_exp_f32 v201, v201\n\t" \
  "v_exp_f32 v202, v202\n\t" \
  "v_smfmac_f32_32x32x32_bf16 %[oa], v[232:235], v[192:199], v248\n\t" \
  "v_exp_f32 v203, v203\n\t" \
  "v_exp_f32 v204, v204\n\t" \
  "v_exp_f32 v205, v205\n\t" \
  "v_exp_f32 v206, v206\n\t" \
  "v_exp_f32 v207, v207\n\t" \
  "v_exp_f32 v208, v208\n\t" \
  "v_exp_f32 v209, v209\n\t" \
  "v_smfmac_f32_32x32x32_bf16 %[oa], v[240:243], v[200:207], v248\n\t" \
  "v_exp_f32 v210, v210\n\t" \
  "v_exp_f32 v211, v211\n\t" \
  "v_exp_f32 v212, v212\n\t" \
  "v_mfma_f32_32x32x16_bf16 v[192:207], v[228:231], %[qa], 0\n\t" \
  "v_exp_f32 v213, v213\n\t" \
  "v_exp_f32 v214, v214\n\t" \
  "v_exp_f32 v215, v215\n\t" \
  "buffer_load_dwordx4 v[68:71], %[vo], %[rs], %[so] offen sc1\n\t" \
  "s_add_u32 %[so], %[so], 1024\n\t" \
  "v_exp_f32 v216, v216\n\t" \
  "v_exp_f32 v217, v217\n\t" \
  "v_exp_f32 v218, v218\n\t" \
  "v_smfmac_f32_32x32x32_bf16 %[ob], v[232:235], v[208:215], v248\n\t" \
  "v_exp_f32 v219, v219\n\t" \
  "v_exp_f32 v220, v220\n\t" \
  "v_exp_f32 v221, v221\n\t" \
  "buffer_load_dwordx4 %[vo], %[rw], %[wo] offen lds\n\t" \
  "s_add_u32 %[wo], %[wo], 262144\n\t" \
  "v_exp_f32 v222, v222\n\t" \
  "v_exp_f32 v223, v223\n\t" \
  "s_waitcnt lgkmcnt(0)\n\t" \
  "v_exp_f32 v192, v192\n\t" \
  "v_exp_f32 v193, v193\n\t" \
  "v_smfmac_f32_32x32x32_bf16 %[ob], v[240:243], v[216:223], v248\n\t" \
  "v_exp_f32 v194, v194\n\t" \
  "v_exp_f32 v195, v195\n\t" \
  "v_exp_f32 v196, v196\n\t" \
  "v_mfma_f32_32x32x16_bf16 v[208:223], v[228:231], %[qb], 0\n\t" \
  "ds_read_b128 v[228:231], %[ka] offset:12480\n\t" \
  "ds_read_b128 v[232:235], %[va0] offset:8320\n\t" \
  "ds_read_b128 v[240:243], %[va1] offset:8320\n\t" \
  "v_exp_f32 v197, v197\n\t" \
  "v_exp_f32 v198, v198\n\t" \
  "v_exp_f32 v199, v199\n\t" \
  "buffer_load_dwordx4 v[72:75], %[vo], %[rs], %[so] offen sc1\n\t" \
  "s_add_u32 %[so], %[so], 1024\n\t" \
  "v_exp_f32 v200, v200\n\t" \
  "v_exp_f32 v201, v201\n\t" \
  "v_exp_f32 v202, v202\n\t" \
  "v_smfmac_f32_32x32x32_bf16 %[oa], v[236:239], v[192:199], v248\n\t" \
  "v_exp_f32 v203, v203\n\t" \
  "v_exp_f32 v204, v204\n\t" \
  "v_exp_f32 v205, v205\n\t" \
  "v_exp_f32 v206, v206\n\t" \
  "v_exp_f32 v207, v207\n\t" \
  "v_exp_f32 v208, v208\n\t" \
  "v_exp_f32 v209, v209\n\t" \
  "v_smfmac_f32_32x32x32_bf16 %[oa], v[244:247], v[200:207], v248\n\t" \
  "v_exp_f32 v210, v210\n\t" \
  "v_exp_f32 v211, v211\n\t" \
  "v_exp_f32 v212, v212\n\t" \
  "v_mfma_f32_32x32x16_bf16 v[192:207], v[224:227], %[qa], 0\n\t" \
  "v_exp_f32 v213, v213\n\t" \
  "v_exp_f32 v214, v214\n\t" \
  "v_exp_f32 v215, v215\n\t" \
  "buffer_load_dwordx4 v[76:79], %[vo], %[rs], %[so] offen sc1\n\t" \
  "s_add_u32 %[so], %[so], 1024\n\t" \
  "v_exp_f32 v216, v216\n\t" \
  "v_exp_f32 v217, v217\n\t" \
  "v_exp_f32 v218, v218\n\t" \
  "v_smfmac_f32_32x32x32_bf16 %[ob], v[236:239], v[208:215], v248\n\t" \
  "v_exp_f32 v219, v219\n\t" \
  "v_exp_f32 v220, v220\n\t" \
  "v_exp_f32 v221, v221\n\t" \
  "v_exp_f32 v222, v222\n\t" \
  "v_exp_f32 v223, v223\n\t" \
  "s_waitcnt lgkmcnt(0)\n\t" \
  "v_exp_f32 v192, v192\n\t" \
  "v_exp_f32 v193, v193\n\t" \
  "v_smfmac_f32_32x32x32_bf16 %[ob], v[244:247], v[216:223], v248\n\t" \
  "v_exp_f32 v194, v194\n\t" \
  "v_exp_f32 v195, v195\n\t" \
  "v_exp_f32 v196, v196\n\t" \
  "v_mfma_f32_32x32x16_bf16 v[208:223], v[224:227], %[qb], 0\n\t" \
  "ds_read_b128 v[224:227], %[ka] offset:16640\n\t" \
  "ds_read_b128 v[236:239], %[va0] offset:12480\n\t" \
  "ds_read_b128 v[244:247], %[va1] offset:12480\n\t" \
  "v_exp_f32 v197, v197\n\t" \
  "v_exp_f32 v198, v198\n\t" \
  "v_exp_f32 v199, v199\n\t" \
  "buffer_load_dwordx4 v[80:83], %[vo], %[rs], %[so] offen sc1\n\t" \
  "s_add_u32 %[so], %[so], 1024\n\t" \
  "v_exp_f32 v200, v200\n\t" \
  "v_exp_f32 v201, v201\n\t" \
  "v_exp_f32 v202, v202\n\t" \
  "v_smfmac_f32_32x32x32_bf16 %[oa], v[232:235], v[192:199], v248\n\t" \
  "v_exp_f32 v203, v203\n\t" \
  "v_exp_f32 v204, v204\n\t" \
  "v_exp_f32 v205, v205\n\t" \
  "buffer_load_dwordx4 %[vo], %[rw], %[wo] offen lds\n\t" \
  "s_add_u32 %[wo], %[wo], 262144\n\t" \
  "v_exp_f32 v206, v206\n\t" \
  "v_exp_f32 v207, v207\n\t" \
  "v_exp_f32 v208, v208\n\t" \
  "v_exp_f32 v209, v209\n\t" \
  "v_smfmac_f32_32x32x32_bf16 %[oa], v[240:243], v[200:207], v248\n\t" \
  "v_exp_f32 v210, v210\n\t" \
  "v_exp_f32 v211, v211\n\t" \
  "v_exp_f32 v212, v212\n\t" \
  "v_mfma_f32_32x32x16_bf16 v[192:207], v[228:231], %[qa], 0\n\t" \
  "v_exp_f32 v213, v213\n\t" \
  "v_exp_f32 v214, v214\n\t" \
  "v_exp_f32 v215, v215\n\t" \
  "buffer_load_dwordx4 v[84:87], %[vo], %[rs], %[so] offen sc1\n\t" \
  "s_add_u32 %[so], %[so], 1024\n\t" \
  "v_exp_f32 v216, v216\n\t" \
  "v_exp_f32 v217, v217\n\t" \
  "v_exp_f32 v218, v218\n\t" \
  "v_smfmac_f32_32x32x32_bf16 %[ob], v[232:235], v[208:215], v248\n\t" \
  "v_exp_f32 v219, v219\n\t" \
  "v_exp_f32 v220, v220\n\t" \
  "v_exp_f32 v221, v221\n\t" \
  "v_exp_f32 v222, v222\n\t" \
  "v_exp_f32 v223, v223\n\t" \
  "s_waitcnt lgkmcnt(0)\n\t" \
  "v_exp_f32 v192, v192\n\t" \
  "v_exp_f32 v193, v193\n\t" \
  "v_smfmac_f32_32x32x32_bf16 %[ob], v[240:243], v[216:223], v248\n\t" \
  "v_exp_f32 v194, v194\n\t" \
  "v_exp_f32 v195, v195\n\t" \
  "v_exp_f32 v196, v196\n\t" \
  "v_mfma_f32_32x32x16_bf16 v[208:223], v[228:231], %[qb], 0\n\t" \
  "ds_read_b128 v[228:231], %[ka] offset:20800\n\t" \
  "ds_read_b128 v[232:235], %[va0] offset:16640\n\t" \
  "ds_read_b128 v[240:243], %[va1] offset:16640\n\t" \
  "v_exp_f32 v197, v197\n\t" \
  "v_exp_f32 v198, v198\n\t" \
  "v_exp_f32 v199, v199\n\t" \
  "buffer_load_dwordx4 v[88:91], %[vo], %[rs], %[so] offen sc1\n\t" \
  "s_add_u32 %[so], %[so], 1024\n\t" \
  "v_exp_f32 v200, v200\n\t" \
  "v_exp_f32 v201, v201\n\t" \
  "v_exp_f32 v202, v202\n\t" \
  "v_smfmac_f32_32x32x32_bf16 %[oa], v[236:239], v[192:199], v248\n\t" \
  "v_exp_f32 v203, v203\n\t" \
  "v_exp_f32 v204, v204\n\t" \
  "v_exp_f32 v205, v205\n\t" \
  "v_exp_f32 v206, v206\n\t" \
  "v_exp_f32 v207, v207\n\t" \
  "v_exp_f32 v208, v208\n\t" \
  "v_exp_f32 v209, v209\n\t" \
  "v_smfmac_f32_32x32x32_bf16 %[oa], v[244:247], v[200:207], v248\n\t" \
  "v_exp_f32 v210, v210\n\t" \
  "v_exp_f32 v211, v211\n\t" \
  "v_exp_f32 v212, v212\n\t" \
  "v_mfma_f32_32x32x16_bf16 v[192:207], v[224:227], %[qa], 0\n\t" \
  "v_exp_f32 v213, v213\n\t" \
  "v_exp_f32 v214, v214\n\t" \
  "v_exp_f32 v215, v215\n\t" \
  "buffer_load_dwordx4 v[92:95], %[vo], %[rs], %[so] offen sc1\n\t" \
  "s_add_u32 %[so], %[so], 1024\n\t" \
  "v_exp_f32 v216, v216\n\t" \
  "v_exp_f32 v217, v217\n\t" \
  "v_exp_f32 v218, v218\n\t" \
  "v_smfmac_f32_32x32x32_bf16 %[ob], v[236:239], v[208:215], v248\n\t" \
  "v_exp_f32 v219, v219\n\t" \
  "v_exp_f32 v220, v220\n\t" \
  "v_exp_f32 v221, v221\n\t" \
  "buffer_load_dwordx4 %[vo], %[rw], %[wo] offen lds\n\t" \
  "s_add_u32 %[wo], %[wo], 262144\n\t" \
  "v_exp_f32 v222, v222\n\t" \
  "v_exp_f32 v223, v223\n\t" \
  "s_waitcnt lgkmcnt(0)\n\t" \
  "v_exp_f32 v192, v192\n\t" \
  "v_exp_f32 v193, v193\n\t" \
  "v_smfmac_f32_32x32x32_bf16 %[ob], v[244:247], v[216:223], v248\n\t" \
  "v_exp_f32 v194, v194\n\t" \
  "v_exp_f32 v195, v195\n\t" \
  "v_exp_f32 v196, v196\n\t" \
  "v_mfma_f32_32x32x16_bf16 v[208:223], v[224:227], %[qb], 0\n\t" \
  "ds_read_b128 v[224:227], %[ka] offset:24960\n\t" \
  "ds_read_b128 v[236:239], %[va0] offset:20800\n\t" \
  "ds_read_b128 v[244:247], %[va1] offset:20800\n\t" \
  "v_exp_f32 v197, v197\n\t" \
  "v_exp_f32 v198, v198\n\t" \
  "v_exp_f32 v199, v199\n\t" \
  "buffer_load_dwordx4 v[96:99], %[vo], %[rs], %[so] offen sc1\n\t" \
  "s_add_u32 %[so], %[so], 1024\n\t" \
  "v_exp_f32 v200, v200\n\t" \
  "v_exp_f32 v201, v201\n\t" \
  "v_exp_f32 v202, v202\n\t" \
  "v_smfmac_f32_32x32x32_bf16 %[oa], v[232:235], v[192:199], v248\n\t" \
  "v_exp_f32 v203, v203\n\t" \
  "v_exp_f32 v204, v204\n\t" \
  "v_exp_f32 v205, v205\n\t" \
  "v_exp_f32 v206, v206\n\t" \
  "v_exp_f32 v207, v207\n\t" \
  "v_exp_f32 v208, v208\n\t" \
  "v_exp_f32 v209, v209\n\t" \
  "v_smfmac_f32_32x32x32_bf16 %[oa], v[240:243], v[200:207], v248\n\t" \
  "v_exp_f32 v210, v210\n\t" \
  "v_exp_f32 v211, v211\n\t" \
  "v_exp_f32 v212, v212\n\t" \
  "v_mfma_f32_32x32x16_bf16 v[192:207], v[228:231], %[qa], 0\n\t" \
  "v_exp_f32 v213, v213\n\t" \
  "v_exp_f32 v214, v214\n\t" \
  "v_exp_f32 v215, v215\n\t" \
  "buffer_load_dwordx4 v[100:103], %[vo], %[rs], %[so] offen sc1\n\t" \
  "s_add_u32 %[so], %[so], 1024\n\t" \
  "v_exp_f32 v216, v216\n\t" \
  "v_exp_f32 v217, v217\n\t" \
  "v_exp_f32 v218, v218\n\t" \
  "v_smfmac_f32_32x32x32_bf16 %[ob], v[232:235], v[208:215], v248\n\t" \
  "v_exp_f32 v219, v219\n\t" \
  "v_exp_f32 v220, v220\n\t" \
  "v_exp_f32 v221, v221\n\t" \
  "v_exp_f32 v222, v222\n\t" \
  "v_exp_f32 v223, v223\n\t" \
  "s_waitcnt lgkmcnt(0)\n\t" \
  "v_exp_f32 v192, v192\n\t" \
  "v_exp_f32 v193, v193\n\t" \
  "v_smfmac_f32_32x32x32_bf16 %[ob], v[240:243], v[216:223], v248\n\t" \
  "v_exp_f32 v194, v194\n\t" \
  "v_exp_f32 v195, v195\n\t" \
  "v_exp_f32 v196, v196\n\t" \
  "v_mfma_f32_32x32x16_bf16 v[208:223], v[228:231], %[qb], 0\n\t" \
  "ds_read_b128 v[228:231], %[ka] offset:29120\n\t" \
  "ds_read_b128 v[232:235], %[va0] offset:24960\n\t" \
  "ds_read_b128 v[240:243], %[va1] offset:24960\n\t" \
  "v_exp_f32 v197, v197\n\t" \
  "v_exp_f32 v198, v198\n\t" \
  "v_exp_f32 v199, v199\n\t" \
  "buffer_load_dwordx4 v[104:107], %[vo], %[rs], %[so] offen sc1\n\t" \
  "s_add_u32 %[so], %[so], 1024\n\t" \
  "v_exp_f32 v200, v200\n\t" \
  "v_exp_f32 v201, v201\n\t" \
  "v_exp_f32 v202, v202\n\t" \
  "v_smfmac_f32_32x32x32_bf16 %[oa], v[236:239], v[192:199], v248\n\t" \
  "v_exp_f32 v203, v203\n\t" \
  "v_exp_f32 v204, v204\n\t" \
  "v_exp_f32 v205, v205\n\t" \
  "buffer_load_dwordx4 %[vo], %[rw], %[wo] offen lds\n\t" \
  "s_add_u32 %[wo], %[wo], 262144\n\t" \
  "v_exp_f32 v206, v206\n\t" \
  "v_exp_f32 v207, v207\n\t" \
  "v_exp_f32 v208, v208\n\t" \
  "v_exp_f32 v209, v209\n\t" \
  "v_smfmac_f32_32x32x32_bf16 %[oa], v[244:247], v[200:207], v248\n\t" \
  "v_exp_f32 v210, v210\n\t" \
  "v_exp_f32 v211, v211\n\t" \
  "v_exp_f32 v212, v212\n\t" \
  "v_mfma_f32_32x32x16_bf16 v[192:207], v[224:227], %[qa], 0\n\t" \
  "v_exp_f32 v213, v213\n\t" \
  "v_exp_f32 v214, v214\n\t" \
  "v_exp_f32 v215, v215\n\t" \
  "buffer_load_dwordx4 v[108:111], %[vo], %[rs], %[so] offen sc1\n\t" \
  "s_add_u32 %[so], %[so], 1024\n\t" \
  "v_exp_f32 v216, v216\n\t" \
  "v_exp_f32 v217, v217\n\t" \
  "v_exp_f32 v218, v218\n\t" \
  "v_smfmac_f32_32x32x32_bf16 %[ob], v[236:239], v[208:215], v248\n\t" \
  "v_exp_f32 v219, v219\n\t" \
  "v_exp_f32 v220, v220\n\t" \
  "v_exp_f32 v221, v221\n\t" \
  "v_exp_f32 v222, v222\n\t" \
  "v_exp_f32 v223, v223\n\t" \
  "s_waitcnt lgkmcnt(0)\n\t" \
  "v_exp_f32 v192, v192\n\t" \
  "v_exp_f32 v193, v193\n\t" \
  "v_smfmac_f32_32x32x32_bf16 %[ob], v[244:247], v[216:223], v248\n\t" \
  "v_exp_f32 v194, v194\n\t" \
  "v_exp_f32 v195, v195\n\t" \
  "v_exp_f32 v196, v196\n\t" \
  "v_mfma_f32_32x32x16_bf16 v[208:223], v[224:227], %[qb], 0\n\t" \
  "ds_read_b128 v[224:227], %[ka] offset:33280\n\t" \
  "ds_read_b128 v[236:239], %[va0] offset:29120\n\t" \
  "ds_read_b128 v[244:247], %[va1] offset:29120\n\t" \
  "v_exp_f32 v197, v197\n\t" \
  "v_exp_f32 v198, v198\n\t" \
  "v_exp_f32 v199, v199\n\t" \
  "buffer_load_dwordx4 v[112:115], %[vo], %[rs], %[so] offen sc1\n\t" \
  "s_add_u32 %[so], %[so], 1024\n\t" \
  "v_exp_f32 v200, v200\n\t" \
  "v_exp_f32 v201, v201\n\t" \
  "v_exp_f32 v202, v202\n\t" \
  "v_smfmac_f32_32x32x32_bf16 %[oa], v[232:235], v[192:199], v248\n\t" \
  "v_exp_f32 v203, v203\n\t" \
  "v_exp_f32 v204, v204\n\t" \
  "v_exp_f32 v205, v205\n\t" \
  "v_exp_f32 v206, v206\n\t" \
  "v_exp_f32 v207, v207\n\t" \
  "v_exp_f32 v208, v208\n\t" \
  "v_exp_f32 v209, v209\n\t" \
  "v_smfmac_f32_32x32x32_bf16 %[oa], v[240:243], v[200:207], v248\n\t" \
  "v_exp_f32 v210, v210\n\t" \
  "v_exp_f32 v211, v211\n\t" \
  "v_exp_f32 v212, v212\n\t" \
  "v_mfma_f32_32x32x16_bf16 v[192:207], v[228:231], %[qa], 0\n\t" \
  "v_exp_f32 v213, v213\n\t" \
  "v_exp_f32 v214, v214\n\t" \
  "v_exp_f32 v215, v215\n\t" \
  "buffer_load_dwordx4 v[116:119], %[vo], %[rs], %[so] offen sc1\n\t" \
  "s_add_u32 %[so], %[so], 1024\n\t" \
  "v_exp_f32 v216, v216\n\t" \
  "v_exp_f32 v217, v217\n\t" \
  "v_exp_f32 v218, v218\n\t" \
  "v_smfmac_f32_32x32x32_bf16 %[ob], v[232:235], v[208:215], v248\n\t" \
  "v_exp_f32 v219, v219\n\t" \
  "v_exp_f32 v220, v220\n\t" \
  "v_exp_f32 v221, v221\n\t" \
  "buffer_load_dwordx4 %[vo], %[rw], %[wo] offen lds\n\t" \
  "s_add_u32 %[wo], %[wo], 262144\n\t" \
  "v_exp_f32 v222, v222\n\t" \
  "v_exp_f32 v223, v223\n\t" \
  "s_waitcnt lgkmcnt(0)\n\t" \
  "v_exp_f32 v192, v192\n\t" \
  "v_exp_f32 v193, v193\n\t" \
  "v_smfmac_f32_32x32x32_bf16 %[ob], v[240:243], v[216:223], v248\n\t" \
  "v_exp_f32 v194, v194\n\t" \
  "v_exp_f32 v195, v195\n\t" \
  "v_exp_f32 v196, v196\n\t" \
  "v_mfma_f32_32x32x16_bf16 v[208:223], v[228:231], %[qb], 0\n\t" \
  "ds_read_b128 v[228:231], %[ka] offset:37440\n\t" \
  "ds_read_b128 v[232:235], %[va0] offset:33280\n\t" \
  "ds_read_b128 v[240:243], %[va1] offset:33280\n\t" \
  "v_exp_f32 v197, v197\n\t" \
  "v_exp_f32 v198, v198\n\t" \
  "v_exp_f32 v199, v199\n\t" \
  "buffer_load_dwordx4 v[120:123], %[vo], %[rs], %[so] offen sc1\n\t" \
  "s_add_u32 %[so], %[so], 1024\n\t" \
  "v_exp_f32 v200, v200\n\t" \
  "v_exp_f32 v201, v201\n\t" \
  "v_exp_f32 v202, v202\n\t" \
  "v_smfmac_f32_32x32x32_bf16 %[oa], v[236:239], v[192:199], v248\n\t" \
  "v_exp_f32 v203, v203\n\t" \
  "v_exp_f32 v204, v204\n\t" \
  "v_exp_f32 v205, v205\n\t" \
  "v_exp_f32 v206, v206\n\t" \
  "v_exp_f32 v207, v207\n\t" \
  "v_exp_f32 v208, v208\n\t" \
  "v_exp_f32 v209, v209\n\t" \
  "v_smfmac_f32_32x32x32_bf16 %[oa], v[244:247], v[200:207], v248\n\t" \
  "v_exp_f32 v210, v210\n\t" \
  "v_exp_f32 v211, v211\n\t" \
  "v_exp_f32 v212, v212\n\t" \
  "v_mfma_f32_32x32x16_bf16 v[192:207], v[224:227], %[qa], 0\n\t" \
  "v_exp_f32 v213, v213\n\t" \
  "v_exp_f32 v214, v214\n\t" \
  "v_exp_f32 v215, v215\n\t" \
  "buffer_load_dwordx4 v[124:127], %[vo], %[rs], %[so] offen sc1\n\t" \
  "s_add_u32 %[so], %[so], 1024\n\t" \
  "v_exp_f32 v216, v216\n\t" \
  "v_exp_f32 v217, v217\n\t" \
  "v_exp_f32 v218, v218\n\t" \
  "v_smfmac_f32_32x32x32_bf16 %[ob], v[236:239], v[208:215], v248\n\t" \
  "v_exp_f32 v219, v219\n\t" \
  "v_exp_f32 v220, v220\n\t" \
  "v_exp_f32 v221, v221\n\t" \
  "v_exp_f32 v222, v222\n\t" \
  "v_exp_f32 v223, v223\n\t" \
  "v_add_u32 %[ka], 33280, %[ka]\n\t" \
  "v_add_u32 %[va0], 33280, %[va0]\n\t" \
  "v_add_u32 %[va1], 33280, %[va1]\n\t" \
  "s_waitcnt lgkmcnt(0)\n\t" \
  "v_exp_f32 v192, v192\n\t" \
  "v_exp_f32 v193, v193\n\t" \
  "v_smfmac_f32_32x32x32_bf16 %[ob], v[244:247], v[216:223], v248\n\t" \
  "v_exp_f32 v194, v194\n\t" \
  "v_exp_f32 v195, v195\n\t" \
  "v_exp_f32 v196, v196\n\t" \
  "v_mfma_f32_32x32x16_bf16 v[208:223], v[224:227], %[qb], 0\n\t" \
  "ds_read_b128 v[224:227], %[ka] offset:8320\n\t" \
  "ds_read_b128 v[236:239], %[va0] offset:4160\n\t" \
  "ds_read_b128 v[244:247], %[va1] offset:4160\n\t" \
  "v_exp_f32 v197, v197\n\t" \
  "v_exp_f32 v198, v198\n\t" \
  "v_exp_f32 v199, v199\n\t" \
  "s_mov_b32 %[so], %[p1]\n\t" \
  "buffer_load_dwordx4 v[128:131], %[vo], %[rs], %[so] offen sc1\n\t" \
  "s_add_u32 %[so], %[so], 1024\n\t" \
  "v_exp_f32 v200, v200\n\t" \
  "v_exp_f32 v201, v201\n\t" \
  "v_exp_f32 v202, v202\n\t" \
  "v_smfmac_f32_32x32x32_bf16 %[oa], v[232:235], v[192:199], v248\n\t" \
  "v_exp_f32 v203, v203\n\t" \
  "v_exp_f32 v204, v204\n\t" \
  "v_exp_f32 v205, v205\n\t" \
  "v_exp_f32 v206, v206\n\t" \
  "v_exp_f32 v207, v207\n\t" \
  "v_exp_f32 v208, v208\n\t" \
  "v_exp_f32 v209, v209\n\t" \
  "v_smfmac_f32_32x32x32_bf16 %[oa], v[240:243], v[200:207], v248\n\t" \
  "v_exp_f32 v210, v210\n\t" \
  "v_exp_f32 v211, v211\n\t" \
  "v_exp_f32 v212, v212\n\t" \
  "v_mfma_f32_32x32x16_bf16 v[192:207], v[228:231], %[qa], 0\n\t" \
  "v_exp_f32 v213, v213\n\t" \
  "v_exp_f32 v214, v214\n\t" \
  "v_exp_f32 v215, v215\n\t" \
  "buffer_load_dwordx4 v[132:135], %[vo], %[rs], %[so] offen sc1\n\t" \
  "s_add_u32 %[so], %[so], 1024\n\t" \
  "v_exp_f32 v216, v216\n\t" \
  "v_exp_f32 v217, v217\n\t" \
  "v_exp_f32 v218, v218\n\t" \
  "v_smfmac_f32_32x32x32_bf16 %[ob], v[232:235], v[208:215], v248\n\t" \
  "v_exp_f32 v219, v219\n\t" \
  "v_exp_f32 v220, v220\n\t" \
  "v_exp_f32 v221, v221\n\t" \
  "buffer_load_dwordx4 %[vo], %[rw], %[wo] offen lds\n\t" \
  "s_add_u32 %[wo], %[wo], 262144\n\t" \
  "v_exp_f32 v222, v222\n\t" \
  "v_exp_f32 v223, v223\n\t" \
  "s_waitcnt lgkmcnt(0)\n\t" \
  "v_exp_f32 v192, v192\n\t" \
  "v_exp_f32 v193, v193\n\t" \
  "v_smfmac_f32_32x32x32_bf16 %[ob], v[240:243], v[216:223], v248\n\t" \
  "v_exp_f32 v194, v194\n\t" \
  "v_exp_f32 v195, v195\n\t" \
  "v_exp_f32 v196, v196\n\t" \
  "v_mfma_f32_32x32x16_bf16 v[208:223], v[228:231], %[qb], 0\n\t" \
  "ds_read_b128 v[228:231], %[ka] offset:12480\n\t" \
  "ds_read_b128 v[232:235], %[va0] offset:8320\n\t" \
  "ds_read_b128 v[240:243], %[va1] offset:8320\n\t" \
  "v_exp_f32 v197, v197\n\t" \
  "v_exp_f32 v198, v198\n\t" \
  "v_exp_f32 v199, v199\n\t" \
  "buffer_load_dwordx4 v[136:139], %[vo], %[rs], %[so] offen sc1\n\t" \
  "s_add_u32 %[so], %[so], 1024\n\t" \
  "v_exp_f32 v200, v200\n\t" \
  "v_exp_f32 v201, v201\n\t" \
  "v_exp_f32 v202, v202\n\t" \
  "v_smfmac_f32_32x32x32_bf16 %[oa], v[236:239], v[192:199], v248\n\t" \
  "v_exp_f32 v203, v203\n\t" \
  "v_exp_f32 v204, v204\n\t" \
  "v_exp_f32 v205, v205\n\t" \
  "v_exp_f32 v206, v206\n\t" \
  "v_exp_f32 v207, v207\n\t" \
  "v_exp_f32 v208, v208\n\t" \
  "v_exp_f32 v209, v209\n\t" \
  "v_smfmac_f32_32x32x32_bf16 %[oa], v[244:247], v[200:207], v248\n\t" \
  "v_exp_f32 v210, v210\n\t" \
  "v_exp_f32 v211, v211\n\t" \
  "v_exp_f32 v212, v212\n\t" \
  "v_mfma_f32_32x32x16_bf16 v[192:207], v[224:227], %[qa], 0\n\t" \
  "v_exp_f32 v213, v213\n\t" \
  "v_exp_f32 v214, v214\n\t" \
  "v_exp_f32 v215, v215\n\t" \
  "buffer_load_dwordx4 v[140:143], %[vo], %[rs], %[so] offen sc1\n\t" \
  "s_add_u32 %[so], %[so], 1024\n\t" \
  "v_exp_f32 v216, v216\n\t" \
  "v_exp_f32 v217, v217\n\t" \
  "v_exp_f32 v218, v218\n\t" \
  "v_smfmac_f32_32x32x32_bf16 %[ob], v[236:239], v[208:215], v248\n\t" \
  "v_exp_f32 v219, v219\n\t" \
  "v_exp_f32 v220, v220\n\t" \
  "v_exp_f32 v221, v221\n\t" \
  "buffer_load_dwordx4 %[vo], %[rw], %[wo] offen lds\n\t" \
  "s_add_u32 %[wo], %[wo], 262144\n\t" \
  "v_exp_f32 v222, v222\n\t" \
  "v_exp_f32 v223, v223\n\t" \
  "s_waitcnt lgkmcnt(0)\n\t" \
  "v_exp_f32 v192, v192\n\t" \
  "v_exp_f32 v193, v193\n\t" \
  "v_smfmac_f32_32x32x32_bf16 %[ob], v[244:247], v[216:223], v248\n\t" \
  "v_exp_f32 v194, v194\n\t" \
  "v_exp_f32 v195, v195\n\t" \
  "v_exp_f32 v196, v196\n\t" \
  "v_mfma_f32_32x32x16_bf16 v[208:223], v[224:227], %[qb], 0\n\t" \
  "ds_read_b128 v[224:227], %[ka] offset:16640\n\t" \
  "ds_read_b128 v[236:239], %[va0] offset:12480\n\t" \
  "ds_read_b128 v[244:247], %[va1] offset:12480\n\t" \
  "v_exp_f32 v197, v197\n\t" \
  "v_exp_f32 v198, v198\n\t" \
  "v_exp_f32 v199, v199\n\t" \
  "buffer_load_dwordx4 v[144:147], %[vo], %[rs], %[so] offen sc1\n\t" \
  "s_add_u32 %[so], %[so], 1024\n\t" \
  "v_exp_f32 v200, v200\n\t" \
  "v_exp_f32 v201, v201\n\t" \
  "v_exp_f32 v202, v202\n\t" \
  "v_smfmac_f32_32x32x32_bf16 %[oa], v[232:235], v[192:199], v248\n\t" \
  "v_exp_f32 v203, v203\n\t" \
  "v_exp_f32 v204, v204\n\t" \
  "v_exp_f32 v205, v205\n\t" \
  "v_exp_f32 v206, v206\n\t" \
  "v_exp_f32 v207, v207\n\t" \
  "v_exp_f32 v208, v208\n\t" \
  "v_exp_f32 v209, v209\n\t" \
  "v_smfmac_f32_32x32x32_bf16 %[oa], v[240:243], v[200:207], v248\n\t" \
  "v_exp_f32 v210, v210\n\t" \
  "v_exp_f32 v211, v211\n\t" \
  "v_exp_f32 v212, v212\n\t" \
  "v_mfma_f32_32x32x16_bf16 v[192:207], v[228:231], %[qa], 0\n\t" \
  "v_exp_f32 v213, v213\n\t" \
  "v_exp_f32 v214, v214\n\t" \
  "v_exp_f32 v215, v215\n\t" \
  "buffer_load_dwordx4 v[148:151], %[vo], %[rs], %[so] offen sc1\n\t" \
  "s_add_u32 %[so], %[so], 1024\n\t" \
  "v_exp_f32 v216, v216\n\t" \
  "v_exp_f32 v217, v217\n\t" \
  "v_exp_f32 v218, v218\n\t" \
  "v_smfmac_f32_32x32x32_bf16 %[ob], v[232:235], v[208:215], v248\n\t" \
  "v_exp_f32 v219, v219\n\t" \
  "v_exp_f32 v220, v220\n\t" \
  "v_exp_f32 v221, v221\n\t" \
  "buffer_load_dwordx4 %[vo], %[rw], %[wo] offen lds\n\t" \
  "s_add_u32 %[wo], %[wo], 262144\n\t" \
  "v_exp_f32 v222, v222\n\t" \
  "v_exp_f32 v223, v223\n\t" \
  "s_waitcnt lgkmcnt(0)\n\t" \
  "v_exp_f32 v192, v192\n\t" \
  "v_exp_f32 v193, v193\n\t" \
  "v_smfmac_f32_32x32x32_bf16 %[ob], v[240:243], v[216:223], v248\n\t" \
  "v_exp_f32 v194, v194\n\t" \
  "v_exp_f32 v195, v195\n\t" \
  "v_exp_f32 v196, v196\n\t" \
  "v_mfma_f32_32x32x16_bf16 v[208:223], v[228:231], %[qb], 0\n\t" \
  "ds_read_b128 v[228:231], %[ka] offset:20800\n\t" \
  "ds_read_b128 v[232:235], %[va0] offset:16640\n\t" \
  "ds_read_b128 v[240:243], %[va1] offset:16640\n\t" \
  "v_exp_f32 v197, v197\n\t" \
  "v_exp_f32 v198, v198\n\t" \
  "v_exp_f32 v199, v199\n\t" \
  "buffer_load_dwordx4 v[152:155], %[vo], %[rs], %[so] offen sc1\n\t" \
  "s_add_u32 %[so], %[so], 1024\n\t" \
  "v_exp_f32 v200, v200\n\t" \
  "v_exp_f32 v201, v201\n\t" \
  "v_exp_f32 v202, v202\n\t" \
  "v_smfmac_f32_32x32x32_bf16 %[oa], v[236:239], v[192:199], v248\n\t" \
  "v_exp_f32 v203, v203\n\t" \
  "v_exp_f32 v204, v204\n\t" \
  "v_exp_f32 v205, v205\n\t" \
  "v_exp_f32 v206, v206\n\t" \
  "v_exp_f32 v207, v207\n\t" \
  "v_exp_f32 v208, v208\n\t" \
  "v_exp_f32 v209, v209\n\t" \
  "v_smfmac_f32_32x32x32_bf16 %[oa], v[244:247], v[200:207], v248\n\t" \
  "v_exp_f32 v210, v210\n\t" \
  "v_exp_f32 v211, v211\n\t" \
  "v_exp_f32 v212, v212\n\t" \
  "v_mfma_f32_32x32x16_bf16 v[192:207], v[224:227], %[qa], 0\n\t" \
  "v_exp_f32 v213, v213\n\t" \
  "v_exp_f32 v214, v214\n\t" \
  "v_exp_f32 v215, v215\n\t" \
  "buffer_load_dwordx4 v[156:159], %[vo], %[rs], %[so] offen sc1\n\t" \
  "s_add_u32 %[so], %[so], 1024\n\t" \
  "v_exp_f32 v216, v216\n\t" \
  "v_exp_f32 v217, v217\n\t" \
  "v_exp_f32 v218, v218\n\t" \
  "v_smfmac_f32_32x32x32_bf16 %[ob], v[236:239], v[208:215], v248\n\t" \
  "v_exp_f32 v219, v219\n\t" \
  "v_exp_f32 v220, v220\n\t" \
  "v_exp_f32 v221, v221\n\t" \
  "buffer_load_dwordx4 %[vo], %[rw], %[wo] offen lds\n\t" \
  "s_add_u32 %[wo], %[wo], 262144\n\t" \
  "v_exp_f32 v222, v222\n\t" \
  "v_exp_f32 v223, v223\n\t" \
  "s_waitcnt lgkmcnt(0)\n\t" \
  "v_exp_f32 v192, v192\n\t" \
  "v_exp_f32 v193, v193\n\t" \
  "v_smfmac_f32_32x32x32_bf16 %[ob], v[244:247], v[216:223], v248\n\t" \
  "v_exp_f32 v194, v194\n\t" \
  "v_exp_f32 v195, v195\n\t" \
  "v_exp_f32 v196, v196\n\t" \
  "v_mfma_f32_32x32x16_bf16 v[208:223], v[224:227], %[qb], 0\n\t" \
  "ds_read_b128 v[224:227], %[ka] offset:24960\n\t" \
  "ds_read_b128 v[236:239], %[va0] offset:20800\n\t" \
  "ds_read_b128 v[244:247], %[va1] offset:20800\n\t" \
  "v_exp_f32 v197, v197\n\t" \
  "v_exp_f32 v198, v198\n\t" \
  "v_exp_f32 v199, v199\n\t" \
  "buffer_load_dwordx4 v[160:163], %[vo], %[rs], %[so] offen sc1\n\t" \
  "s_add_u32 %[so], %[so], 1024\n\t" \
  "v_exp_f32 v200, v200\n\t" \
  "v_exp_f32 v201, v201\n\t" \
  "v_exp_f32 v202, v202\n\t" \
  "v_smfmac_f32_32x32x32_bf16 %[oa], v[232:235], v[192:199], v248\n\t" \
  "v_exp_f32 v203, v203\n\t" \
  "v_exp_f32 v204, v204\n\t" \
  "v_exp_f32 v205, v205\n\t" \
  "v_exp_f32 v206, v206\n\t" \
  "v_exp_f32 v207, v207\n\t" \
  "v_exp_f32 v208, v208\n\t" \
  "v_exp_f32 v209, v209\n\t" \
  "v_smfmac_f32_32x32x32_bf16 %[oa], v[240:243], v[200:207], v248\n\t" \
  "v_exp_f32 v210, v210\n\t" \
  "v_exp_f32 v211, v211\n\t" \
  "v_exp_f32 v212, v212\n\t" \
  "v_mfma_f32_32x32x16_bf16 v[192:207], v[228:231], %[qa], 0\n\t" \
  "v_exp_f32 v213, v213\n\t" \
  "v_exp_f32 v214, v214\n\t" \
  "v_exp_f32 v215, v215\n\t" \
  "s_mov_b32 %[so], %[p2]\n\t" \
  "buffer_load_dwordx4 v[164:167], %[vo], %[rs], %[so] offen sc1\n\t" \
  "s_add_u32 %[so], %[so], 1024\n\t" \
  "v_exp_f32 v216, v216\n\t" \
  "v_exp_f32 v217, v217\n\t" \
  "v_exp_f32 v218, v218\n\t" \
  "v_smfmac_f32_32x32x32_bf16 %[ob], v[232:235], v[208:215], v248\n\t" \
  "v_exp_f32 v219, v219\n\t" \
  "v_exp_f32 v220, v220\n\t" \
  "v_exp_f32 v221, v221\n\t" \
  "buffer_load_dwordx4 %[vo], %[rw], %[wo] offen lds\n\t" \
  "s_add_u32 %[wo], %[wo], 262144\n\t" \
  "v_exp_f32 v222, v222\n\t" \
  "v_exp_f32 v223, v223\n\t" \
  "s_waitcnt lgkmcnt(0)\n\t" \
  "v_exp_f32 v192, v192\n\t" \
  "v_exp_f32 v193, v193\n\t" \
  "v_smfmac_f32_32x32x32_bf16 %[ob], v[240:243], v[216:223], v248\n\t" \
  "v_exp_f32 v194, v194\n\t" \
  "v_exp_f32 v195, v195\n\t" \
  "v_exp_f32 v196, v196\n\t" \
  "v_mfma_f32_32x32x16_bf16 v[208:223], v[228:231], %[qb], 0\n\t" \
  "ds_read_b128 v[228:231], %[ka] offset:29120\n\t" \
  "ds_read_b128 v[232:235], %[va0] offset:24960\n\t" \
  "ds_read_b128 v[240:243], %[va1] offset:24960\n\t" \
  "v_exp_f32 v197, v197\n\t" \
  "v_exp_f32 v198, v198\n\t" \
  "v_exp_f32 v199, v199\n\t" \
  "buffer_load_dwordx4 v[168:171], %[vo], %[rs], %[so] offen sc1\n\t" \
  "s_add_u32 %[so], %[so], 1024\n\t" \
  "v_exp_f32 v200, v200\n\t" \
  "v_exp_f32 v201, v201\n\t" \
  "v_exp_f32 v202, v202\n\t" \
  "v_smfmac_f32_32x32x32_bf16 %[oa], v[236:239], v[192:199], v248\n\t" \
  "v_exp_f32 v203, v203\n\t" \
  "v_exp_f32 v204, v204\n\t" \
  "v_exp_f32 v205, v205\n\t" \
  "v_exp_f32 v206, v206\n\t" \
  "v_exp_f32 v207, v207\n\t" \
  "v_exp_f32 v208, v208\n\t" \
  "v_exp_f32 v209, v209\n\t" \
  "v_smfmac_f32_32x32x32_bf16 %[oa], v[244:247], v[200:207], v248\n\t" \
  "v_exp_f32 v210, v210\n\t" \
  "v_exp_f32 v211, v211\n\t" \
  "v_exp_f32 v212, v212\n\t" \
  "v_mfma_f32_32x32x16_bf16 v[192:207], v[224:227], %[qa], 0\n\t" \
  "v_exp_f32 v213, v213\n\t" \
  "v_exp_f32 v214, v214\n\t" \
  "v_exp_f32 v215, v215\n\t" \
  "buffer_load_dwordx4 v[172:175], %[vo], %[rs], %[so] offen sc1\n\t" \
  "s_add_u32 %[so], %[so], 1024\n\t" \
  "v_exp_f32 v216, v216\n\t" \
  "v_exp_f32 v217, v217\n\t" \
  "v_exp_f32 v218, v218\n\t" \
  "v_smfmac_f32_32x32x32_bf16 %[ob], v[236:239], v[208:215], v248\n\t" \
  "v_exp_f32 v219, v219\n\t" \
  "v_exp_f32 v220, v220\n\t" \
  "v_exp_f32 v221, v221\n\t" \
  "buffer_load_dwordx4 %[vo], %[rw], %[wo] offen lds\n\t" \
  "s_add_u32 %[wo], %[wo], 262144\n\t" \
  "v_exp_f32 v222, v222\n\t" \
  "v_exp_f32 v223, v223\n\t" \
  "s_waitcnt lgkmcnt(0)\n\t" \
  "v_exp_f32 v192, v192\n\t" \
  "v_exp_f32 v193, v193\n\t" \
  "v_smfmac_f32_32x32x32_bf16 %[ob], v[244:247], v[216:223], v248\n\t" \
  "v_exp_f32 v194, v194\n\t" \
  "v_exp_f32 v195, v195\n\t" \
  "v_exp_f32 v196, v196\n\t" \
  "v_mfma_f32_32x32x16_bf16 v[208:223], v[224:227], %[qb], 0\n\t" \
  "ds_read_b128 v[224:227], %[ka] offset:33280\n\t" \
  "ds_read_b128 v[236:239], %[va0] offset:29120\n\t" \
  "ds_read_b128 v[244:247], %[va1] offset:29120\n\t" \
  "v_exp_f32 v197, v197\n\t" \
  "v_exp_f32 v198, v198\n\t" \
  "v_exp_f32 v199, v199\n\t" \
  "buffer_load_dwordx4 v[176:179], %[vo], %[rs], %[so] offen sc1\n\t" \
  "s_add_u32 %[so], %[so], 1024\n\t" \
  "v_exp_f32 v200, v200\n\t" \
  "v_exp_f32 v201, v201\n\t" \
  "v_exp_f32 v202, v202\n\t" \
  "v_smfmac_f32_32x32x32_bf16 %[oa], v[232:235], v[192:199], v248\n\t" \
  "v_exp_f32 v203, v203\n\t" \
  "v_exp_f32 v204, v204\n\t" \
  "v_exp_f32 v205, v205\n\t" \
  "v_exp_f32 v206, v206\n\t" \
  "v_exp_f32 v207, v207\n\t" \
  "v_exp_f32 v208, v208\n\t" \
  "v_exp_f32 v209, v209\n\t" \
  "v_smfmac_f32_32x32x32_bf16 %[oa], v[240:243], v[200:207], v248\n\t" \
  "v_exp_f32 v210, v210\n\t" \
  "v_exp_f32 v211, v211\n\t" \
  "v_exp_f32 v212, v212\n\t" \
  "v_mfma_f32_32x32x16_bf16 v[192:207], v[228:231], %[qa], 0\n\t" \
  "v_exp_f32 v213, v213\n\t" \
  "v_exp_f32 v214, v214\n\t" \
  "v_exp_f32 v215, v215\n\t" \
  "buffer_load_dwordx4 v[180:183], %[vo], %[rs], %[so] offen sc1\n\t" \
  "s_add_u32 %[so], %[so], 1024\n\t" \
  "v_exp_f32 v216, v216\n\t" \
  "v_exp_f32 v217, v217\n\t" \
  "v_exp_f32 v218, v218\n\t" \
  "v_smfmac_f32_32x32x32_bf16 %[ob], v[232:235], v[208:215], v248\n\t" \
  "v_exp_f32 v219, v219\n\t" \
  "v_exp_f32 v220, v220\n\t" \
  "v_exp_f32 v221, v221\n\t" \
  "buffer_load_dwordx4 %[vo], %[rw], %[wo] offen lds\n\t" \
  "s_add_u32 %[wo], %[wo], 262144\n\t" \
  "v_exp_f32 v222, v222\n\t" \
  "v_exp_f32 v223, v223\n\t" \
  "s_waitcnt lgkmcnt(0)\n\t" \
  "v_exp_f32 v192, v192\n\t" \
  "v_exp_f32 v193, v193\n\t" \
  "v_smfmac_f32_32x32x32_bf16 %[ob], v[240:243], v[216:223], v248\n\t" \
  "v_exp_f32 v194, v194\n\t" \
  "v_exp_f32 v195, v195\n\t" \
  "v_exp_f32 v196, v196\n\t" \
  "v_mfma_f32_32x32x16_bf16 v[208:223], v[228:231], %[qb], 0\n\t" \
  "ds_read_b128 v[228:231], %[ka] offset:37440\n\t" \
  "ds_read_b128 v[232:235], %[va0] offset:33280\n\t" \
  "ds_read_b128 v[240:243], %[va1] offset:33280\n\t" \
  "v_exp_f32 v197, v197\n\t" \
  "v_exp_f32 v198, v198\n\t" \
  "v_exp_f32 v199, v199\n\t" \
  "buffer_load_dwordx4 v[184:187], %[vo], %[rs], %[so] offen sc1\n\t" \
  "s_add_u32 %[so], %[so], 1024\n\t" \
  "v_exp_f32 v200, v200\n\t" \
  "v_exp_f32 v201, v201\n\t" \
  "v_exp_f32 v202, v202\n\t" \
  "v_smfmac_f32_32x32x32_bf16 %[oa], v[236:239], v[192:199], v248\n\t" \
  "v_exp_f32 v203, v203\n\t" \
  "v_exp_f32 v204, v204\n\t" \
  "v_exp_f32 v205, v205\n\t" \
  "v_exp_f32 v206, v206\n\t" \
  "v_exp_f32 v207, v207\n\t" \
  "v_exp_f32 v208, v208\n\t" \
  "v_exp_f32 v209, v209\n\t" \
  "v_smfmac_f32_32x32x32_bf16 %[oa], v[244:247], v[200:207], v248\n\t" \
  "v_exp_f32 v210, v210\n\t" \
  "v_exp_f32 v211, v211\n\t" \
  "v_exp_f32 v212, v212\n\t" \
  "v_mfma_f32_32x32x16_bf16 v[192:207], v[224:227], %[qa], 0\n\t" \
  "v_exp_f32 v213, v213\n\t" \
  "v_exp_f32 v214, v214\n\t" \
  "v_exp_f32 v215, v215\n\t" \
  "buffer_load_dwordx4 v[188:191], %[vo], %[rs], %[so] offen sc1\n\t" \
  "s_add_u32 %[so], %[so], 1024\n\t" \
  "v_exp_f32 v216, v216\n\t" \
  "v_exp_f32 v217, v217\n\t" \
  "v_exp_f32 v218, v218\n\t" \
  "v_smfmac_f32_32x32x32_bf16 %[ob], v[236:239], v[208:215], v248\n\t" \
  "v_exp_f32 v219, v219\n\t" \
  "v_exp_f32 v220, v220\n\t" \
  "v_exp_f32 v221, v221\n\t" \
  "v_exp_f32 v222, v222\n\t" \
  "v_exp_f32 v223, v223\n\t" \
  "v_add_u32 %[ka], 33280, %[ka]\n\t" \
  "v_add_u32 %[va0], 33280, %[va0]\n\t" \
  "v_add_u32 %[va1], 33280, %[va1]\n\t" \
  "1:\n\t" \
  "s_waitcnt lgkmcnt(0)\n\t" \
  "v_exp_f32 v192, v192\n\t" \
  "v_exp_f32 v193, v193\n\t" \
  "v_smfmac_f32_32x32x32_bf16 %[ob], v[244:247], v[216:223], v248\n\t" \
  "v_exp_f32 v194, v194\n\t" \
  "v_exp_f32 v195, v195\n\t" \
  "v_exp_f32 v196, v196\n\t" \
  "v_mfma_f32_32x32x16_bf16 v[208:223], v[224:227], %[qb], 0\n\t" \
  "ds_read_b128 v[224:227], %[ka] offset:8320\n\t" \
  "ds_read_b128 v[236:239], %[va0] offset:4160\n\t" \
  "ds_read_b128 v[244:247], %[va1] offset:4160\n\t" \
  "v_exp_f32 v197, v197\n\t" \
  "v_exp_f32 v198, v198\n\t" \
  "v_exp_f32 v199, v199\n\t" \
  "v_exp_f32 v200, v200\n\t" \
  "v_exp_f32 v201, v201\n\t" \
  "v_exp_f32 v202, v202\n\t" \
  "v_smfmac_f32_32x32x32_bf16 %[oa], v[232:235], v[192:199], v248\n\t" \
  "v_exp_f32 v203, v203\n\t" \
  "v_exp_f32 v204, v204\n\t" \
  "v_exp_f32 v205, v205\n\t" \
  "v_exp_f32 v206, v206\n\t" \
  "v_exp_f32 v207, v207\n\t" \
  "v_exp_f32 v208, v208\n\t" \
  "v_exp_f32 v209, v209\n\t" \
  "v_smfmac_f32_32x32x32_bf16 %[oa], v[240:243], v[200:207], v248\n\t" \
  "v_exp_f32 v210, v210\n\t" \
  "v_exp_f32 v211, v211\n\t" \
  "v_exp_f32 v212, v212\n\t" \
  "v_mfma_f32_32x32x16_bf16 v[192:207], v[228:231], %[qa], 0\n\t" \
  "v_exp_f32 v213, v213\n\t" \
  "v_exp_f32 v214, v214\n\t" \
  "v_exp_f32 v215, v215\n\t" \
  "v_exp_f32 v216, v216\n\t" \
  "v_exp_f32 v217, v217\n\t" \
  "v_exp_f32 v218, v218\n\t" \
  "v_smfmac_f32_32x32x32_bf16 %[ob], v[232:235], v[208:215], v248\n\t" \
  "v_exp_f32 v219, v219\n\t" \
  "v_exp_f32 v220, v220\n\t" \
  "v_exp_f32 v221, v221\n\t" \
  "v_exp_f32 v222, v222\n\t" \
  "v_exp_f32 v223, v223\n\t" \
  "s_waitcnt lgkmcnt(0)\n\t" \
  "v_exp_f32 v192, v192\n\t" \
  "v_exp_f32 v193, v193\n\t" \
  "v_smfmac_f32_32x32x32_bf16 %[ob], v[240:243], v[216:223], v248\n\t" \
  "v_exp_f32 v194, v194\n\t" \
  "v_exp_f32 v195, v195\n\t" \
  "v_exp_f32 v196, v196\n\t" \
  "v_mfma_f32_32x32x16_bf16 v[208:223], v[228:231], %[qb], 0\n\t" \
  "ds_read_b128 v[228:231], %[ka] offset:12480\n\t" \
  "ds_read_b128 v[232:235], %[va0] offset:8320\n\t" \
  "ds_read_b128 v[240:243], %[va1] offset:8320\n\t" \
  "v_exp_f32 v197, v197\n\t" \
  "v_exp_f32 v198, v198\n\t" \
  "v_exp_f32 v199, v199\n\t" \
  "v_exp_f32 v200, v200\n\t" \
  "v_exp_f32 v201, v201\n\t" \
  "v_exp_f32 v202, v202\n\t" \
  "v_smfmac_f32_32x32x32_bf16 %[oa], v[236:239], v[192:199], v248\n\t" \
  "v_exp_f32 v203, v203\n\t" \
  "v_exp_f32 v204, v204\n\t" \
  "v_exp_f32 v205, v205\n\t" \
  "v_exp_f32 v206, v206\n\t" \
  "v_exp_f32 v207, v207\n\t" \
  "v_exp_f32 v208, v208\n\t" \
  "v_exp_f32 v209, v209\n\t" \
  "v_smfmac_f32_32x32x32_bf16 %[oa], v[244:247], v[200:207], v248\n\t" \
  "v_exp_f32 v210, v210\n\t" \
  "v_exp_f32 v211, v211\n\t" \
  "v_exp_f32 v212, v212\n\t" \
  "v_mfma_f32_32x32x16_bf16 v[192:207], v[224:227], %[qa], 0\n\t" \
  "v_exp_f32 v213, v213\n\t" \
  "v_exp_f32 v214, v214\n\t" \
  "v_exp_f32 v215, v215\n\t" \
  "v_exp_f32 v216, v216\n\t" \
  "v_exp_f32 v217, v217\n\t" \
  "v_exp_f32 v218, v218\n\t" \
  "v_smfmac_f32_32x32x32_bf16 %[ob], v[236:239], v[208:215], v248\n\t" \
  "v_exp_f32 v219, v219\n\t" \
  "v_exp_f32 v220, v220\n\t" \
  "v_exp_f32 v221, v221\n\t" \
  "v_exp_f32 v222, v222\n\t" \
  "v_exp_f32 v223, v223\n\t" \
  "s_waitcnt lgkmcnt(0)\n\t" \
  "v_exp_f32 v192, v192\n\t" \
  "v_exp_f32 v193, v193\n\t" \
  "v_smfmac_f32_32x32x32_bf16 %[ob], v[244:247], v[216:223], v248\n\t" \
  "v_exp_f32 v194, v194\n\t" \
  "v_exp_f32 v195, v195\n\t" \
  "v_exp_f32 v196, v196\n\t" \
  "v_mfma_f32_32x32x16_bf16 v[208:223], v[224:227], %[qb], 0\n\t" \
  "ds_read_b128 v[224:227], %[ka] offset:16640\n\t" \
  "ds_read_b128 v[236:239], %[va0] offset:12480\n\t" \
  "ds_read_b128 v[244:247], %[va1] offset:12480\n\t" \
  "v_exp_f32 v197, v197\n\t" \
  "v_exp_f32 v198, v198\n\t" \
  "v_exp_f32 v199, v199\n\t" \
  "v_exp_f32 v200, v200\n\t" \
  "v_exp_f32 v201, v201\n\t" \
  "v_exp_f32 v202, v202\n\t" \
  "v_smfmac_f32_32x32x32_bf16 %[oa], v[232:235], v[192:199], v248\n\t" \
  "v_exp_f32 v203, v203\n\t" \
  "v_exp_f32 v204, v204\n\t" \
  "v_exp_f32 v205, v205\n\t" \
  "v_exp_f32 v206, v206\n\t" \
  "v_exp_f32 v207, v207\n\t" \
  "v_exp_f32 v208, v208\n\t" \
  "v_exp_f32 v209, v209\n\t" \
  "v_smfmac_f32_32x32x32_bf16 %[oa], v[240:243], v[200:207], v248\n\t" \
  "v_exp_f32 v210, v210\n\t" \
  "v_exp_f32 v211, v211\n\t" \
  "v_exp_f32 v212, v212\n\t" \
  "v_mfma_f32_32x32x16_bf16 v[192:207], v[228:231], %[qa], 0\n\t" \
  "v_exp_f32 v213, v213\n\t" \
  "v_exp_f32 v214, v214\n\t" \
  "v_exp_f32 v215, v215\n\t" \
  "v_exp_f32 v216, v216\n\t" \
  "v_exp_f32 v217, v217\n\t" \
  "v_exp_f32 v218, v218\n\t" \
  "v_smfmac_f32_32x32x32_bf16 %[ob], v[232:235], v[208:215], v248\n\t" \
  "v_exp_f32 v219, v219\n\t" \
  "v_exp_f32 v220, v220\n\t" \
  "v_exp_f32 v221, v221\n\t" \
  "v_exp_f32 v222, v222\n\t" \
  "v_exp_f32 v223, v223\n\t" \
  "s_waitcnt lgkmcnt(0)\n\t" \
  "v_exp_f32 v192, v192\n\t" \
  "v_exp_f32 v193, v193\n\t" \
  "v_smfmac_f32_32x32x32_bf16 %[ob], v[240:243], v[216:223], v248\n\t" \
  "v_exp_f32 v194, v194\n\t" \
  "v_exp_f32 v195, v195\n\t" \
  "v_exp_f32 v196, v196\n\t" \
  "v_mfma_f32_32x32x16_bf16 v[208:223], v[228:231], %[qb], 0\n\t" \
  "ds_read_b128 v[228:231], %[ka] offset:20800\n\t" \
  "ds_read_b128 v[232:235], %[va0] offset:16640\n\t" \
  "ds_read_b128 v[240:243], %[va1] offset:16640\n\t" \
  "v_exp_f32 v197, v197\n\t" \
  "v_exp_f32 v198, v198\n\t" \
  "v_exp_f32 v199, v199\n\t" \
  "v_exp_f32 v200, v200\n\t" \
  "v_exp_f32 v201, v201\n\t" \
  "v_exp_f32 v202, v202\n\t" \
  "v_smfmac_f32_32x32x32_bf16 %[oa], v[236:239], v[192:199], v248\n\t" \
  "v_exp_f32 v203, v203\n\t" \
  "v_exp_f32 v204, v204\n\t" \
  "v_exp_f32 v205, v205\n\t" \
  "v_exp_f32 v206, v206\n\t" \
  "v_exp_f32 v207, v207\n\t" \
  "v_exp_f32 v208, v208\n\t" \
  "v_exp_f32 v209, v209\n\t" \
  "v_smfmac_f32_32x32x32_bf16 %[oa], v[244:247], v[200:207], v248\n\t" \
  "v_exp_f32 v210, v210\n\t" \
  "v_exp_f32 v211, v211\n\t" \
  "v_exp_f32 v212, v212\n\t" \
  "v_mfma_f32_32x32x16_bf16 v[192:207], v[224:227], %[qa], 0\n\t" \
  "v_exp_f32 v213, v213\n\t" \
  "v_exp_f32 v214, v214\n\t" \
  "v_exp_f32 v215, v215\n\t" \
  "v_exp_f32 v216, v216\n\t" \
  "v_exp_f32 v217, v217\n\t" \
  "v_exp_f32 v218, v218\n\t" \
  "v_smfmac_f32_32x32x32_bf16 %[ob], v[236:239], v[208:215], v248\n\t" \
  "v_exp_f32 v219, v219\n\t" \
  "v_exp_f32 v220, v220\n\t" \
  "v_exp_f32 v221, v221\n\t" \
  "v_exp_f32 v222, v222\n\t" \
  "v_exp_f32 v223, v223\n\t" \
  "v_add_u32 %[ka], 16640, %[ka]\n\t" \
  "v_add_u32 %[va0], 16640, %[va0]\n\t" \
  "v_add_u32 %[va1], 16640, %[va1]\n\t" \
  "s_add_u32 %[cnt], %[cnt], 1\n\t" \
  "s_cmp_lg_u32 %[cnt], 2\n\t" \
  "s_cbranch_scc1 1b\n\t" \
  "s_waitcnt vmcnt(0) lgkmcnt(0)\n\t" \
  "v_smfmac_f32_32x32x32_bf16 %[ob], v[244:247], v[216:223], v248\n\t" \
  "s_mov_b32 m0, %[keep]\n\t" \
  "s_nop 11\n\t" \
  "s_branch 9f\n\t" \
  "8:\n\t" \
  "s_mov_b32 %[so], %[p0]\n\t" \
  "buffer_load_dwordx4 v[64:67], %[vo], %[rs], %[so] offen sc1\n\t" \
  "s_add_u32 %[so], %[so], 1024\n\t" \
  "buffer_load_dwordx4 v[68:71], %[vo], %[rs], %[so] offen sc1\n\t" \
  "s_add_u32 %[so], %[so], 1024\n\t" \
  "buffer_load_dwordx4 v[72:75], %[vo], %[rs], %[so] offen sc1\n\t" \
  "s_add_u32 %[so], %[so], 1024\n\t" \
  "buffer_load_dwordx4 v[76:79], %[vo], %[rs], %[so] offen sc1\n\t" \
  "s_add_u32 %[so], %[so], 1024\n\t" \
  "buffer_load_dwordx4 v[80:83], %[vo], %[rs], %[so] offen sc1\n\t" \
  "s_add_u32 %[so], %[so], 1024\n\t" \
  "buffer_load_dwordx4 v[84:87], %[vo], %[rs], %[so] offen sc1\n\t" \
  "s_add_u32 %[so], %[so], 1024\n\t" \
  "buffer_load_dwordx4 v[88:91], %[vo], %[rs], %[so] offen sc1\n\t" \
  "s_add_u32 %[so], %[so], 1024\n\t" \
  "buffer_load_dwordx4 v[92:95], %[vo], %[rs], %[so] offen sc1\n\t" \
  "s_add_u32 %[so], %[so], 1024\n\t" \
  "buffer_load_dwordx4 v[96:99], %[vo], %[rs], %[so] offen sc1\n\t" \
  "s_add_u32 %[so], %[so], 1024\n\t" \
  "buffer_load_dwordx4 v[100:103], %[vo], %[rs], %[so] offen sc1\n\t" \
  "s_add_u32 %[so], %[so], 1024\n\t" \
  "buffer_load_dwordx4 v[104:107], %[vo], %[rs], %[so] offen sc1\n\t" \
  "s_add_u32 %[so], %[so], 1024\n\t" \
  "buffer_load_dwordx4 v[108:111], %[vo], %[rs], %[so] offen sc1\n\t" \
  "s_add_u32 %[so], %[so], 1024\n\t" \
  "buffer_load_dwordx4 v[112:115], %[vo], %[rs], %[so] offen sc1\n\t" \
  "s_add_u32 %[so], %[so], 1024\n\t" \
  "buffer_load_dwordx4 v[116:119], %[vo], %[rs], %[so] offen sc1\n\t" \
  "s_add_u32 %[so], %[so], 1024\n\t" \
  "buffer_load_dwordx4 v[120:123], %[vo], %[rs], %[so] offen sc1\n\t" \
  "s_add_u32 %[so], %[so], 1024\n\t" \
  "buffer_load_dwordx4 v[124:127], %[vo], %[rs], %[so] offen sc1\n\t" \
  "s_add_u32 %[so], %[so], 1024\n\t" \
  "s_mov_b32 %[so], %[p1]\n\t" \
  "buffer_load_dwordx4 v[128:131], %[vo], %[rs], %[so] offen sc1\n\t" \
  "s_add_u32 %[so], %[so], 1024\n\t" \
  "buffer_load_dwordx4 v[132:135], %[vo], %[rs], %[so] offen sc1\n\t" \
  "s_add_u32 %[so], %[so], 1024\n\t" \
  "buffer_load_dwordx4 v[136:139], %[vo], %[rs], %[so] offen sc1\n\t" \
  "s_add_u32 %[so], %[so], 1024\n\t" \
  "buffer_load_dwordx4 v[140:143], %[vo], %[rs], %[so] offen sc1\n\t" \
  "s_add_u32 %[so], %[so], 1024\n\t" \
  "buffer_load_dwordx4 v[144:147], %[vo], %[rs], %[so] offen sc1\n\t" \
  "s_add_u32 %[so], %[so], 1024\n\t" \
  "buffer_load_dwordx4 v[148:151], %[vo], %[rs], %[so] offen sc1\n\t" \
  "s_add_u32 %[so], %[so], 1024\n\t" \
  "buffer_load_dwordx4 v[152:155], %[vo], %[rs], %[so] offen sc1\n\t" \
  "s_add_u32 %[so], %[so], 1024\n\t" \
  "buffer_load_dwordx4 v[156:159], %[vo], %[rs], %[so] offen sc1\n\t" \
  "s_add_u32 %[so], %[so], 1024\n\t" \
  "buffer_load_dwordx4 v[160:163], %[vo], %[rs], %[so] offen sc1\n\t" \
  "s_add_u32 %[so], %[so], 1024\n\t" \
  "s_mov_b32 %[so], %[p2]\n\t" \
  "buffer_load_dwordx4 v[164:167], %[vo], %[rs], %[so] offen sc1\n\t" \
  "s_add_u32 %[so], %[so], 1024\n\t" \
  "buffer_load_dwordx4 v[168:171], %[vo], %[rs], %[so] offen sc1\n\t" \
  "s_add_u32 %[so], %[so], 1024\n\t" \
  "buffer_load_dwordx4 v[172:175], %[vo], %[rs], %[so] offen sc1\n\t" \
  "s_add_u32 %[so], %[so], 1024\n\t" \
  "buffer_load_dwordx4 v[176:179], %[vo], %[rs], %[so] offen sc1\n\t" \
  "s_add_u32 %[so], %[so], 1024\n\t" \
  "buffer_load_dwordx4 v[180:183], %[vo], %[rs], %[so] offen sc1\n\t" \
  "s_add_u32 %[so], %[so], 1024\n\t" \
  "buffer_load_dwordx4 v[184:187], %[vo], %[rs], %[so] offen sc1\n\t" \
  "s_add_u32 %[so], %[so], 1024\n\t" \
  "buffer_load_dwordx4 v[188:191], %[vo], %[rs], %[so] offen sc1\n\t" \
  "s_add_u32 %[so], %[so], 1024\n\t" \
  "s_waitcnt vmcnt(0)\n\t" \
  "9:\n\t"
#define ATTN_FAST_CLOBBERS "v192", "v193", "v194", "v195", "v196", "v197", "v198", "v199", "v200", "v201", "v202", "v203", "v204", "v205", "v206", "v207", "v208", "v209", "v210", "v211", "v212", "v213", "v214", "v215", "v216", "v217", "v218", "v219", "v220", "v221", "v222", "v223", "v224", "v225", "v226", "v227", "v228", "v229", "v230", "v231", "v232", "v233", "v234", "v235", "v236", "v237", "v238", "v239", "v240", "v241", "v242", "v243", "v244", "v245", "v246", "v247", "v248", "v249", "v250", "v251", "vcc"

__device__ __forceinline__ void attn_main(const Params &p, const Ctx &c0, int layer, f32x16 (&o)[2], bool &fast, v16u (&pre)[8] STAMP_ARGS) {
  const Ctx c = fresh(c0);
  const int head = 4 * c.bx + (c.bj >> 3), qb = c.bj & 7, l = lane_id(), h = l >> 5, tt = qb * 8 + 2 * (c.w & 3);
  STAMP(layer, PH_ATT, 0);
  if (c.w == 0) {
    const int hp = head >> 1, cq = hp / 3, ck = (16 + hp) / 3, cv = (32 + hp) / 3;
    poll(p, c, layer * 5 + PH_QKV, l < 16 ? ck * 16 + l : l < 32 ? cv * 16 + (l & 15) : l < 34 ? cq * 16 + 2 * qb + (l & 1) : -1);
  }
  BAR();
  STAMP(layer, PH_ATT, 1);
  v4u qa, qb2; float kn;
  const unsigned skv = OFF_KV + layer * 4 * MB + head * 131072 + c.w * 2048;
  {
    const unsigned sq = OFF_QF + layer * 2 * MB + (head * 64 + tt) * 1024, sk = OFF_KN + layer * 8192 + head * 256;
#pragma unroll
    for (int i = 0; i < 2; ++i) {
      glds<true>(c.rws, l * 16, skv + i * 16384, c.lds0 + (8 * i + c.w) * KVP);
      glds<true>(c.rws, l * 16, skv + i * 16384 + 1024, c.lds0 + (8 * i + c.w) * KVP + 1024);
      if (i == 0)
        asm volatile("buffer_load_dwordx4 %0, %3, %5, %6 offen sc1\n\tbuffer_load_dwordx4 %1, %3, %5, %6 offen offset:1024 sc1\n\t"
                     "buffer_load_dword %2, %4, %5, %7 offen sc1"
                     : "=&v"(qa), "=&v"(qb2), "=&v"(kn) : "v"(l * 16), "v"(l * 4), "s"(c.rws), "s"(sq), "s"(sk) : "memory");
    }
    if (l < 16) { unsigned one = (l & 1) ? 0u : ONES2; OPAQUE_V(one); *(LAS v4u *)(c.lds + (8 * c.w + (l >> 1)) * KVP + 2048 + (l & 1) * 16) = v4u{one, one, one, one}; }
    asm volatile("s_waitcnt vmcnt(2)" : "+v"(qa), "+v"(qb2), "+v"(kn) :: "memory");
  }
  float km = max32(kn);
  km = __builtin_fmaxf(__builtin_bit_cast(float, __builtin_amdgcn_readlane(__builtin_bit_cast(int, km), 31)),
                       __builtin_bit_cast(float, __builtin_amdgcn_readlane(__builtin_bit_cast(int, km), 63)));
  const float qn = halfsum2(dot2(qa.w, qa.w, dot2(qa.z, qa.z, dot2(qa.y, qa.y, dot2(qa.x, qa.x, 0.f)))),
                            dot2(qb2.w, qb2.w, dot2(qb2.z, qb2.z, dot2(qb2.y, qb2.y, dot2(qb2.x, qb2.x, 0.f)))));
  const unsigned kb0 = c.lds0 + (c.w >> 2) * KVP;
  unsigned ka = kb0 + l * 16;
  unsigned va0 = kb0 + ((l & 16) ? ((l & 15) ? 2064 : 2048) : 1024 + h * 256 + (l & 15) * 16), va1 = va0 + ((l & 16) ? 0 : 512);
  fast = rfl(__all(qn * km <= 4096.f));
  asm volatile("" : "=v"(o[0]), "=v"(o[1]));
  unsigned p0, p1, p2;
  if (c.w < 4) { p0 = OFF_WTO + (2 * (c.bj & 7) * 32 + (c.w >> 1) * 16) * 1024; p1 = p0 + 32768; p2 = p1 + 9 * 1024; }
  else { p0 = OFF_WT1 + up_nt0(c.bj & 15) * NTP + 25 * (c.w - 4) * 1024; p1 = p0 + 16384; p2 = OFF_WT2 + ((2 * (c.bj & 7) + (c.w & 1)) * 86 + ((c.w - 4) >> 1) * 43) * 1024; }
  asm volatile("s_waitcnt vmcnt(0) lgkmcnt(0)\n\ts_barrier" ::: "memory");
  STAMP(layer, PH_ATT, 4);
  if (c.w == 4 && layer == 0)
    __builtin_amdgcn_raw_buffer_store_b64(v2u{(unsigned)c.tag, (unsigned)(c.tag >> 32)}, c.rws, (unsigned)l << 26,
                                          OFF_FLAGS + (SLOT_PREP * 256 + c.bj * 8 + c.bx) * FSTR, SC1);
  const int si = (c.w & 3) * 64 + l;
  if (!fast) {
#pragma unroll
    for (int i = 2; i < 8; ++i) {
      glds<true>(c.rws, l * 16, skv + i * 16384, c.lds0 + (8 * i + c.w) * KVP);
      glds<true>(c.rws, l * 16, skv + i * 16384 + 1024, c.lds0 + (8 * i + c.w) * KVP + 1024);
    }
    poll(p, c, SLOT_PREP, si);
    asm volatile("s_waitcnt vmcnt(0)\n\ts_barrier\n\ts_barrier" ::: "memory");
    int idx = 0xdddd;
    OPAQUE_V(idx);
#pragma unroll
    for (int j = 0; j < 2; ++j) {
      const v4u q = j ? qb2 : qa;
      f32x16 acc = {};
      float m = -__builtin_inff();
#pragma unroll 1
      for (int t = 0; t < 32; ++t) {
        const v4u kf = *(const LAS v4u *)(uintptr_t)(ka + t * 2 * KVP);
        const v4u v0 = *(const LAS v4u *)(uintptr_t)(va0 + t * 2 * KVP), v1 = *(const LAS v4u *)(uintptr_t)(va1 + t * 2 * KVP);
        f32x16 s = mfma(kf, q, f32x16{});
        float tm = s[0];
#pragma unroll
        for (int r = 1; r < 16; ++r) tm = __builtin_fmaxf(tm, s[r]);
        auto sw = __builtin_amdgcn_permlane32_swap(__builtin_bit_cast(unsigned, tm), __builtin_bit_cast(unsigned, tm), false, false);
        const unsigned sw0 = sw[0], sw1 = sw[1];
        const float mn = __builtin_fmaxf(m, __builtin_fmaxf(__uint_as_float(sw0), __uint_as_float(sw1)));
        const float al = __builtin_amdgcn_exp2f(m - mn);
        m = mn;
        v8u e0, e1;
#pragma unroll
        for (int r = 0; r < 8; ++r) {
          e0[r] = __float_as_uint(__builtin_amdgcn_exp2f(s[r] - mn));
          e1[r] = __float_as_uint(__builtin_amdgcn_exp2f(s[8 + r] - mn));
        }
#pragma unroll
        for (int r = 0; r < 16; ++r) acc[r] *= al;
        acc = __builtin_amdgcn_smfmac_f32_32x32x32_bf16(__builtin_bit_cast(bf16x8, v0), __builtin_bit_cast(bf16x16, e0), acc, idx, 0, 0);
        acc = __builtin_amdgcn_smfmac_f32_32x32x32_bf16(__builtin_bit_cast(bf16x8, v1), __builtin_bit_cast(bf16x16, e1), acc, idx, 0, 0);
      }
      acc[9] = m;
      o[j] = acc;
    }
  }
  unsigned cnt = rfl(fast), keep, so = rfl(skv + 32768), m0v = rfl(c.lds0 + (16 + c.w) * KVP), wo = rfl(OFF_WT2 + (c.bj * 8 + c.w) * 1024);
  const rsrc_t rw = mkrsrc(p.ws, layer == 0 ? WS_BYTES : OFF_WTQ);
  asm volatile(ATTN_FAST_ASM
               : [oa] "+v"(o[0]), [ob] "+v"(o[1]), [cnt] "+s"(cnt), [keep] "=&s"(keep), [so] "+s"(so), [m0v] "+s"(m0v), [wo] "+s"(wo),
                 [ka] "+v"(ka), [va0] "+v"(va0), [va1] "+v"(va1),
                 "=&{v[64:79]}"(pre[0]), "=&{v[80:95]}"(pre[1]), "=&{v[96:111]}"(pre[2]), "=&{v[112:127]}"(pre[3]),
                 "=&{v[128:143]}"(pre[4]), "=&{v[144:159]}"(pre[5]), "=&{v[160:175]}"(pre[6]), "=&{v[176:191]}"(pre[7])
               : [qa] "v"(qa), [qb] "v"(qb2), [vo] "v"(l * 16), [rs] "s"(c.rws), [rw] "s"(rw), [dummy] "s"(rfl(c.lds0 + L_DUMMY)),
                 [p0] "s"(rfl(p0)), [p1] "s"(rfl(p1)), [p2] "s"(rfl(p2)), [fo] "v"(OFF_FLAGS + (SLOT_PREP * 256 + si) * FSTR), [tag] "s"(rfl64(c.tag))
               : "memory", "scc", ATTN_FAST_CLOBBERS);
  STAMP(layer, PH_ATT, 2);
}
__device__ __forceinline__ void attn_send(const Ctx &c0, const f32x16 (&o)[2]) {
  const Ctx c = fresh(c0);
  const int l = lane_id();
  LAS float *xp = (LAS float *)(c.lds + L_APART + (c.w & 3) * 5120);
#pragma unroll
  for (int j = 0; j < 2; ++j) {
    *(LAS f32x4 *)(xp + j * 640 + l * 4) = f32x4{o[j][0], o[j][1], o[j][2], o[j][3]};
    *(LAS f32x4 *)(xp + j * 640 + 256 + l * 4) = f32x4{o[j][4], o[j][5], o[j][6], o[j][7]};
    *(LAS v2u *)(xp + j * 640 + 512 + l * 2) = v2u{__float_as_uint(o[j][8]), __float_as_uint(o[j][9])};
  }
  BAR();
}
__device__ __forceinline__ void attn_finish(const Params &p, const Ctx &c0, int layer, const f32x16 (&o)[2], bool fast STAMP_ARGS) {
  const Ctx c = fresh(c0);
  const int head = 4 * c.bx + (c.bj >> 3), qb = c.bj & 7, l = lane_id(), tt = qb * 8 + 2 * c.w;
  const LAS float *xp = (const LAS float *)(c.lds + L_APART + c.w * 5120);
  BAR();
#pragma unroll
  for (int j = 0; j < 2; ++j) {
    const f32x4 a0 = *(const LAS f32x4 *)(xp + j * 640 + l * 4), a1 = *(const LAS f32x4 *)(xp + j * 640 + 256 + l * 4);
    const v2u a2 = *(const LAS v2u *)(xp + j * 640 + 512 + l * 2);
    float f0 = 1.f, f1 = 1.f;
    if (!fast) {
      const float m1 = __uint_as_float(a2.y), mn = __builtin_fmaxf(o[j][9], m1);
      f0 = __builtin_amdgcn_exp2f(o[j][9] - mn); f1 = __builtin_amdgcn_exp2f(m1 - mn);
    }
    const float lp = o[j][8] * f0 + __uint_as_float(a2.x) * f1;
    const float inv = __builtin_amdgcn_rcpf(halfsum2(lp, lp));
    f0 *= inv; f1 *= inv;
    st16(v4u{pk2(o[j][0] * f0 + a0[0] * f1, o[j][1] * f0 + a0[1] * f1), pk2(o[j][2] * f0 + a0[2] * f1, o[j][3] * f0 + a0[3] * f1),
             pk2(o[j][4] * f0 + a1[0] * f1, o[j][5] * f0 + a1[1] * f1), pk2(o[j][6] * f0 + a1[2] * f1, o[j][7] * f0 + a1[3] * f1)},
         c.rws, l * 16, OFF_OF + layer * 2 * MB + ((tt + j) * 32 + head) * 1024);
  }
  STAMP(layer, PH_ATT, 5);
  publish(p, c, layer * 5 + PH_ATT, qb * 32 + head);
  STAMP(layer, PH_ATT, 3);
}

__global__ __launch_bounds__(512, 2) void fused_kernel(Params p) {
  extern __shared__ __attribute__((aligned(16))) char smem[];
  Ctx c;
  c.rws = mkrsrc(p.ws, WS_BYTES);
  c.lds = (LAS char *)smem;
  c.lds0 = (unsigned)(uintptr_t)c.lds;
  c.w = __builtin_amdgcn_readfirstlane(threadIdx.x >> 6);
  c.bx = blockIdx.x & 7; c.bj = blockIdx.x >> 3;
  c.tag = 0;
  if (c.w == 0) *cnt_ptr(c, lane_id()) = 0;
  if (CODEWARM && c.w == 7) {
    u64 pc; int len;
    asm volatile("s_getpc_b64 %0\n.Lwarm_from:\n\ts_mov_b32 %1, .Lwarm_to-.Lwarm_from\n\ts_max_i32 %1, %1, 0" : "=s"(pc), "=s"(len) :: "scc");
    const rsrc_t rc = mkrsrc((const void *)(pc & ~(u64)15), (unsigned)len & ~15u);
    glds<false>(rc, lane_id() * 16, c.bj * 2048, c.lds0 + L_JUNK);
    glds<false>(rc, lane_id() * 16, c.bj * 2048 + 1024, c.lds0 + L_JUNK);
  }
  BAR();
  if (c.w >= 4) helpers_start(p, c STAMP_PASS); else ln_gemm<PH_QKV, true>(p, c, 0 STAMP_PASS);
  c.tag = rfl64(c.tag);
#pragma unroll 1
  for (int layer = 0; layer < 2; ++layer) {
    f32x16 o[2];
    bool fast;
    v16u pre[8];
    attn_main(p, c, layer, o, fast, pre STAMP_PASS);
    if (c.w < 4) {
      attn_finish(p, c, layer, o, fast STAMP_PASS);
      res_gemm<PH_WO>(p, c, layer, pre STAMP_PASS);
      ln_gemm<PH_UP, false>(p, c, layer STAMP_PASS);
      res_gemm<PH_DOWN>(p, c, layer, pre STAMP_PASS);
      if (layer == 0) ln_gemm<PH_QKV, false>(p, c, 1 STAMP_PASS);
    } else {
      attn_send(c, o);
      helpers_chain(p, c, layer, pre STAMP_PASS);
    }
  }
  if (CODEWARM) asm volatile(".Lwarm_to:");
  if (blockIdx.x == 0 && c.w == 0 && lane_id() == 0) __hip_atomic_store((GAS u64 *)(p.ws + OFF_EPOCH), c.tag, __ATOMIC_RELAXED, __HIP_MEMORY_SCOPE_AGENT);
}

extern "C" void kernel_launch(void *const *d_in, const int *in_sizes, int n_in, void *d_out, int out_size, void *d_ws, size_t ws_size,
                              hipStream_t stream) {
  Params p;
  p.x = d_in[0]; p.wqkv = d_in[1]; p.wo = d_in[2]; p.w1 = d_in[4]; p.b1 = d_in[5]; p.w2 = d_in[6];
  p.out = d_out; p.ws = (char *)d_ws;
  (void)hipFuncSetAttribute((const void *)fused_kernel, hipFuncAttributeMaxDynamicSharedMemorySize, LDS_BYTES);
  fused_kernel<<<256, 512, LDS_BYTES, stream>>>(p);
}
```

```cpp
#include <hip/hip_runtime.h>
#include <hip/hip_bf16.h>
#include <stdint.h>
#include <type_traits>

#define STAMPS 0

#define LAS __attribute__((address_space(3)))
#define GAS __attribute__((address_space(1)))
typedef unsigned v4u __attribute__((ext_vector_type(4)));
typedef unsigned v2u __attribute__((ext_vector_type(2)));
typedef float f32x2 __attribute__((ext_vector_type(2)));
typedef float f32x4 __attribute__((ext_vector_type(4)));
typedef float f32x16 __attribute__((ext_vector_type(16)));
typedef __bf16 bf16x2 __attribute__((ext_vector_type(2)));
typedef __bf16 bf16x8 __attribute__((ext_vector_type(8)));
typedef __bf16 bf16x16 __attribute__((ext_vector_type(16)));
typedef short s16x4 __attribute__((ext_vector_type(4)));
typedef unsigned v8u __attribute__((ext_vector_type(8)));
typedef unsigned v16u __attribute__((ext_vector_type(16)));
typedef __amdgpu_buffer_rsrc_t rsrc_t;
typedef unsigned long long u64;

constexpr unsigned MB = 1u << 20;
constexpr unsigned OFF_EPOCH = 0;
constexpr unsigned OFF_FLAGS = 65536;
constexpr unsigned OFF_KN = 32768;
constexpr unsigned OFF_QF = MB;
constexpr unsigned OFF_KV = OFF_QF + 4 * MB;
constexpr unsigned OFF_OF = OFF_KV + 8 * MB;
constexpr unsigned OFF_X1 = OFF_OF + 4 * MB;
constexpr unsigned OFF_X2 = OFF_X1 + 4 * MB;
constexpr unsigned OFF_HF = OFF_X2 + 2 * MB;
constexpr unsigned HF_LAYER = 6 * MB;
constexpr unsigned OFF_ST = OFF_HF + 2 * HF_LAYER;
constexpr unsigned OFF_ST0 = OFF_ST + 3 * 262144;
constexpr unsigned OFF_WT1 = OFF_ST + MB;
constexpr unsigned OFF_WTO = OFF_WT1 + 43 * 33 * 1024, OFF_WT2 = OFF_WTO + 16 * 32 * 1024, OFF_WTQ = OFF_WT2 + 16 * 86 * 1024;
constexpr unsigned WS_BYTES = OFF_WTQ + 48 * 33 * 1024;
constexpr unsigned LDS_BYTES = 163840;
constexpr unsigned NTP = 33 * 1024;
constexpr unsigned L_VSCR = 3 * NTP;
constexpr unsigned L_PREP = L_VSCR + 4 * 4608;
constexpr unsigned L_XCH = 25 * 4096;
constexpr unsigned L_APART = 136 * 1024;
constexpr unsigned L_DUMMY = 135168;
constexpr unsigned L_CNT = 159744;
constexpr unsigned L_JUNK = L_CNT + 1024;
constexpr unsigned KVP = 2080;
constexpr int SC1 = 16;
#define NW_WO 16
#define REWARM 0
#define XPD 32
#define XUP 8
#define CODEWARM 1
#define FSTR 128
#define PSLEEP 1
enum { PH_QKV = 0, PH_ATT = 1, PH_WO = 2, PH_UP = 3, PH_DOWN = 4, SLOT_PREP = 10 };

struct Params {
  const void *x, *wqkv, *wo, *w1, *b1, *w2;
  void *out;
  char *ws;
};

struct Ctx {
  rsrc_t rws;
  LAS char *lds;
  unsigned lds0;
  int w;
  int bx, bj;
  u64 tag;
};

#define SB() __builtin_amdgcn_sched_barrier(0)
#define OPAQUE_V(x) asm volatile("" : "+v"(x))
__device__ __forceinline__ Ctx fresh(const Ctx &c0) { Ctx c = c0; asm volatile("" : "+s"(c.w), "+s"(c.bx), "+s"(c.bj), "+s"(c.lds0), "+s"(c.tag)); return c; }
#define BAR() asm volatile("s_waitcnt lgkmcnt(0)\n\ts_barrier" ::: "memory")
#define VMWAIT(n) asm volatile("s_waitcnt vmcnt(" #n ")" ::: "memory")
__device__ __forceinline__ unsigned rfl(unsigned v) { return (unsigned)__builtin_amdgcn_readfirstlane((int)v); }
__device__ __forceinline__ u64 rfl64(u64 v) { return (u64)rfl((unsigned)v) | ((u64)rfl((unsigned)(v >> 32)) << 32); }
__device__ __forceinline__ rsrc_t mkrsrc(const void *p, unsigned bytes) { return __builtin_amdgcn_make_buffer_rsrc((void *)p, (short)0, (int)bytes, 0x00020000); }
__device__ __forceinline__ int lane_id() { int l; asm volatile("v_mbcnt_lo_u32_b32 %0, -1, 0\n\tv_mbcnt_hi_u32_b32 %0, -1, %0" : "=v"(l)); return l; }
__device__ __forceinline__ f32x16 mfma(v4u a, v4u b, f32x16 c) {
  return __builtin_amdgcn_mfma_f32_32x32x16_bf16(__builtin_bit_cast(bf16x8, a), __builtin_bit_cast(bf16x8, b), c, 0, 0, 0);
}
__device__ __forceinline__ unsigned pk2(float lo, float hi) { bf16x2 v = {(__bf16)lo, (__bf16)hi}; return __builtin_bit_cast(unsigned, v); }
__device__ __forceinline__ float dot2(unsigned a, unsigned b, float c) {
  return __builtin_amdgcn_fdot2_f32_bf16(__builtin_bit_cast(bf16x2, a), __builtin_bit_cast(bf16x2, b), c, false);
}
constexpr unsigned ONES2 = 0x3f803f80u;
__device__ __forceinline__ float bf_lo(unsigned u) { return __builtin_bit_cast(float, u << 16); }
__device__ __forceinline__ float bf_hi(unsigned u) { return __builtin_bit_cast(float, u & 0xffff0000u); }
__device__ __forceinline__ float halfsum2(float a, float b) {
  auto r = __builtin_amdgcn_permlane32_swap(__builtin_bit_cast(unsigned, a), __builtin_bit_cast(unsigned, b), false, false);
  const unsigned r0 = r[0], r1 = r[1];
  return __uint_as_float(r0) + __uint_as_float(r1);
}
template <int CTRL, int RMASK> __device__ __forceinline__ float dppmax(float v) {
  const int o = __builtin_amdgcn_update_dpp(0, __builtin_bit_cast(int, v), CTRL, RMASK, 0xf, true);
  return __builtin_fmaxf(v, __builtin_bit_cast(float, o));
}
template <int CTRL, int RMASK> __device__ __forceinline__ float dppadd(float v) {
  return v + __builtin_bit_cast(float, __builtin_amdgcn_update_dpp(0, __builtin_bit_cast(int, v), CTRL, RMASK, 0xf, true));
}
__device__ __forceinline__ float sum32(float v) {
  v = dppadd<0x111, 0xf>(v); v = dppadd<0x112, 0xf>(v); v = dppadd<0x114, 0xf>(v); v = dppadd<0x118, 0xf>(v);
  return dppadd<0x142, 0xa>(v);
}
__device__ __forceinline__ float max32(float v) {
  v = dppmax<0x111, 0xf>(v); v = dppmax<0x112, 0xf>(v); v = dppmax<0x114, 0xf>(v); v = dppmax<0x118, 0xf>(v);
  return dppmax<0x142, 0xa>(v);
}
template <bool DEV> __device__ __forceinline__ void glds(rsrc_t rs, unsigned voff, unsigned soff, unsigned ldsaddr) {
  unsigned keep;
  if constexpr (DEV)
    asm volatile("s_mov_b32 %0, m0\n\ts_mov_b32 m0, %1\n\ts_nop 2\n\tbuffer_load_dwordx4 %2, %3, %4 offen sc1 lds\n\ts_mov_b32 m0, %0"
                 : "=&s"(keep) : "s"(ldsaddr), "v"(voff), "s"(rs), "s"(soff) : "memory");
  else
    asm volatile("s_mov_b32 %0, m0\n\ts_mov_b32 m0, %1\n\ts_nop 2\n\tbuffer_load_dwordx4 %2, %3, %4 offen lds\n\ts_mov_b32 m0, %0"
                 : "=&s"(keep) : "s"(ldsaddr), "v"(voff), "s"(rs), "s"(soff) : "memory");
}
__device__ __forceinline__ v2u trd(const LAS char *p) {
  return __builtin_bit_cast(v2u, __builtin_amdgcn_ds_read_tr16_b64_v4i16((LAS s16x4 *)p));
}
__device__ __forceinline__ v4u trfrag(const LAS char *p0, const LAS char *p1) { const v2u a = trd(p0), b = trd(p1); return v4u{a.x, a.y, b.x, b.y}; }
__device__ __forceinline__ v4u ld16(rsrc_t rs, unsigned voff, unsigned soff) { return __builtin_amdgcn_raw_buffer_load_b128(rs, voff, soff, SC1); }
template <int AUX = SC1> __device__ __forceinline__ void st16(v4u v, rsrc_t rs, unsigned voff, unsigned soff) {
  __builtin_amdgcn_raw_buffer_store_b128(v, rs, voff, soff, AUX);
  asm volatile("s_nop 1" :: "v"(v) : "memory");
}

#define STAMP(layer, ph, i) do {} while (0)
#define STAMP_ARGS
#define STAMP_PASS

__device__ __forceinline__ GAS u64 *flag_ptr(const Params &p, int slot, int idx) { return (GAS u64 *)(p.ws + OFF_FLAGS + (slot * 256 + idx) * FSTR); }
__device__ __forceinline__ LAS unsigned *cnt_ptr(const Ctx &c, int i) { return (LAS unsigned *)(c.lds + L_CNT) + i; }
__device__ __forceinline__ void publish(const Params &p, const Ctx &c, int slot, int idx) {
  unsigned old, t1;
  asm volatile("v_mov_b32 %0, %2\n\tv_mov_b32 %1, 1\n\ts_waitcnt vmcnt(0)\n\ts_mov_b64 exec, 1\n\tds_add_rtn_u32 %0, %0, %1\n\t"
               "s_mov_b64 exec, -1\n\ts_waitcnt lgkmcnt(0)" : "=&v"(old), "=&v"(t1) : "s"(c.lds0 + L_CNT + 4 * slot) : "memory");
  if (rfl(old) == 3)
    __builtin_amdgcn_raw_buffer_store_b64(v2u{(unsigned)c.tag, (unsigned)(c.tag >> 32)}, c.rws, (unsigned)lane_id() << 26,
                                          OFF_FLAGS + (slot * 256 + idx) * FSTR, SC1);
}
__device__ __forceinline__ void poll(const Params &p, const Ctx &c, int slot, int idx  ) {
  GAS u64 *f = flag_ptr(p, slot, idx);
  bool wait = idx >= 0;
#pragma unroll 1
  for (unsigned spins = 0; spins < (1u << 20); ++spins) {
    if (wait) wait = __hip_atomic_load(f, __ATOMIC_RELAXED, __HIP_MEMORY_SCOPE_AGENT) != c.tag;
    if (!__any(wait)) break;
    __builtin_amdgcn_s_sleep(PSLEEP);
  }
  __builtin_amdgcn_fence(__ATOMIC_ACQUIRE, "wavefront");
}
__device__ __forceinline__ void lds_arrive(const Ctx &c, int i) {
  unsigned t0, t1;
  asm volatile("v_mov_b32 %0, %2\n\tv_mov_b32 %1, 1\n\ts_mov_b64 exec, 1\n\tds_add_u32 %0, %1\n\ts_mov_b64 exec, -1"
               : "=&v"(t0), "=&v"(t1) : "s"(c.lds0 + L_CNT + 4 * i) : "memory");
}
__device__ __forceinline__ void lds_wait(const Ctx &c, int i, unsigned n) {
#pragma unroll 1
  while (rfl(__hip_atomic_load(cnt_ptr(c, i), __ATOMIC_RELAXED, __HIP_MEMORY_SCOPE_WORKGROUP)) < n) __builtin_amdgcn_s_sleep(1);
  asm volatile("" ::: "memory");
}

__device__ __forceinline__ int up_nt0(int cb) { return cb < 11 ? 3 * cb : 33 + 2 * (cb - 11); }
__device__ __forceinline__ v2u aug_quad(float cs, unsigned bias_bits) {
  const unsigned hi = pk2(cs, 0.f) & 0xffffu;
  return v2u{pk2(cs, cs - bf_lo(hi)), hi | (bias_bits << 16)};
}
template <int I> __device__ __forceinline__ v4u frag(const v16u (&pre)[8]) {
  return __builtin_shufflevector(pre[I >> 2], pre[I >> 2], 4 * (I & 3), 4 * (I & 3) + 1, 4 * (I & 3) + 2, 4 * (I & 3) + 3);
}
template <int N, class F> __device__ __forceinline__ void static_for(F f) {
  if constexpr (N > 0) { static_for<N - 1>(f); f(std::integral_constant<int, N - 1>{}); }
}
template <bool FIRST> __device__ __forceinline__ void ln_tile(const Ctx &c, int &rb, int &cb) {
  if (FIRST) { rb = 4 * (c.bx >> 1) + (c.bj >> 3); cb = 8 * (c.bx & 1) + (c.bj & 7); }
  else { rb = 2 * c.bx + (c.bj >> 4); cb = c.bj & 15; }
}

__device__ __forceinline__ void helpers_start(const Params &p, Ctx &c STAMP_ARGS) {
  const int l = lane_id(), hj = c.w - 4, id = blockIdx.x, h = l >> 5;
  int rb, cb;
  ln_tile<true>(c, rb, cb);
  v4u r0, r1; v2u ep;
  asm volatile("s_nop 4\n\tbuffer_load_dwordx4 %0, %3, %4, %5 offen\n\tbuffer_load_dwordx4 %1, %3, %4, %5 offen offset:1024\n\t"
               "buffer_load_dwordx2 %2, off, %6, 0 sc1"
               : "=&v"(r0), "=&v"(r1), "=&v"(ep) : "v"(l * 16), "s"(mkrsrc(p.x, 2048 * 1024)), "s"((id * 8 + 2 * hj) * 1024), "s"(c.rws) : "memory");
  const rsrc_t rq = mkrsrc(p.wqkv, 512 * 3072);
  unsigned voff[3];
#pragma unroll
  for (int i = 0; i < 3; ++i) { const int u = 64 * i + l, row = (u * 43691) >> 19; voff[i] = row * 3072 + (u - 12 * row) * 16; }
#define STG(i) _Pragma("unroll") for (int t = 0; t < 3; ++t) glds<false>(rq, voff[t], (4 * (i) + hj) * 16 * 3072 + cb * 192, c.lds0 + (4 * (i) + hj) * 3072 + t * 1024)
  STG(0); STG(1); STG(2); STG(3);
  asm volatile("s_waitcnt vmcnt(12)" : "+v"(r0), "+v"(r1), "+v"(ep) :: "memory");
  c.tag = rfl64(((u64)ep.y << 32 | ep.x) + 1);
  const float t1 = sum32(halfsum2(dot2(r0.w, ONES2, dot2(r0.z, ONES2, dot2(r0.y, ONES2, dot2(r0.x, ONES2, 0.f)))),
                                  dot2(r1.w, ONES2, dot2(r1.z, ONES2, dot2(r1.y, ONES2, dot2(r1.x, ONES2, 0.f))))));
  const float t2 = sum32(halfsum2(dot2(r0.w, r0.w, dot2(r0.z, r0.z, dot2(r0.y, r0.y, dot2(r0.x, r0.x, 0.f)))),
                                  dot2(r1.w, r1.w, dot2(r1.z, r1.z, dot2(r1.y, r1.y, dot2(r1.x, r1.x, 0.f))))));
  st16(v4u{__float_as_uint(t1), (unsigned)c.tag, __float_as_uint(t2), (unsigned)(c.tag >> 32)}, c.rws, (unsigned)(~l & 31) << 26 | h * 16,
       OFF_ST0 + (id * 8 + 2 * hj) * 16);
  const void *sw; unsigned pitch, rows, np, kb0, dst, tp;
  if (id < 48) { sw = p.wqkv; pitch = 3072; rows = 512; np = id >> 1; kb0 = (id & 1) * 16; dst = OFF_WTQ; tp = NTP; }
  else if (id < 92) { sw = p.w1; pitch = 2752; rows = 512; np = (id - 48) >> 1; kb0 = (id & 1) * 16; dst = OFF_WT1; tp = NTP; }
  else if (id < 108) { sw = p.wo; pitch = 1024; rows = 512; np = (id - 92) >> 1; kb0 = (id & 1) * 16; dst = OFF_WTO; tp = 32768; }
  else { sw = p.w2; pitch = 1024; rows = id < 156 ? 1376 : 0; np = (id - 108) & 7; kb0 = ((id - 108) >> 3) * 16; dst = OFF_WT2; tp = 86 * 1024; }
  sw = (const void *)rfl64((u64)sw); pitch = rfl(pitch); rows = rfl(rows); np = rfl(np); kb0 = rfl(kb0); dst = rfl(dst); tp = rfl(tp);
  const v4u ones = {ONES2, ONES2, ONES2, ONES2};
  const LAS char *blk = c.lds + hj * 3072 + (8 * h + ((l & 15) >> 2)) * 192 + ((l >> 4) & 1) * 32 + (l & 3) * 8;
  f32x16 cs[3] = {};
#define CSUM(i) _Pragma("unroll") for (int u = 0; u < 6; ++u) { const LAS char *q = blk + (2 * (i) + u / 3) * 12288 + (u % 3) * 64; cs[u % 3] = mfma(ones, trfrag(q, q + 768), cs[u % 3]); }
  VMWAIT(7); lds_arrive(c, 52);
  STG(4); STG(5);
  CSUM(0);
  VMWAIT(6); lds_arrive(c, 53);
  STG(6); STG(7);
  CSUM(1);
  VMWAIT(6); lds_arrive(c, 54);
  {
    const rsrc_t rw = mkrsrc(sw, rows * pitch);
    const unsigned vo = (l >> 3) * pitch + ((l & 7) ^ ((l >> 2) & 4)) * 16;
#pragma unroll
    for (int i = 0; i < 8; ++i) glds<false>(rw, vo, (kb0 * 16 + 64 * hj + 8 * i) * pitch + np * 128, c.lds0 + L_PREP + (8 * hj + i) * 1024);
  }
  __builtin_amdgcn_s_setprio(2);
  VMWAIT(8); lds_arrive(c, 55);
  STAMP(0, PH_QKV, 0);
  CSUM(2);
  CSUM(3);
#undef CSUM
#undef STG
#pragma unroll
  for (int t = 0; t < 3; ++t)
    *(LAS v2u *)(c.lds + 32 * 3072 + t * 1024 + ((l & 31) + 32 * (hj >> 1)) * 16 + (hj & 1) * 8) = aug_quad(cs[t][0], 0);
  lds_arrive(c, 56);
  __builtin_amdgcn_s_setprio(0);
  STAMP(0, PH_QKV, 1);
  if (id < 156) {
    const unsigned nt = 2 * np + (hj & 1), kb = kb0 + 8 * (hj >> 1), q = (kb >> 3) & 3;
    unsigned bias = 0;
    if (id >= 48 && id < 92 && q < 2 && nt < 43) bias = ((const GAS unsigned short *)p.b1)[nt * 32 + (l & 31)];
    VMWAIT(0); lds_arrive(c, 58);
    lds_wait(c, 58, 4);
    const LAS char *sp = c.lds + L_PREP + (hj >> 1) * 16384 + (4 * h + ((l & 15) >> 2)) * 128 + (((hj & 1) * 64) ^ ((l & 8) << 3)) + ((l >> 4) & 1) * 32 + (l & 3) * 8;
    f32x16 ss = {};
    v4u f[8];
#pragma unroll
    for (int i = 0; i < 8; ++i) { f[i] = trfrag(sp + i * 2048, sp + i * 2048 + 1024); ss = mfma(ones, f[i], ss); }
    v2u aq = aug_quad(ss[0], bias);
    OPAQUE_V(aq);
    const unsigned d0 = dst + nt * tp, off = id >= 48 && id < 92 && nt == 43 ? 1u << 31 : 0;
    lds_wait(c, PH_QKV, 4);
#pragma unroll
    for (int i = 0; i < 8; ++i) st16(f[i], c.rws, (kb + i < 86 ? off : 1u << 31) | l * 16, d0 + (kb + i) * 1024);
    if (id < 92) __builtin_amdgcn_raw_buffer_store_b64(aq, c.rws, off | (unsigned)h << 30 | ((l + 32 * (q >> 1)) * 16 + (q & 1) * 8), d0 + 32768, SC1);
  }
  STAMP(0, PH_QKV, 3);
}

__device__ __forceinline__ void store_stats(const Ctx &c, const unsigned (&o)[8], int l, unsigned st_tt, int g) {
  float s1 = 0.f, s2 = 0.f, s1b = 0.f, s2b = 0.f;
#pragma unroll
  for (int i = 0; i < 8; i += 2) { s1 = dot2(o[i], ONES2, s1); s2 = dot2(o[i], o[i], s2); s1b = dot2(o[i + 1], ONES2, s1b); s2b = dot2(o[i + 1], o[i + 1], s2b); }
  __builtin_amdgcn_raw_buffer_store_b32(__float_as_uint(halfsum2(s1 + s1b, s2 + s2b)), c.rws, l * 4, st_tt + g * 256, SC1);
}

template <int PH, bool FIRST> __device__ __forceinline__ void ln_gemm(const Params &p, Ctx &c0, int layer STAMP_ARGS) {
  Ctx c = fresh(c0);
  int rb, cb;
  ln_tile<FIRST>(c, rb, cb);
  const int nt0 = PH == PH_QKV ? 3 * cb : up_nt0(cb);
  const int l = lane_id(), h = l >> 5, tt = rb * 4 + c.w;
  constexpr int PD = 11;
  v4u a[32];
  float st[16], rstd = 0.f, sp = 0.f, nm = 0.f;
  const unsigned base = (PH == PH_QKV ? OFF_X2 : OFF_X1 + layer * 2 * MB) + tt * 32768;
#define LDA(ks) a[ks] = ld16(c.rws, l * 16 + ((ks) & 3) * 1024, base + ((ks) >> 2) * 4096)
  STAMP(layer, PH, 0);
  [[maybe_unused]] LAS char *sb = c.lds + L_VSCR + c.w * 4608;
  [[maybe_unused]] const unsigned w0 = (l >> 3) * 128 + ((l & 7) ^ (l >> 4)) * 16, r0 = (l & 31) * 128 + (h ^ ((l >> 1) & 7)) * 16;
  [[maybe_unused]] u64 e = 0;
  [[maybe_unused]] v4u g = {};
  [[maybe_unused]] const rsrc_t rx = mkrsrc(p.x, 2048 * 1024);
#define LDX(ks) a[ks] = __builtin_amdgcn_raw_buffer_load_b128(rx, (l >> 3) * 1024 + (l & 7) * 16 + ((ks) >> 2) * 128, tt * 32768 + ((ks) & 3) * 8192, 0)
#define XW(ch, i) *(LAS v4u *)(sb + (i) * 1024 + (w0 ^ (((i) & 1) * 64))) = a[4 * (ch) + (i)]
#define XR(ch, i) a[4 * (ch) + (i)] = *(const LAS v4u *)(sb + (r0 ^ ((i) * 32)))
  if constexpr (FIRST) {
#pragma unroll
    for (int ks = 0; ks < XPD; ++ks) LDX(ks);
    e = __hip_atomic_load((GAS u64 *)(p.ws + OFF_EPOCH), __ATOMIC_RELAXED, __HIP_MEMORY_SCOPE_AGENT) + 1;
    SB();
#pragma unroll
    for (int ch = 0; ch < XUP; ++ch) {
#pragma unroll
      for (int i = 0; i < 4; ++i) XW(ch, i);
      asm volatile("" ::: "memory");
#pragma unroll
      for (int i = 0; i < 4; ++i) XR(ch, i);
      asm volatile("" ::: "memory");
    }
    STAMP(layer, PH, 4);
    lds_wait(c, 52, 4);
    STAMP(layer, PH, 1);
  } else {
    if (c.w == 0) poll(p, c, PH == PH_QKV ? PH_DOWN : layer * 5 + PH_WO, l < 16 ? rb * 16 + l : -1);
    STAMP(layer, PH, 4);
    BAR();
    STAMP(layer, PH, 1);
#pragma unroll
    for (int ks = 0; ks < PD; ++ks) LDA(ks);
  }
  SB();
  const LAS char *b0 = c.lds + (FIRST ? (8 * h + ((l & 15) >> 2)) * 192 + ((l >> 4) & 1) * 32 + (l & 3) * 8 : l * 16);
  const LAS char *b2 = b0 + (FIRST ? 20 * 3072 : 2 * NTP - 4096);
  OPAQUE_V(b2);
  f32x16 acc[3] = {};
  v4u wf[3][3], aug = {};
  auto WF = [&](int ks, int nt) -> v4u {
    if (FIRST) {
      if (ks == 32) return *(const LAS v4u *)(c.lds + 32 * 3072 + nt * 1024 + l * 16);
      const LAS char *q = (ks >= 20 ? b2 + (ks - 20) * 3072 : b0 + ks * 3072) + nt * 64;
      return trfrag(q, q + 768);
    }
    return *(const LAS v4u *)(nt == 2 ? b2 + (ks + 4) * 1024 : nt == 1 && ks > 30 ? b2 + (ks - 29) * 1024 : b0 + nt * NTP + ks * 1024);
  };
#pragma unroll
  for (int ks = 0; ks < 2; ++ks)
#pragma unroll
    for (int nt = 0; nt < 3; ++nt) wf[ks][nt] = WF(ks, nt);
  SB();
#pragma unroll
  for (int ks = 0; ks < 33; ++ks) {
#pragma unroll
    for (int nt = 0; nt < 3; ++nt) {
      if (FIRST && nt == 0 && (ks == 6 || ks == 14 || ks == 22 || ks == 27 || ks == 30)) { OPAQUE_V(acc[0]); OPAQUE_V(acc[1]); OPAQUE_V(acc[2]); }
      if (FIRST && nt == 0 && (ks == 6 || ks == 14 || ks == 22)) { lds_wait(c, 53 + (ks >> 3), 4); SB(); }
      if (FIRST && nt == 0 && ks == 30) { lds_wait(c, 56, 4); SB(); }
      if (FIRST && nt == 0 && (ks == 7 || ks == 15 || ks == 23 || ks == 27)) { STAMP(2, 0, ks == 27 ? 3 : ks >> 3); SB(); }
      if (ks + 2 < 33) wf[(ks + 2) % 3][nt] = WF(ks + 2, nt);
      acc[nt] = mfma(wf[ks % 3][nt], ks < 32 ? a[ks] : aug, acc[nt]);
      if constexpr (FIRST) {
        const int ch = (ks >> 2) + XUP, u = 3 * (ks & 3) + nt;
        if (ch < 8 && u >= 2 && u < 6) { XW(ch, u - 2); asm volatile("" ::: "memory"); }
        if (ch < 8 && u >= 6 && u < 10) { XR(ch, u - 6); asm volatile("" ::: "memory"); }
        if (3 * ks + nt + XPD < 32) LDX(3 * ks + nt + XPD);
        if (ks == 12 && nt == 0) g = ld16(c.rws, (l & 31) * 16, OFF_ST0 + tt * 512);
        if (ks == 27 && nt == 0) {
          c0.tag = c.tag = rfl64(e);
#pragma unroll 1
          for (unsigned spins = 0; spins < (1u << 20); ++spins) {
            if (__builtin_expect(__all(g.y == (unsigned)c.tag && g.w == (unsigned)(c.tag >> 32)), 1)) break;
            g = ld16(c.rws, (l & 31) * 16, OFF_ST0 + tt * 512);
          }
          sp = __uint_as_float(h ? g.z : g.x);
        }
      } else {
        if (nt == 1 && ks + PD < 32) LDA(ks + PD);
        if (PH == PH_UP && nt == 2 && ks + PD == 31) lds_arrive(c, 40 + layer * 5 + PH);
        if (nt == 2 && ks >= 8 && ks < 16) {
#pragma unroll
          for (int g = 2 * (ks - 8); g < 2 * (ks - 7); ++g)
            st[g] = __uint_as_float(__builtin_amdgcn_raw_buffer_load_b32(c.rws, l * 4 + g * 256, OFF_ST + (PH == PH_UP ? layer : 2) * 262144 + tt * 4096, SC1));
        }
        if (ks == 27) { if (nt == 0) sp = (st[0] + st[1]) + (st[2] + st[3]); else sp += (st[4 * nt] + st[4 * nt + 1]) + (st[4 * nt + 2] + st[4 * nt + 3]); OPAQUE_V(sp); }
        if (ks == 28 && nt == 0) { sp += (st[12] + st[13]) + (st[14] + st[15]); OPAQUE_V(sp); }
      }
      if (ks == 28 && nt == 1) {
        auto sw = __builtin_amdgcn_permlane32_swap(__float_as_uint(sp), __float_as_uint(sp), false, false);
        const unsigned u1 = sw[0], u2 = sw[1];
        nm = __uint_as_float(u1) * (-1.f / 512.f); sp = __uint_as_float(u2) * (1.f / 512.f);
        OPAQUE_V(nm); OPAQUE_V(sp);
      }
      if (ks == 28 && nt == 2) { sp = __builtin_fmaxf(sp - nm * nm, 0.f) + 1e-5f; rstd = __builtin_amdgcn_rsqf(sp); OPAQUE_V(sp); OPAQUE_V(rstd); }
      if (ks == 29 && nt == 0) { sp *= rstd; aug.x = pk2(nm, nm); aug.y = pk2(0.f, sp); OPAQUE_V(sp); OPAQUE_V(aug); }
      if (ks == 29 && nt == 1) { aug.w = pk2(nm - bf_lo(aug.x), sp - bf_hi(aug.y)); aug.y |= aug.w & 0xffffu; aug.z = aug.x; OPAQUE_V(aug); }
      SB();
    }
  }
#undef LDA
#undef XW
#undef XR
#undef LDX
  if (PH == PH_UP) lds_arrive(c, 16 + layer * 5 + PH);
  STAMP(layer, PH, 2);
  if constexpr (PH == PH_QKV) {
    auto scaled = [&](int nt, float rs, v4u &wa, v4u &wb) {
      unsigned o[8];
#pragma unroll
      for (int i = 0; i < 8; ++i) { const f32x2 v = f32x2{acc[nt][2 * i], acc[nt][2 * i + 1]} * rs; o[i] = pk2(v.x, v.y); }
      wa = v4u{o[0], o[1], o[2], o[3]}; wb = v4u{o[4], o[5], o[6], o[7]};
    };
    auto qk = [&](int nt) {
      const int NT = nt0 + nt, head = 2 * (NT & 15);
      const bool isq = NT < 16;
      v4u wa, wb;
      scaled(nt, isq ? rstd * (0.25f * 1.4426950408889634f) : rstd, wa, wb);
      const unsigned so = isq ? OFF_QF + layer * 2 * MB + (head * 64 + tt) * 1024 : OFF_KV + layer * 4 * MB + (head * 64 + tt) * 2048;
      st16(wa, c.rws, l * 16, so); st16(wb, c.rws, l * 16, so + (isq ? 65536 : 131072));
      const float na = dot2(wa.w, wa.w, dot2(wa.z, wa.z, dot2(wa.y, wa.y, dot2(wa.x, wa.x, 0.f))));
      const float nb = dot2(wb.w, wb.w, dot2(wb.z, wb.z, dot2(wb.y, wb.y, dot2(wb.x, wb.x, 0.f))));
      __builtin_amdgcn_raw_buffer_store_b32(__float_as_uint(max32(halfsum2(na, nb))), c.rws, (unsigned)(~l & 31) << 26 | h * 256,
                                            isq ? 1u << 31 : OFF_KN + layer * 8192 + (head * 64 + tt) * 4, SC1);
    };
    LAS char *vs = c.lds + L_VSCR + c.w * 4608;
    auto vput = [&](int nt) {
      v4u wa, wb;
      scaled(nt, rstd, wa, wb);
      *(LAS v4u *)(vs + (nt & 1) * 2304 + (l & 31) * 32 + h * 16) = wa;
      *(LAS v4u *)(vs + (nt & 1) * 2304 + 1152 + (l & 31) * 32 + h * 16) = wb;
      asm volatile("" ::: "memory");
    };
    auto vget = [&](int nt) {
      const LAS char *rp = vs + (nt & 1) * 2304 + ((l >> 4) & 1) * 1152 + (8 * h + ((l & 15) >> 2)) * 32 + (l & 3) * 8;
      const v4u f0 = trfrag(rp, rp + 128), f1 = trfrag(rp + 512, rp + 640);
      const int i = l & 15, d = (i & 3) | ((i & 4) << 1) | ((i & 8) >> 1);
      const unsigned vo = ((l >> 4) & 1) * 131072 + h * 256 + d * 16, so = OFF_KV + layer * 4 * MB + (2 * ((nt0 + nt) & 15) * 64 + tt) * 2048 + 1024;
      st16(f0, c.rws, vo, so); st16(f1, c.rws, vo, so + 512);
    };
    if (cb < 10) { qk(0); qk(1); qk(2); }
    else if (cb == 10) { vput(2); qk(0); vget(2); qk(1); }
    else { vput(0); vput(1); vget(0); vput(2); vget(1); vget(2); }
  } else {
#pragma unroll
    for (int nt = 0; nt < 3; ++nt) {
      const int NT = nt0 + nt;
      unsigned o[8];
      const float rl = rstd * 1.4426950408889634f;
      const f32x2 one = {1.f, 1.f}, rs2 = {rstd, rstd}, rm2 = {-2.f * rstd, -2.f * rstd};
#pragma unroll
      for (int i = 0; i < 8; ++i) {
        const f32x2 x = {acc[nt][2 * i], acc[nt][2 * i + 1]}, hl = x * rl;
        const f32x2 t = f32x2{__builtin_amdgcn_exp2f(hl.x), __builtin_amdgcn_exp2f(hl.y)} + one;
        const f32x2 d = __builtin_elementwise_fma(t, t, one);
        const f32x2 v = x * __builtin_elementwise_fma(f32x2{__builtin_amdgcn_rcpf(d.x), __builtin_amdgcn_rcpf(d.y)}, rm2, rs2);
        o[i] = pk2(v.x, v.y);
      }
      if (nt < 2 || cb < 11) {
        const unsigned so = OFF_HF + layer * HF_LAYER + (tt * 86 + 2 * NT) * 1024;
        st16(v4u{o[0], o[1], o[2], o[3]}, c.rws, l * 16, so); st16(v4u{o[4], o[5], o[6], o[7]}, c.rws, l * 16, so + 1024);
      }
    }
  }
  STAMP(layer, PH, 5);
  publish(p, c, layer * 5 + PH, PH == PH_QKV ? cb * 16 + rb : rb * 16 + cb);
  if (REWARM && PH == PH_UP) {
#pragma unroll
    for (int i = 0; i < 6; ++i)
      if (i < 4 || cb < 11) glds<true>(c.rws, l * 16, OFF_HF + layer * HF_LAYER + (tt * 86 + 2 * nt0 + i) * 1024, c.lds0 + L_JUNK);
  }
  STAMP(layer, PH, 3);
}

template <int PH> __device__ __forceinline__ void res_gemm(const Params &p, const Ctx &c0, int layer, const v16u (&pre)[8] STAMP_ARGS) {
  const Ctx c = fresh(c0);
  constexpr int KB = PH == PH_WO ? 32 : 86, KH = KB / 2;
  const int rb = 4 * c.bx + (c.bj >> 3), cb = c.bj & 7;
  const int l = lane_id(), h = l >> 5, kh = c.w >> 1, tt = rb * 2 + (c.w & 1);
  const unsigned fm = (tt * 32 + cb * 4 + kh * 2) * 1024;
  STAMP(layer, PH, 0);
  v4u res[2];
  auto load_res = [&]() {
    if (PH == PH_WO && layer == 0) {
      const rsrc_t rx = mkrsrc((const void *)rfl64((u64)p.x), 2048 * 1024);
      const unsigned voff = (l & 31) * 1024 + h * 8, so = tt * 32768 + cb * 128 + kh * 64;
      const v2u r0 = __builtin_amdgcn_raw_buffer_load_b64(rx, voff, so, 0), r1 = __builtin_amdgcn_raw_buffer_load_b64(rx, voff + 16, so, 0);
      const v2u r2 = __builtin_amdgcn_raw_buffer_load_b64(rx, voff + 32, so, 0), r3 = __builtin_amdgcn_raw_buffer_load_b64(rx, voff + 48, so, 0);
      res[0] = v4u{r0.x, r0.y, r1.x, r1.y}; res[1] = v4u{r2.x, r2.y, r3.x, r3.y};
    } else {
      const unsigned so = (PH == PH_WO ? OFF_X2 : OFF_X1 + layer * 2 * MB) + fm;
      res[0] = ld16(c.rws, l * 16, so); res[1] = ld16(c.rws, l * 16 + 1024, so);
    }
  };
  load_res();
  if (PH == PH_WO) BAR();
  if (c.w == 0) { if (PH == PH_WO) { lds_wait(c, layer * 5 + PH_ATT, 4); poll(p, c, layer * 5 + PH_ATT, l < 32 ? (rb >> 2) * 32 + l : -1); } else poll(p, c, layer * 5 + PH_UP, l < 16 ? (rb >> 1) * 16 + l : -1); }
  STAMP(layer, PH, 4);
  if constexpr (PH == PH_WO) { if (c.w == 0) lds_arrive(c, 57); else lds_wait(c, 57, layer + 1); } else BAR();
  STAMP(layer, PH, 1);
  v4u a[KH];
  const unsigned base = (PH == PH_WO ? OFF_OF + layer * 2 * MB : OFF_HF + layer * HF_LAYER) + (tt * KB + kh * KH) * 1024;
#define LDA(ks) a[ks] = ld16(c.rws, l * 16 + ((ks) & 3) * 1024, base + ((ks) >> 2) * 4096)
  constexpr int PD = PH == PH_WO ? 16 : 13;
#pragma unroll
  for (int ks = 0; ks < PD; ++ks) LDA(ks);
  if (PH == PH_WO) lds_arrive(c, 40 + layer * 5 + PH);
  SB();
  f32x16 acc[2] = {};
  if constexpr (PH == PH_WO) {
    static_for<16>([&](auto I) {
      constexpr int ks = I.value;
      acc[0] = mfma(frag<ks>(pre), a[ks], acc[0]);
      if constexpr (PD + 2 * ks < KH) LDA(PD + 2 * ks);
      SB();
      acc[1] = mfma(frag<16 + ks>(pre), a[ks], acc[1]);
      if constexpr (PD + 2 * ks + 1 < KH) LDA(PD + 2 * ks + 1);
      SB();
    });
  } else {
    const LAS char *b0 = c.lds + kh * 2048 + l * 16, *b1 = b0 + 65536, *b2 = b0 + 131072;
    OPAQUE_V(b1); OPAQUE_V(b2);
    v4u wf[4][2];
#define SLOT(ks) ((ks) < 39 ? (ks) : (ks) - 39)
#define WF(ks, nt) (*(const LAS v4u *)((SLOT(ks) >= 32 ? b2 : SLOT(ks) >= 16 ? b1 : b0) + (SLOT(ks) & 15) * 4096 + (nt) * 1024))
#pragma unroll
    for (int ks = 0; ks < 3; ++ks)
#pragma unroll
      for (int nt = 0; nt < 2; ++nt) wf[ks][nt] = WF(ks, nt);
    SB();
#pragma unroll
    for (int ks = 0; ks < KH; ++ks) {
      if (ks == 4 || ks == 36) { asm volatile("s_barrier" ::: "memory"); SB(); }
#pragma unroll
      for (int nt = 0; nt < 2; ++nt) {
        if (ks + 3 < KH) wf[(ks + 3) % 4][nt] = WF(ks + 3, nt);
        acc[nt] = mfma(wf[ks % 4][nt], a[ks], acc[nt]);
        if (nt == 1 && PD + ks < KH) LDA(PD + ks);
        if (nt == 1 && PD + ks == KH - 1) lds_arrive(c, 40 + layer * 5 + PH);
        SB();
      }
    }
#undef WF
#undef SLOT
    lds_arrive(c, 16 + layer * 5 + PH);
  }
#undef LDA
  constexpr unsigned XCH = L_XCH;
  const unsigned tok = layer * 2 + (PH == PH_WO ? 1 : 2);
  {
    const f32x16 s = kh ? acc[0] : acc[1];
    LAS f32x4 *xw = (LAS f32x4 *)(c.lds + XCH + c.w * 4096 + l * 16);
#pragma unroll
    for (int g = 0; g < 4; ++g) xw[g * 64] = f32x4{s[4 * g], s[4 * g + 1], s[4 * g + 2], s[4 * g + 3]};
  }
  lds_arrive(c, 32 + c.w);
  const f32x16 m = kh ? acc[1] : acc[0];
  f32x2 own[8];
#pragma unroll
  for (int i = 0; i < 8; ++i) { const unsigned r = res[i >> 2][i & 3]; own[i] = f32x2{m[2 * i], m[2 * i + 1]} + f32x2{bf_lo(r), bf_hi(r)}; OPAQUE_V(own[i]); }
  lds_wait(c, 32 + (c.w ^ 2), tok);
  STAMP(layer, PH, 2);
  const LAS f32x4 *xr = (const LAS f32x4 *)(c.lds + XCH + (c.w ^ 2) * 4096 + l * 16);
  unsigned o[8];
#pragma unroll
  for (int g = 0; g < 4; ++g) {
    const f32x4 q = xr[g * 64];
    const f32x2 u = own[2 * g] + f32x2{q[0], q[1]}, v = own[2 * g + 1] + f32x2{q[2], q[3]};
    o[2 * g] = pk2(u.x, u.y); o[2 * g + 1] = pk2(v.x, v.y);
  }
  if (PH == PH_DOWN && layer == 1) {
    const rsrc_t ro = mkrsrc((const void *)rfl64((u64)p.out), 2048 * 1024);
#pragma unroll
    for (int t = 0; t < 2; ++t) {
      auto s0 = __builtin_amdgcn_permlane32_swap(o[4 * t], o[4 * t + 2], false, false);
      auto s1 = __builtin_amdgcn_permlane32_swap(o[4 * t + 1], o[4 * t + 3], false, false);
      st16<0>(v4u{s0[0], s1[0], s0[1], s1[1]}, ro, (l & 31) * 1024 + h * 16 + t * 32, tt * 32768 + cb * 128 + kh * 64);
    }
  } else {
    const unsigned so = (PH == PH_WO ? OFF_X1 + layer * 2 * MB : OFF_X2) + fm;
    st16(v4u{o[0], o[1], o[2], o[3]}, c.rws, l * 16, so); st16(v4u{o[4], o[5], o[6], o[7]}, c.rws, l * 16 + 1024, so);
    store_stats(c, o, l, OFF_ST + (PH == PH_WO ? layer : 2) * 262144 + tt * 4096, 2 * cb + kh);
    STAMP(layer, PH, 5);
    publish(p, c, layer * 5 + PH, rb * 8 + cb);
    if (REWARM) { glds<true>(c.rws, l * 16, so, c.lds0 + L_JUNK); glds<true>(c.rws, l * 16, so + 1024, c.lds0 + L_JUNK); }
  }
  STAMP(layer, PH, 3);
}

__device__ __forceinline__ void helpers_chain(const Params &p, const Ctx &c0, int layer, const v16u (&pre)[8] STAMP_ARGS) {
  const Ctx c = fresh(c0);
  const int l = lane_id(), hj = c.w - 4;
  LAS char *lw = c.lds + l * 16;
  publish(p, c, layer * 5 + PH_ATT, (c.bj & 7) * 32 + 4 * c.bx + (c.bj >> 3));
  STAMP(layer, PH_ATT, 3);
  static_for<25>([&](auto I) { *(LAS v4u *)(lw + (25 * hj + I.value) * 1024) = frag<I.value>(pre); });
  const unsigned src = OFF_WT2 + ((2 * (c.bj & 7) + (hj & 1)) * 86 + (hj >> 1) * 43) * 1024;
  v4u w2[22];
#define LDW(i) w2[i] = ld16(c.rws, l * 16, src + ((i) < 18 ? 7 + (i) : 21 + (i)) * 1024)
  STAMP(layer, PH_WO, 0);
  lds_wait(c, 40 + layer * 5 + PH_WO, 4);
#pragma unroll
  for (int i = 0; i < NW_WO; ++i) LDW(i);
  STAMP(layer, PH_WO, 3);
  BAR();
  lds_wait(c, 40 + layer * 5 + PH_UP, 4);
#pragma unroll
  for (int i = NW_WO; i < 22; ++i) LDW(i);
#pragma unroll
  for (int ks = 25; ks < 39; ++ks) glds<true>(c.rws, l * 16, src + ks * 1024, c.lds0 + ks * 4096 + hj * 1024);
#undef LDW
  STAMP(layer, PH_UP, 3);
  lds_wait(c, 16 + layer * 5 + PH_UP, 4);
  static_for<7>([&](auto I) { *(LAS v4u *)(lw + I.value * 4096 + hj * 1024) = frag<25 + I.value>(pre); });
#pragma unroll
  for (int i = 0; i < 18; ++i) *(LAS v4u *)(lw + (7 + i) * 4096 + hj * 1024) = w2[i];
  VMWAIT(0);
  STAMP(layer, PH_DOWN, 1);
  BAR();
  BAR();
#pragma unroll
  for (int i = 0; i < 4; ++i) *(LAS v4u *)(lw + i * 4096 + hj * 1024) = w2[18 + i];
  if (layer == 0) {
    v4u wq[25];
    const unsigned sq = OFF_WTQ + 3 * (c.bj & 15) * NTP + 25 * hj * 1024;
    asm volatile("s_waitcnt lgkmcnt(0)" ::: "memory");
    lds_wait(c, 40 + PH_DOWN, 4);
#pragma unroll
    for (int i = 0; i < 6; ++i) wq[i] = ld16(c.rws, l * 16, sq + i * 1024);
    BAR();
#pragma unroll
    for (int i = 6; i < 25; ++i) wq[i] = ld16(c.rws, l * 16, sq + i * 1024);
    STAMP(1, PH_QKV, 0);
    lds_wait(c, 16 + PH_DOWN, 4);
#pragma unroll
    for (int i = 0; i < 25; ++i) *(LAS v4u *)(lw + (25 * hj + i) * 1024) = wq[i];
    STAMP(1, PH_QKV, 1);
    BAR();
  } else BAR();
}

#define ATTN_FAST_ASM \
  "s_cmp_eq_u32 %[cnt], 0\n\t" \
  "s_cbranch_scc1 8f\n\t" \
  "s_mov_b32 %[keep], m0\n\t" \
  "v_mov_b32 v248, 0xdddd\n\t" \
  "ds_read_b128 v[224:227], %[ka]\n\t" \
  "ds_read_b128 v[232:235], %[va0]\n\t" \
  "ds_read_b128 v[240:243], %[va1]\n\t" \
  "ds_read_b128 v[228:231], %[ka] offset:4160\n\t" \
  "v_mov_b32 v244, 0\n\t" \
  "v_mov_b32 v245, 0\n\t" \
  "v_mov_b32 v246, 0\n\t" \
  "v_mov_b32 v247, 0\n\t" \
  "s_mov_b32 %[cnt], 0\n\t" \
  "s_nop 0\n\t" \
  "v_mfma_f32_32x32x16_bf16 %[oa], v[244:247], v[244:247], 0\n\t" \
  "v_mfma_f32_32x32x16_bf16 %[ob], v[244:247], v[244:247], 0\n\t" \
  "v_mfma_f32_32x32x16_bf16 v[208:223], v[244:247], v[244:247], 0\n\t" \
  "s_waitcnt lgkmcnt(3)\n\t" \
  "v_mfma_f32_32x32x16_bf16 v[192:207], v[224:227], %[qa], 0\n\t" \
  "s_nop 11\n\t" \
  "s_waitcnt lgkmcnt(0)\n\t" \
  "v_exp_f32 v192, v192\n\t" \
  "v_exp_f32 v193, v193\n\t" \
  "v_smfmac_f32_32x32x32_bf16 %[ob], v[244:247], v[216:223], v248\n\t" \
  "v_exp_f32 v194, v194\n\t" \
  "v_exp_f32 v195, v195\n\t" \
  "v_exp_f32 v196, v196\n\t" \
  "v_mfma_f32_32x32x16_bf16 v[208:223], v[224:227], %[qb], 0\n\t" \
  "ds_read_b128 v[224:227], %[ka] offset:8320\n\t" \
  "ds_read_b128 v[236:239], %[va0] offset:4160\n\t" \
  "ds_read_b128 v[244:247], %[va1] offset:4160\n\t" \
  "v_exp_f32 v197, v197\n\t" \
  "v_exp_f32 v198, v198\n\t" \
  "v_exp_f32 v199, v199\n\t" \
  "s_mov_b32 m0, %[m0v]\n\t" \
  "s_add_u32 %[m0v], %[m0v], 16640\n\t" \
  "buffer_load_dwordx4 %[vo], %[rs], %[so] offen sc1 lds\n\t" \
  "v_exp_f32 v200, v200\n\t" \
  "v_exp_f32 v201, v201\n\t" \
  "v_exp_f32 v202, v202\n\t" \
  "v_smfmac_f32_32x32x32_bf16 %[oa], v[232:235], v[192:199], v248\n\t" \
  "v_exp_f32 v203, v203\n\t" \
  "v_exp_f32 v204, v204\n\t" \
  "v_exp_f32 v205, v205\n\t" \
  "buffer_load_dwordx4 %[vo], %[rs], %[so] offen offset:1024 sc1 lds\n\t" \
  "s_add_u32 %[so], %[so], 16384\n\t" \
  "v_exp_f32 v206, v206\n\t" \
  "v_exp_f32 v207, v207\n\t" \
  "v_exp_f32 v208, v208\n\t" \
  "v_exp_f32 v209, v209\n\t" \
  "v_smfmac_f32_32x32x32_bf16 %[oa], v[240:243], v[200:207], v248\n\t" \
  "v_exp_f32 v210, v210\n\t" \
  "v_exp_f32 v211, v211\n\t" \
  "v_exp_f32 v212, v212\n\t" \
  "v_mfma_f32_32x32x16_bf16 v[192:207], v[228:231], %[qa], 0\n\t" \
  "v_exp_f32 v213, v213\n\t" \
  "v_exp_f32 v214, v214\n\t" \
  "v_exp_f32 v215, v215\n\t" \
  "s_mov_b32 m0, %[m0v]\n\t" \
  "s_add_u32 %[m0v], %[m0v], 16640\n\t" \
  "buffer_load_dwordx4 %[vo], %[rs], %[so] offen sc1 lds\n\t" \
  "v_exp_f32 v216, v216\n\t" \
  "v_exp_f32 v217, v217\n\t" \
  "v_exp_f32 v218, v218\n\t" \
  "v_smfmac_f32_32x32x32_bf16 %[ob], v[232:235], v[208:215], v248\n\t" \
  "v_exp_f32 v219, v219\n\t" \
  "v_exp_f32 v220, v220\n\t" \
  "v_exp_f32 v221, v221\n\t" \
  "buffer_load_dwordx4 %[vo], %[rs], %[so] offen offset:1024 sc1 lds\n\t" \
  "s_add_u32 %[so], %[so], 16384\n\t" \
  "v_exp_f32 v222, v222\n\t" \
  "v_exp_f32 v223, v223\n\t" \
  "s_waitcnt lgkmcnt(0)\n\t" \
  "v_exp_f32 v192, v192\n\t" \
  "v_exp_f32 v193, v193\n\t" \
  "v_smfmac_f32_32x32x32_bf16 %[ob], v[240:243], v[216:223], v248\n\t" \
  "v_exp_f32 v194, v194\n\t" \
  "v_exp_f32 v195, v195\n\t" \
  "v_exp_f32 v196, v196\n\t" \
  "v_mfma_f32_32x32x16_bf16 v[208:223], v[228:231], %[qb], 0\n\t" \
  "ds_read_b128 v[228:231], %[ka] offset:12480\n\t" \
  "ds_read_b128 v[232:235], %[va0] offset:8320\n\t" \
  "ds_read_b128 v[240:243], %[va1] offset:8320\n\t" \
  "v_exp_f32 v197, v197\n\t" \
  "v_exp_f32 v198, v198\n\t" \
  "v_exp_f32 v199, v199\n\t" \
  "s_mov_b32 m0, %[m0v]\n\t" \
  "s_add_u32 %[m0v], %[m0v], 16640\n\t" \
  "buffer_load_dwordx4 %[vo], %[rs], %[so] offen sc1 lds\n\t" \
  "v_exp_f32 v200, v200\n\t" \
  "v_exp_f32 v201, v201\n\t" \
  "v_exp_f32 v202, v202\n\t" \
  "v_smfmac_f32_32x32x32_bf16 %[oa], v[236:239], v[192:199], v248\n\t" \
  "v_exp_f32 v203, v203\n\t" \
  "v_exp_f32 v204, v204\n\t" \
  "v_exp_f32 v205, v205\n\t" \
  "buffer_load_dwordx4 %[vo], %[rs], %[so] offen offset:1024 sc1 lds\n\t" \
  "s_add_u32 %[so], %[so], 16384\n\t" \
  "v_exp_f32 v206, v206\n\t" \
  "v_exp_f32 v207, v207\n\t" \
  "v_exp_f32 v208, v208\n\t" \
  "v_exp_f32 v209, v209\n\t" \
  "v_smfmac_f32_32x32x32_bf16 %[oa], v[244:247], v[200:207], v248\n\t" \
  "v_exp_f32 v210, v210\n\t" \
  "v_exp_f32 v211, v211\n\t" \
  "v_exp_f32 v212, v212\n\t" \
  "v_mfma_f32_32x32x16_bf16 v[192:207], v[224:227], %[qa], 0\n\t" \
  "v_exp_f32 v213, v213\n\t" \
  "v_exp_f32 v214, v214\n\t" \
  "v_exp_f32 v215, v215\n\t" \
  "s_mov_b32 m0, %[m0v]\n\t" \
  "s_add_u32 %[m0v], %[m0v], 16640\n\t" \
  "buffer_load_dwordx4 %[vo], %[rs], %[so] offen sc1 lds\n\t" \
  "v_exp_f32 v216, v216\n\t" \
  "v_exp_f32 v217, v217\n\t" \
  "v_exp_f32 v218, v218\n\t" \
  "v_smfmac_f32_32x32x32_bf16 %[ob], v[236:239], v[208:215], v248\n\t" \
  "v_exp_f32 v219, v219\n\t" \
  "v_exp_f32 v220, v220\n\t" \
  "v_exp_f32 v221, v221\n\t" \
  "buffer_load_dwordx4 %[vo], %[rs], %[so] offen offset:1024 sc1 lds\n\t" \
  "s_add_u32 %[so], %[so], 16384\n\t" \
  "v_exp_f32 v222, v222\n\t" \
  "v_exp_f32 v223, v223\n\t" \
  "s_waitcnt vmcnt(6)\n\t" \
  "s_barrier\n\t" \
  "s_waitcnt lgkmcnt(0)\n\t" \
  "v_exp_f32 v192, v192\n\t" \
  "v_exp_f32 v193, v193\n\t" \
  "v_smfmac_f32_32x32x32_bf16 %[ob], v[244:247], v[216:223], v248\n\t" \
  "v_exp_f32 v194, v194\n\t" \
  "v_exp_f32 v195, v195\n\t" \
  "v_exp_f32 v196, v196\n\t" \
  "v_mfma_f32_32x32x16_bf16 v[208:223], v[224:227], %[qb], 0\n\t" \
  "ds_read_b128 v[224:227], %[ka] offset:16640\n\t" \
  "ds_read_b128 v[236:239], %[va0] offset:12480\n\t" \
  "ds_read_b128 v[244:247], %[va1] offset:12480\n\t" \
  "v_exp_f32 v197, v197\n\t" \
  "v_exp_f32 v198, v198\n\t" \
  "v_exp_f32 v199, v199\n\t" \
  "s_mov_b32 m0, %[m0v]\n\t" \
  "s_add_u32 %[m0v], %[m0v], 16640\n\t" \
  "buffer_load_dwordx4 %[vo], %[rs], %[so] offen sc1 lds\n\t" \
  "v_exp_f32 v200, v200\n\t" \
  "v_exp_f32 v201, v201\n\t" \
  "v_exp_f32 v202, v202\n\t" \
  "v_smfmac_f32_32x32x32_bf16 %[oa], v[232:235], v[192:199], v248\n\t" \
  "v_exp_f32 v203, v203\n\t" \
  "v_exp_f32 v204, v204\n\t" \
  "v_exp_f32 v205, v205\n\t" \
  "buffer_load_dwordx4 %[vo], %[rs], %[so] offen offset:1024 sc1 lds\n\t" \
  "s_add_u32 %[so], %[so], 16384\n\t" \
  "v_exp_f32 v206, v206\n\t" \
  "v_exp_f32 v207, v207\n\t" \
  "v_exp_f32 v208, v208\n\t" \
  "v_exp_f32 v209, v209\n\t" \
  "v_smfmac_f32_32x32x32_bf16 %[oa], v[240:243], v[200:207], v248\n\t" \
  "v_exp_f32 v210, v210\n\t" \
  "v_exp_f32 v211, v211\n\t" \
  "v_exp_f32 v212, v212\n\t" \
  "v_mfma_f32_32x32x16_bf16 v[192:207], v[228:231], %[qa], 0\n\t" \
  "v_exp_f32 v213, v213\n\t" \
  "v_exp_f32 v214, v214\n\t" \
  "v_exp_f32 v215, v215\n\t" \
  "s_mov_b32 m0, %[m0v]\n\t" \
  "s_add_u32 %[m0v], %[m0v], 16640\n\t" \
  "buffer_load_dwordx4 %[vo], %[rs], %[so] offen sc1 lds\n\t" \
  "v_exp_f32 v216, v216\n\t" \
  "v_exp_f32 v217, v217\n\t" \
  "v_exp_f32 v218, v218\n\t" \
  "v_smfmac_f32_32x32x32_bf16 %[ob], v[232:235], v[208:215], v248\n\t" \
  "v_exp_f32 v219, v219\n\t" \
  "v_exp_f32 v220, v220\n\t" \
  "v_exp_f32 v221, v221\n\t" \
  "buffer_load_dwordx4 %[vo], %[rs], %[so] offen offset:1024 sc1 lds\n\t" \
  "s_add_u32 %[so], %[so], 16384\n\t" \
  "v_exp_f32 v222, v222\n\t" \
  "v_exp_f32 v223, v223\n\t" \
  "s_waitcnt lgkmcnt(0)\n\t" \
  "v_exp_f32 v192, v192\n\t" \
  "v_exp_f32 v193, v193\n\t" \
  "v_smfmac_f32_32x32x32_bf16 %[ob], v[240:243], v[216:223], v248\n\t" \
  "v_exp_f32 v194, v194\n\t" \
  "v_exp_f32 v195, v195\n\t" \
  "v_exp_f32 v196, v196\n\t" \
  "v_mfma_f32_32x32x16_bf16 v[208:223], v[228:231], %[qb], 0\n\t" \
  "ds_read_b128 v[228:231], %[ka] offset:20800\n\t" \
  "ds_read_b128 v[232:235], %[va0] offset:16640\n\t" \
  "ds_read_b128 v[240:243], %[va1] offset:16640\n\t" \
  "v_exp_f32 v197, v197\n\t" \
  "v_exp_f32 v198, v198\n\t" \
  "v_exp_f32 v199, v199\n\t" \
  "s_mov_b32 m0, %[m0v]\n\t" \
  "s_add_u32 %[m0v], %[m0v], 16640\n\t" \
  "buffer_load_dwordx4 %[vo], %[rs], %[so] offen sc1 lds\n\t" \
  "v_exp_f32 v200, v200\n\t" \
  "v_exp_f32 v201, v201\n\t" \
  "v_exp_f32 v202, v202\n\t" \
  "v_smfmac_f32_32x32x32_bf16 %[oa], v[236:239], v[192:199], v248\n\t" \
  "v_exp_f32 v203, v203\n\t" \
  "v_exp_f32 v204, v204\n\t" \
  "v_exp_f32 v205, v205\n\t" \
  "buffer_load_dwordx4 %[vo], %[rs], %[so] offen offset:1024 sc1 lds\n\t" \
  "s_add_u32 %[so], %[so], 16384\n\t" \
  "v_exp_f32 v206, v206\n\t" \
  "v_exp_f32 v207, v207\n\t" \
  "v_exp_f32 v208, v208\n\t" \
  "v_exp_f32 v209, v209\n\t" \
  "v_smfmac_f32_32x32x32_bf16 %[oa], v[244:247], v[200:207], v248\n\t" \
  "v_exp_f32 v210, v210\n\t" \
  "v_exp_f32 v211, v211\n\t" \
  "v_exp_f32 v212, v212\n\t" \
  "v_mfma_f32_32x32x16_bf16 v[192:207], v[224:227], %[qa], 0\n\t" \
  "v_exp_f32 v213, v213\n\t" \
  "v_exp_f32 v214, v214\n\t" \
  "v_exp_f32 v215, v215\n\t" \
  "v_exp_f32 v216, v216\n\t" \
  "v_exp_f32 v217, v217\n\t" \
  "v_exp_f32 v218, v218\n\t" \
  "v_smfmac_f32_32x32x32_bf16 %[ob], v[236:239], v[208:215], v248\n\t" \
  "v_exp_f32 v219, v219\n\t" \
  "v_exp_f32 v220, v220\n\t" \
  "v_exp_f32 v221, v221\n\t" \
  "v_exp_f32 v222, v222\n\t" \
  "v_exp_f32 v223, v223\n\t" \
  "s_waitcnt vmcnt(10)\n\t" \
  "s_barrier\n\t" \
  "s_waitcnt lgkmcnt(0)\n\t" \
  "v_exp_f32 v192, v192\n\t" \
  "v_exp_f32 v193, v193\n\t" \
  "v_smfmac_f32_32x32x32_bf16 %[ob], v[244:247], v[216:223], v248\n\t" \
  "v_exp_f32 v194, v194\n\t" \
  "v_exp_f32 v195, v195\n\t" \
  "v_exp_f32 v196, v196\n\t" \
  "v_mfma_f32_32x32x16_bf16 v[208:223], v[224:227], %[qb], 0\n\t" \
  "ds_read_b128 v[224:227], %[ka] offset:24960\n\t" \
  "ds_read_b128 v[236:239], %[va0] offset:20800\n\t" \
  "ds_read_b128 v[244:247], %[va1] offset:20800\n\t" \
  "v_exp_f32 v197, v197\n\t" \
  "v_exp_f32 v198, v198\n\t" \
  "v_exp_f32 v199, v199\n\t" \
  "v_exp_f32 v200, v200\n\t" \
  "v_exp_f32 v201, v201\n\t" \
  "v_exp_f32 v202, v202\n\t" \
  "v_smfmac_f32_32x32x32_bf16 %[oa], v[232:235], v[192:199], v248\n\t" \
  "v_exp_f32 v203, v203\n\t" \
  "v_exp_f32 v204, v204\n\t" \
  "v_exp_f32 v205, v205\n\t" \
  "v_exp_f32 v206, v206\n\t" \
  "v_exp_f32 v207, v207\n\t" \
  "v_exp_f32 v208, v208\n\t" \
  "v_exp_f32 v209, v209\n\t" \
  "v_smfmac_f32_32x32x32_bf16 %[oa], v[240:243], v[200:207], v248\n\t" \
  "v_exp_f32 v210, v210\n\t" \
  "v_exp_f32 v211, v211\n\t" \
  "v_exp_f32 v212, v212\n\t" \
  "v_mfma_f32_32x32x16_bf16 v[192:207], v[228:231], %[qa], 0\n\t" \
  "v_exp_f32 v213, v213\n\t" \
  "v_exp_f32 v214, v214\n\t" \
  "v_exp_f32 v215, v215\n\t" \
  "v_exp_f32 v216, v216\n\t" \
  "v_exp_f32 v217, v217\n\t" \
  "v_exp_f32 v218, v218\n\t" \
  "v_smfmac_f32_32x32x32_bf16 %[ob], v[232:235], v[208:215], v248\n\t" \
  "v_exp_f32 v219, v219\n\t" \
  "v_exp_f32 v220, v220\n\t" \
  "v_exp_f32 v221, v221\n\t" \
  "v_exp_f32 v222, v222\n\t" \
  "v_exp_f32 v223, v223\n\t" \
  "s_waitcnt lgkmcnt(0)\n\t" \
  "v_exp_f32 v192, v192\n\t" \
  "v_exp_f32 v193, v193\n\t" \
  "v_smfmac_f32_32x32x32_bf16 %[ob], v[240:243], v[216:223], v248\n\t" \
  "v_exp_f32 v194, v194\n\t" \
  "v_exp_f32 v195, v195\n\t" \
  "v_exp_f32 v196, v196\n\t" \
  "v_mfma_f32_32x32x16_bf16 v[208:223], v[228:231], %[qb], 0\n\t" \
  "ds_read_b128 v[228:231], %[ka] offset:29120\n\t" \
  "ds_read_b128 v[232:235], %[va0] offset:24960\n\t" \
  "ds_read_b128 v[240:243], %[va1] offset:24960\n\t" \
  "v_exp_f32 v197, v197\n\t" \
  "v_exp_f32 v198, v198\n\t" \
  "v_exp_f32 v199, v199\n\t" \
  "v_exp_f32 v200, v200\n\t" \
  "v_exp_f32 v201, v201\n\t" \
  "v_exp_f32 v202, v202\n\t" \
  "v_smfmac_f32_32x32x32_bf16 %[oa], v[236:239], v[192:199], v248\n\t" \
  "v_exp_f32 v203, v203\n\t" \
  "v_exp_f32 v204, v204\n\t" \
  "v_exp_f32 v205, v205\n\t" \
  "v_exp_f32 v206, v206\n\t" \
  "v_exp_f32 v207, v207\n\t" \
  "v_exp_f32 v208, v208\n\t" \
  "v_exp_f32 v209, v209\n\t" \
  "v_smfmac_f32_32x32x32_bf16 %[oa], v[244:247], v[200:207], v248\n\t" \
  "v_exp_f32 v210, v210\n\t" \
  "v_exp_f32 v211, v211\n\t" \
  "v_exp_f32 v212, v212\n\t" \
  "v_mfma_f32_32x32x16_bf16 v[192:207], v[224:227], %[qa], 0\n\t" \
  "v_exp_f32 v213, v213\n\t" \
  "v_exp_f32 v214, v214\n\t" \
  "v_exp_f32 v215, v215\n\t" \
  "v_exp_f32 v216, v216\n\t" \
  "v_exp_f32 v217, v217\n\t" \
  "v_exp_f32 v218, v218\n\t" \
  "v_smfmac_f32_32x32x32_bf16 %[ob], v[236:239], v[208:215], v248\n\t" \
  "v_exp_f32 v219, v219\n\t" \
  "v_exp_f32 v220, v220\n\t" \
  "v_exp_f32 v221, v221\n\t" \
  "v_exp_f32 v222, v222\n\t" \
  "v_exp_f32 v223, v223\n\t" \
  "s_waitcnt lgkmcnt(0)\n\t" \
  "v_exp_f32 v192, v192\n\t" \
  "v_exp_f32 v193, v193\n\t" \
  "v_smfmac_f32_32x32x32_bf16 %[ob], v[244:247], v[216:223], v248\n\t" \
  "v_exp_f32 v194, v194\n\t" \
  "v_exp_f32 v195, v195\n\t" \
  "v_exp_f32 v196, v196\n\t" \
  "v_mfma_f32_32x32x16_bf16 v[208:223], v[224:227], %[qb], 0\n\t" \
  "ds_read_b128 v[224:227], %[ka] offset:33280\n\t" \
  "ds_read_b128 v[236:239], %[va0] offset:29120\n\t" \
  "ds_read_b128 v[244:247], %[va1] offset:29120\n\t" \
  "v_exp_f32 v197, v197\n\t" \
  "v_exp_f32 v198, v198\n\t" \
  "v_exp_f32 v199, v199\n\t" \
  "buffer_load_dwordx2 v[250:251], %[fo], %[rs], 0 offen sc1\n\t" \
  "v_exp_f32 v200, v200\n\t" \
  "v_exp_f32 v201, v201\n\t" \
  "v_exp_f32 v202, v202\n\t" \
  "v_smfmac_f32_32x32x32_bf16 %[oa], v[232:235], v[192:199], v248\n\t" \
  "v_exp_f32 v203, v203\n\t" \
  "v_exp_f32 v204, v204\n\t" \
  "v_exp_f32 v205, v205\n\t" \
  "v_exp_f32 v206, v206\n\t" \
  "v_exp_f32 v207, v207\n\t" \
  "v_exp_f32 v208, v208\n\t" \
  "v_exp_f32 v209, v209\n\t" \
  "v_smfmac_f32_32x32x32_bf16 %[oa], v[240:243], v[200:207], v248\n\t" \
  "v_exp_f32 v210, v210\n\t" \
  "v_exp_f32 v211, v211\n\t" \
  "v_exp_f32 v212, v212\n\t" \
  "v_mfma_f32_32x32x16_bf16 v[192:207], v[228:231], %[qa], 0\n\t" \
  "v_exp_f32 v213, v213\n\t" \
  "v_exp_f32 v214, v214\n\t" \
  "v_exp_f32 v215, v215\n\t" \
  "v_exp_f32 v216, v216\n\t" \
  "v_exp_f32 v217, v217\n\t" \
  "v_exp_f32 v218, v218\n\t" \
  "v_smfmac_f32_32x32x32_bf16 %[ob], v[232:235], v[208:215], v248\n\t" \
  "v_exp_f32 v219, v219\n\t" \
  "v_exp_f32 v220, v220\n\t" \
  "v_exp_f32 v221, v221\n\t" \
  "v_exp_f32 v222, v222\n\t" \
  "v_exp_f32 v223, v223\n\t" \
  "s_waitcnt lgkmcnt(0)\n\t" \
  "v_exp_f32 v192, v192\n\t" \
  "v_exp_f32 v193, v193\n\t" \
  "v_smfmac_f32_32x32x32_bf16 %[ob], v[240:243], v[216:223], v248\n\t" \
  "v_exp_f32 v194, v194\n\t" \
  "v_exp_f32 v195, v195\n\t" \
  "v_exp_f32 v196, v196\n\t" \
  "v_mfma_f32_32x32x16_bf16 v[208:223], v[228:231], %[qb], 0\n\t" \
  "ds_read_b128 v[228:231], %[ka] offset:37440\n\t" \
  "ds_read_b128 v[232:235], %[va0] offset:33280\n\t" \
  "ds_read_b128 v[240:243], %[va1] offset:33280\n\t" \
  "v_exp_f32 v197, v197\n\t" \
  "v_exp_f32 v198, v198\n\t" \
  "v_exp_f32 v199, v199\n\t" \
  "v_exp_f32 v200, v200\n\t" \
  "v_exp_f32 v201, v201\n\t" \
  "v_exp_f32 v202, v202\n\t" \
  "v_smfmac_f32_32x32x32_bf16 %[oa], v[236:239], v[192:199], v248\n\t" \
  "v_exp_f32 v203, v203\n\t" \
  "v_exp_f32 v204, v204\n\t" \
  "v_exp_f32 v205, v205\n\t" \
  "v_exp_f32 v206, v206\n\t" \
  "v_exp_f32 v207, v207\n\t" \
  "v_exp_f32 v208, v208\n\t" \
  "v_exp_f32 v209, v209\n\t" \
  "v_smfmac_f32_32x32x32_bf16 %[oa], v[244:247], v[200:207], v248\n\t" \
  "v_exp_f32 v210, v210\n\t" \
  "v_exp_f32 v211, v211\n\t" \
  "v_exp_f32 v212, v212\n\t" \
  "v_mfma_f32_32x32x16_bf16 v[192:207], v[224:227], %[qa], 0\n\t" \
  "v_exp_f32 v213, v213\n\t" \
  "v_exp_f32 v214, v214\n\t" \
  "v_exp_f32 v215, v215\n\t" \
  "v_exp_f32 v216, v216\n\t" \
  "v_exp_f32 v217, v217\n\t" \
  "v_exp_f32 v218, v218\n\t" \
  "v_smfmac_f32_32x32x32_bf16 %[ob], v[236:239], v[208:215], v248\n\t" \
  "v_exp_f32 v219, v219\n\t" \
  "v_exp_f32 v220, v220\n\t" \
  "v_exp_f32 v221, v221\n\t" \
  "v_exp_f32 v222, v222\n\t" \
  "v_exp_f32 v223, v223\n\t" \
  "v_add_u32 %[ka], 33280, %[ka]\n\t" \
  "v_add_u32 %[va0], 33280, %[va0]\n\t" \
  "v_add_u32 %[va1], 33280, %[va1]\n\t" \
  "s_mov_b32 m0, %[dummy]\n\t" \
  "s_mov_b32 %[so], 0x100000\n\t" \
  "5:\n\t" \
  "s_waitcnt vmcnt(0)\n\t" \
  "v_cmp_ne_u64 vcc, %[tag], v[250:251]\n\t" \
  "s_cbranch_vccz 6f\n\t" \
  "buffer_load_dwordx2 v[250:251], %[fo], %[rs], 0 offen sc1\n\t" \
  "s_sub_u32 %[so], %[so], 1\n\t" \
  "s_cbranch_scc0 5b\n\t" \
  "6:\n\t" \
  "s_barrier\n\t" \
  "s_waitcnt lgkmcnt(0)\n\t" \
  "v_exp_f32 v192, v192\n\t" \
  "v_exp_f32 v193, v193\n\t" \
  "v_smfmac_f32_32x32x32_bf16 %[ob], v[244:247], v[216:223], v248\n\t" \
  "v_exp_f32 v194, v194\n\t" \
  "v_exp_f32 v195, v195\n\t" \
  "v_exp_f32 v196, v196\n\t" \
  "v_mfma_f32_32x32x16_bf16 v[208:223], v[224:227], %[qb], 0\n\t" \
  "ds_read_b128 v[224:227], %[ka] offset:8320\n\t" \
  "ds_read_b128 v[236:239], %[va0] offset:4160\n\t" \
  "ds_read_b128 v[244:247], %[va1] offset:4160\n\t" \
  "v_exp_f32 v197, v197\n\t" \
  "v_exp_f32 v198, v198\n\t" \
  "v_exp_f32 v199, v199\n\t" \
  "s_mov_b32 %[so], %[p0]\n\t" \
  "buffer_load_dwordx4 v[64:67], %[vo], %[rs], %[so] offen sc1\n\t" \
  "s_add_u32 %[so], %[so], 1024\n\t" \
  "v_exp_f32 v200, v200\n\t" \
  "v_exp_f32 v201, v201\n\t" \
  "v_exp_f32 v202, v202\n\t" \
  "v_smfmac_f32_32x32x32_bf16 %[oa], v[232:235], v[192:199], v248\n\t" \
  "v_exp_f32 v203, v203\n\t" \
  "v_exp_f32 v204, v204\n\t" \
  "v_exp_f32 v205, v205\n\t" \
  "v_exp_f32 v206, v206\n\t" \
  "v_exp_f32 v207, v207\n\t" \
  "v_exp_f32 v208, v208\n\t" \
  "v_exp_f32 v209, v209\n\t" \
  "v_smfmac_f32_32x32x32_bf16 %[oa], v[240:243], v[200:207], v248\n\t" \
  "v_exp_f32 v210, v210\n\t" \
  "v_exp_f32 v211, v211\n\t" \
  "v_exp_f32 v212, v212\n\t" \
  "v_mfma_f32_32x32x16_bf16 v[192:207], v[228:231], %[qa], 0\n\t" \
  "v_exp_f32 v213, v213\n\t" \
  "v_exp_f32 v214, v214\n\t" \
  "v_exp_f32 v215, v215\n\t" \
  "buffer_load_dwordx4 v[68:71], %[vo], %[rs], %[so] offen sc1\n\t" \
  "s_add_u32 %[so], %[so], 1024\n\t" \
  "v_exp_f32 v216, v216\n\t" \
  "v_exp_f32 v217, v217\n\t" \
  "v_exp_f32 v218, v218\n\t" \
  "v_smfmac_f32_32x32x32_bf16 %[ob], v[232:235], v[208:215], v248\n\t" \
  "v_exp_f32 v219, v219\n\t" \
  "v_exp_f32 v220, v220\n\t" \
  "v_exp_f32 v221, v221\n\t" \
  "buffer_load_dwordx4 %[vo], %[rw], %[wo] offen lds\n\t" \
  "s_add_u32 %[wo], %[wo], 262144\n\t" \
  "v_exp_f32 v222, v222\n\t" \
  "v_exp_f32 v223, v223\n\t" \
  "s_waitcnt lgkmcnt(0)\n\t" \
  "v_exp_f32 v192, v192\n\t" \
  "v_exp_f32 v193, v193\n\t" \
  "v_smfmac_f32_32x32x32_bf16 %[ob], v[240:243], v[216:223], v248\n\t" \
  "v_exp_f32 v194, v194\n\t" \
  "v_exp_f32 v195, v195\n\t" \
  "v_exp_f32 v196, v196\n\t" \
  "v_mfma_f32_32x32x16_bf16 v[208:223], v[228:231], %[qb], 0\n\t" \
  "ds_read_b128 v[228:231], %[ka] offset:12480\n\t" \
  "ds_read_b128 v[232:235], %[va0] offset:8320\n\t" \
  "ds_read_b128 v[240:243], %[va1] offset:8320\n\t" \
  "v_exp_f32 v197, v197\n\t" \
  "v_exp_f32 v198, v198\n\t" \
  "v_exp_f32 v199, v199\n\t" \
  "buffer_load_dwordx4 v[72:75], %[vo], %[rs], %[so] offen sc1\n\t" \
  "s_add_u32 %[so], %[so], 1024\n\t" \
  "v_exp_f32 v200, v200\n\t" \
  "v_exp_f32 v201, v201\n\t" \
  "v_exp_f32 v202, v202\n\t" \
  "v_smfmac_f32_32x32x32_bf16 %[oa], v[236:239], v[192:199], v248\n\t" \
  "v_exp_f32 v203, v203\n\t" \
  "v_exp_f32 v204, v204\n\t" \
  "v_exp_f32 v205, v205\n\t" \
  "v_exp_f32 v206, v206\n\t" \
  "v_exp_f32 v207, v207\n\t" \
  "v_exp_f32 v208, v208\n\t" \
  "v_exp_f32 v209, v209\n\t" \
  "v_smfmac_f32_32x32x32_bf16 %[oa], v[244:247], v[200:207], v248\n\t" \
  "v_exp_f32 v210, v210\n\t" \
  "v_exp_f32 v211, v211\n\t" \
  "v_exp_f32 v212, v212\n\t" \
  "v_mfma_f32_32x32x16_bf16 v[192:207], v[224:227], %[qa], 0\n\t" \
  "v_exp_f32 v213, v213\n\t" \
  "v_exp_f32 v214, v214\n\t" \
  "v_exp_f32 v215, v215\n\t" \
  "buffer_load_dwordx4 v[76:79], %[vo], %[rs], %[so] offen sc1\n\t" \
  "s_add_u32 %[so], %[so], 1024\n\t" \
  "v_exp_f32 v216, v216\n\t" \
  "v_exp_f32 v217, v217\n\t" \
  "v_exp_f32 v218, v218\n\t" \
  "v_smfmac_f32_32x32x32_bf16 %[ob], v[236:239], v[208:215], v248\n\t" \
  "v_exp_f32 v219, v219\n\t" \
  "v_exp_f32 v220, v220\n\t" \
  "v_exp_f32 v221, v221\n\t" \
  "v_exp_f32 v222, v222\n\t" \
  "v_exp_f32 v223, v223\n\t" \
  "s_waitcnt lgkmcnt(0)\n\t" \
  "v_exp_f32 v192, v192\n\t" \
  "v_exp_f32 v193, v193\n\t" \
  "v_smfmac_f32_32x32x32_bf16 %[ob], v[244:247], v[216:223], v248\n\t" \
  "v_exp_f32 v194, v194\n\t" \
  "v_exp_f32 v195, v195\n\t" \
  "v_exp_f32 v196, v196\n\t" \
  "v_mfma_f32_32x32x16_bf16 v[208:223], v[224:227], %[qb], 0\n\t" \
  "ds_read_b128 v[224:227], %[ka] offset:16640\n\t" \
  "ds_read_b128 v[236:239], %[va0] offset:12480\n\t" \
  "ds_read_b128 v[244:247], %[va1] offset:12480\n\t" \
  "v_exp_f32 v197, v197\n\t" \
  "v_exp_f32 v198, v198\n\t" \
  "v_exp_f32 v199, v199\n\t" \
  "buffer_load_dwordx4 v[80:83], %[vo], %[rs], %[so] offen sc1\n\t" \
  "s_add_u32 %[so], %[so], 1024\n\t" \
  "v_exp_f32 v200, v200\n\t" \
  "v_exp_f32 v201, v201\n\t" \
  "v_exp_f32 v202, v202\n\t" \
  "v_smfmac_f32_32x32x32_bf16 %[oa], v[232:235], v[192:199], v248\n\t" \
  "v_exp_f32 v203, v203\n\t" \
  "v_exp_f32 v204, v204\n\t" \
  "v_exp_f32 v205, v205\n\t" \
  "buffer_load_dwordx4 %[vo], %[rw], %[wo] offen lds\n\t" \
  "s_add_u32 %[wo], %[wo], 262144\n\t" \
  "v_exp_f32 v206, v206\n\t" \
  "v_exp_f32 v207, v207\n\t" \
  "v_exp_f32 v208, v208\n\t" \
  "v_exp_f32 v209, v209\n\t" \
  "v_smfmac_f32_32x32x32_bf16 %[oa], v[240:243], v[200:207], v248\n\t" \
  "v_exp_f32 v210, v210\n\t" \
  "v_exp_f32 v211, v211\n\t" \
  "v_exp_f32 v212, v212\n\t" \
  "v_mfma_f32_32x32x16_bf16 v[192:207], v[228:231], %[qa], 0\n\t" \
  "v_exp_f32 v213, v213\n\t" \
  "v_exp_f32 v214, v214\n\t" \
  "v_exp_f32 v215, v215\n\t" \
  "buffer_load_dwordx4 v[84:87], %[vo], %[rs], %[so] offen sc1\n\t" \
  "s_add_u32 %[so], %[so], 1024\n\t" \
  "v_exp_f32 v216, v216\n\t" \
  "v_exp_f32 v217, v217\n\t" \
  "v_exp_f32 v218, v218\n\t" \
  "v_smfmac_f32_32x32x32_bf16 %[ob], v[232:235], v[208:215], v248\n\t" \
  "v_exp_f32 v219, v219\n\t" \
  "v_exp_f32 v220, v220\n\t" \
  "v_exp_f32 v221, v221\n\t" \
  "v_exp_f32 v222, v222\n\t" \
  "v_exp_f32 v223, v223\n\t" \
  "s_waitcnt lgkmcnt(0)\n\t" \
  "v_exp_f32 v192, v192\n\t" \
  "v_exp_f32 v193, v193\n\t" \
  "v_smfmac_f32_32x32x32_bf16 %[ob], v[240:243], v[216:223], v248\n\t" \
  "v_exp_f32 v194, v194\n\t" \
  "v_exp_f32 v195, v195\n\t" \
  "v_exp_f32 v196, v196\n\t" \
  "v_mfma_f32_32x32x16_bf16 v[208:223], v[228:231], %[qb], 0\n\t" \
  "ds_read_b128 v[228:231], %[ka] offset:20800\n\t" \
  "ds_read_b128 v[232:235], %[va0] offset:16640\n\t" \
  "ds_read_b128 v[240:243], %[va1] offset:16640\n\t" \
  "v_exp_f32 v197, v197\n\t" \
  "v_exp_f32 v198, v198\n\t" \
  "v_exp_f32 v199, v199\n\t" \
  "buffer_load_dwordx4 v[88:91], %[vo], %[rs], %[so] offen sc1\n\t" \
  "s_add_u32 %[so], %[so], 1024\n\t" \
  "v_exp_f32 v200, v200\n\t" \
  "v_exp_f32 v201, v201\n\t" \
  "v_exp_f32 v202, v202\n\t" \
  "v_smfmac_f32_32x32x32_bf16 %[oa], v[236:239], v[192:199], v248\n\t" \
  "v_exp_f32 v203, v203\n\t" \
  "v_exp_f32 v204, v204\n\t" \
  "v_exp_f32 v205, v205\n\t" \
  "v_exp_f32 v206, v206\n\t" \
  "v_exp_f32 v207, v207\n\t" \
  "v_exp_f32 v208, v208\n\t" \
  "v_exp_f32 v209, v209\n\t" \
  "v_smfmac_f32_32x32x32_bf16 %[oa], v[244:247], v[200:207], v248\n\t" \
  "v_exp_f32 v210, v210\n\t" \
  "v_exp_f32 v211, v211\n\t" \
  "v_exp_f32 v212, v212\n\t" \
  "v_mfma_f32_32x32x16_bf16 v[192:207], v[224:227], %[qa], 0\n\t" \
  "v_exp_f32 v213, v213\n\t" \
  "v_exp_f32 v214, v214\n\t" \
  "v_exp_f32 v215, v215\n\t" \
  "buffer_load_dwordx4 v[92:95], %[vo], %[rs], %[so] offen sc1\n\t" \
  "s_add_u32 %[so], %[so], 1024\n\t" \
  "v_exp_f32 v216, v216\n\t" \
  "v_exp_f32 v217, v217\n\t" \
  "v_exp_f32 v218, v218\n\t" \
  "v_smfmac_f32_32x32x32_bf16 %[ob], v[236:239], v[208:215], v248\n\t" \
  "v_exp_f32 v219, v219\n\t" \
  "v_exp_f32 v220, v220\n\t" \
  "v_exp_f32 v221, v221\n\t" \
  "buffer_load_dwordx4 %[vo], %[rw], %[wo] offen lds\n\t" \
  "s_add_u32 %[wo], %[wo], 262144\n\t" \
  "v_exp_f32 v222, v222\n\t" \
  "v_exp_f32 v223, v223\n\t" \
  "s_waitcnt lgkmcnt(0)\n\t" \
  "v_exp_f32 v192, v192\n\t" \
  "v_exp_f32 v193, v193\n\t" \
  "v_smfmac_f32_32x32x32_bf16 %[ob], v[244:247], v[216:223], v248\n\t" \
  "v_exp_f32 v194, v194\n\t" \
  "v_exp_f32 v195, v195\n\t" \
  "v_exp_f32 v196, v196\n\t" \
  "v_mfma_f32_32x32x16_bf16 v[208:223], v[224:227], %[qb], 0\n\t" \
  "ds_read_b128 v[224:227], %[ka] offset:24960\n\t" \
  "ds_read_b128 v[236:239], %[va0] offset:20800\n\t" \
  "ds_read_b128 v[244:247], %[va1] offset:20800\n\t" \
  "v_exp_f32 v197, v197\n\t" \
  "v_exp_f32 v198, v198\n\t" \
  "v_exp_f32 v199, v199\n\t" \
  "buffer_load_dwordx4 v[96:99], %[vo], %[rs], %[so] offen sc1\n\t" \
  "s_add_u32 %[so], %[so], 1024\n\t" \
  "v_exp_f32 v200, v200\n\t" \
  "v_exp_f32 v201, v201\n\t" \
  "v_exp_f32 v202, v202\n\t" \
  "v_smfmac_f32_32x32x32_bf16 %[oa], v[232:235], v[192:199], v248\n\t" \
  "v_exp_f32 v203, v203\n\t" \
  "v_exp_f32 v204, v204\n\t" \
  "v_exp_f32 v205, v205\n\t" \
  "v_exp_f32 v206, v206\n\t" \
  "v_exp_f32 v207, v207\n\t" \
  "v_exp_f32 v208, v208\n\t" \
  "v_exp_f32 v209, v209\n\t" \
  "v_smfmac_f32_32x32x32_bf16 %[oa], v[240:243], v[200:207], v248\n\t" \
  "v_exp_f32 v210, v210\n\t" \
  "v_exp_f32 v211, v211\n\t" \
  "v_exp_f32 v212, v212\n\t" \
  "v_mfma_f32_32x32x16_bf16 v[192:207], v[228:231], %[qa], 0\n\t" \
  "v_exp_f32 v213, v213\n\t" \
  "v_exp_f32 v214, v214\n\t" \
  "v_exp_f32 v215, v215\n\t" \
  "buffer_load_dwordx4 v[100:103], %[vo], %[rs], %[so] offen sc1\n\t" \
  "s_add_u32 %[so], %[so], 1024\n\t" \
  "v_exp_f32 v216, v216\n\t" \
  "v_exp_f32 v217, v217\n\t" \
  "v_exp_f32 v218, v218\n\t" \
  "v_smfmac_f32_32x32x32_bf16 %[ob], v[232:235], v[208:215], v248\n\t" \
  "v_exp_f32 v219, v219\n\t" \
  "v_exp_f32 v220, v220\n\t" \
  "v_exp_f32 v221, v221\n\t" \
  "v_exp_f32 v222, v222\n\t" \
  "v_exp_f32 v223, v223\n\t" \
  "s_waitcnt lgkmcnt(0)\n\t" \
  "v_exp_f32 v192, v192\n\t" \
  "v_exp_f32 v193, v193\n\t" \
  "v_smfmac_f32_32x32x32_bf16 %[ob], v[240:243], v[216:223], v248\n\t" \
  "v_exp_f32 v194, v194\n\t" \
  "v_exp_f32 v195, v195\n\t" \
  "v_exp_f32 v196, v196\n\t" \
  "v_mfma_f32_32x32x16_bf16 v[208:223], v[228:231], %[qb], 0\n\t" \
  "ds_read_b128 v[228:231], %[ka] offset:29120\n\t" \
  "ds_read_b128 v[232:235], %[va0] offset:24960\n\t" \
  "ds_read_b128 v[240:243], %[va1] offset:24960\n\t" \
  "v_exp_f32 v197, v197\n\t" \
  "v_exp_f32 v198, v198\n\t" \
  "v_exp_f32 v199, v199\n\t" \
  "buffer_load_dwordx4 v[104:107], %[vo], %[rs], %[so] offen sc1\n\t" \
  "s_add_u32 %[so], %[so], 1024\n\t" \
  "v_exp_f32 v200, v200\n\t" \
  "v_exp_f32 v201, v201\n\t" \
  "v_exp_f32 v202, v202\n\t" \
  "v_smfmac_f32_32x32x32_bf16 %[oa], v[236:239], v[192:199], v248\n\t" \
  "v_exp_f32 v203, v203\n\t" \
  "v_exp_f32 v204, v204\n\t" \
  "v_exp_f32 v205, v205\n\t" \
  "buffer_load_dwordx4 %[vo], %[rw], %[wo] offen lds\n\t" \
  "s_add_u32 %[wo], %[wo], 262144\n\t" \
  "v_exp_f32 v206, v206\n\t" \
  "v_exp_f32 v207, v207\n\t" \
  "v_exp_f32 v208, v208\n\t" \
  "v_exp_f32 v209, v209\n\t" \
  "v_smfmac_f32_32x32x32_bf16 %[oa], v[244:247], v[200:207], v248\n\t" \
  "v_exp_f32 v210, v210\n\t" \
  "v_exp_f32 v211, v211\n\t" \
  "v_exp_f32 v212, v212\n\t" \
  "v_mfma_f32_32x32x16_bf16 v[192:207], v[224:227], %[qa], 0\n\t" \
  "v_exp_f32 v213, v213\n\t" \
  "v_exp_f32 v214, v214\n\t" \
  "v_exp_f32 v215, v215\n\t" \
  "buffer_load_dwordx4 v[108:111], %[vo], %[rs], %[so] offen sc1\n\t" \
  "s_add_u32 %[so], %[so], 1024\n\t" \
  "v_exp_f32 v216, v216\n\t" \
  "v_exp_f32 v217, v217\n\t" \
  "v_exp_f32 v218, v218\n\t" \
  "v_smfmac_f32_32x32x32_bf16 %[ob], v[236:239], v[208:215], v248\n\t" \
  "v_exp_f32 v219, v219\n\t" \
  "v_exp_f32 v220, v220\n\t" \
  "v_exp_f32 v221, v221\n\t" \
  "v_exp_f32 v222, v222\n\t" \
  "v_exp_f32 v223, v223\n\t" \
  "s_waitcnt lgkmcnt(0)\n\t" \
  "v_exp_f32 v192, v192\n\t" \
  "v_exp_f32 v193, v193\n\t" \
  "v_smfmac_f32_32x32x32_bf16 %[ob], v[244:247], v[216:223], v248\n\t" \
  "v_exp_f32 v194, v194\n\t" \
  "v_exp_f32 v195, v195\n\t" \
  "v_exp_f32 v196, v196\n\t" \
  "v_mfma_f32_32x32x16_bf16 v[208:223], v[224:227], %[qb], 0\n\t" \
  "ds_read_b128 v[224:227], %[ka] offset:33280\n\t" \
  "ds_read_b128 v[236:239], %[va0] offset:29120\n\t" \
  "ds_read_b128 v[244:247], %[va1] offset:29120\n\t" \
  "v_exp_f32 v197, v197\n\t" \
  "v_exp_f32 v198, v198\n\t" \
  "v_exp_f32 v199, v199\n\t" \
  "buffer_load_dwordx4 v[112:115], %[vo], %[rs], %[so] offen sc1\n\t" \
  "s_add_u32 %[so], %[so], 1024\n\t" \
  "v_exp_f32 v200, v200\n\t" \
  "v_exp_f32 v201, v201\n\t" \
  "v_exp_f32 v202, v202\n\t" \
  "v_smfmac_f32_32x32x32_bf16 %[oa], v[232:235], v[192:199], v248\n\t" \
  "v_exp_f32 v203, v203\n\t" \
  "v_exp_f32 v204, v204\n\t" \
  "v_exp_f32 v205, v205\n\t" \
  "v_exp_f32 v206, v206\n\t" \
  "v_exp_f32 v207, v207\n\t" \
  "v_exp_f32 v208, v208\n\t" \
  "v_exp_f32 v209, v209\n\t" \
  "v_smfmac_f32_32x32x32_bf16 %[oa], v[240:243], v[200:207], v248\n\t" \
  "v_exp_f32 v210, v210\n\t" \
  "v_exp_f32 v211, v211\n\t" \
  "v_exp_f32 v212, v212\n\t" \
  "v_mfma_f32_32x32x16_bf16 v[192:207], v[228:231], %[qa], 0\n\t" \
  "v_exp_f32 v213, v213\n\t" \
  "v_exp_f32 v214, v214\n\t" \
  "v_exp_f32 v215, v215\n\t" \
  "buffer_load_dwordx4 v[116:119], %[vo], %[rs], %[so] offen sc1\n\t" \
  "s_add_u32 %[so], %[so], 1024\n\t" \
  "v_exp_f32 v216, v216\n\t" \
  "v_exp_f32 v217, v217\n\t" \
  "v_exp_f32 v218, v218\n\t" \
  "v_smfmac_f32_32x32x32_bf16 %[ob], v[232:235], v[208:215], v248\n\t" \
  "v_exp_f32 v219, v219\n\t" \
  "v_exp_f32 v220, v220\n\t" \
  "v_exp_f32 v221, v221\n\t" \
  "buffer_load_dwordx4 %[vo], %[rw], %[wo] offen lds\n\t" \
  "s_add_u32 %[wo], %[wo], 262144\n\t" \
  "v_exp_f32 v222, v222\n\t" \
  "v_exp_f32 v223, v223\n\t" \
  "s_waitcnt lgkmcnt(0)\n\t" \
  "v_exp_f32 v192, v192\n\t" \
  "v_exp_f32 v193, v193\n\t" \
  "v_smfmac_f32_32x32x32_bf16 %[ob], v[240:243], v[216:223], v248\n\t" \
  "v_exp_f32 v194, v194\n\t" \
  "v_exp_f32 v195, v195\n\t" \
  "v_exp_f32 v196, v196\n\t" \
  "v_mfma_f32_32x32x16_bf16 v[208:223], v[228:231], %[qb], 0\n\t" \
  "ds_read_b128 v[228:231], %[ka] offset:37440\n\t" \
  "ds_read_b128 v[232:235], %[va0] offset:33280\n\t" \
  "ds_read_b128 v[240:243], %[va1] offset:33280\n\t" \
  "v_exp_f32 v197, v197\n\t" \
  "v_exp_f32 v198, v198\n\t" \
  "v_exp_f32 v199, v199\n\t" \
  "buffer_load_dwordx4 v[120:123], %[vo], %[rs], %[so] offen sc1\n\t" \
  "s_add_u32 %[so], %[so], 1024\n\t" \
  "v_exp_f32 v200, v200\n\t" \
  "v_exp_f32 v201, v201\n\t" \
  "v_exp_f32 v202, v202\n\t" \
  "v_smfmac_f32_32x32x32_bf16 %[oa], v[236:239], v[192:199], v248\n\t" \
  "v_exp_f32 v203, v203\n\t" \
  "v_exp_f32 v204, v204\n\t" \
  "v_exp_f32 v205, v205\n\t" \
  "v_exp_f32 v206, v206\n\t" \
  "v_exp_f32 v207, v207\n\t" \
  "v_exp_f32 v208, v208\n\t" \
  "v_exp_f32 v209, v209\n\t" \
  "v_smfmac_f32_32x32x32_bf16 %[oa], v[244:247], v[200:207], v248\n\t" \
  "v_exp_f32 v210, v210\n\t" \
  "v_exp_f32 v211, v211\n\t" \
  "v_exp_f32 v212, v212\n\t" \
  "v_mfma_f32_32x32x16_bf16 v[192:207], v[224:227], %[qa], 0\n\t" \
  "v_exp_f32 v213, v213\n\t" \
  "v_exp_f32 v214, v214\n\t" \
  "v_exp_f32 v215, v215\n\t" \
  "buffer_load_dwordx4 v[124:127], %[vo], %[rs], %[so] offen sc1\n\t" \
  "s_add_u32 %[so], %[so], 1024\n\t" \
  "v_exp_f32 v216, v216\n\t" \
  "v_exp_f32 v217, v217\n\t" \
  "v_exp_f32 v218, v218\n\t" \
  "v_smfmac_f32_32x32x32_bf16 %[ob], v[236:239], v[208:215], v248\n\t" \
  "v_exp_f32 v219, v219\n\t" \
  "v_exp_f32 v220, v220\n\t" \
  "v_exp_f32 v221, v221\n\t" \
  "v_exp_f32 v222, v222\n\t" \
  "v_exp_f32 v223, v223\n\t" \
  "v_add_u32 %[ka], 33280, %[ka]\n\t" \
  "v_add_u32 %[va0], 33280, %[va0]\n\t" \
  "v_add_u32 %[va1], 33280, %[va1]\n\t" \
  "s_waitcnt lgkmcnt(0)\n\t" \
  "v_exp_f32 v192, v192\n\t" \
  "v_exp_f32 v193, v193\n\t" \
  "v_smfmac_f32_32x32x32_bf16 %[ob], v[244:247], v[216:223], v248\n\t" \
  "v_exp_f32 v194, v194\n\t" \
  "v_exp_f32 v195, v195\n\t" \
  "v_exp_f32 v196, v196\n\t" \
  "v_mfma_f32_32x32x16_bf16 v[208:223], v[224:227], %[qb], 0\n\t" \
  "ds_read_b128 v[224:227], %[ka] offset:8320\n\t" \
  "ds_read_b128 v[236:239], %[va0] offset:4160\n\t" \
  "ds_read_b128 v[244:247], %[va1] offset:4160\n\t" \
  "v_exp_f32 v197, v197\n\t" \
  "v_exp_f32 v198, v198\n\t" \
  "v_exp_f32 v199, v199\n\t" \
  "s_mov_b32 %[so], %[p1]\n\t" \
  "buffer_load_dwordx4 v[128:131], %[vo], %[rs], %[so] offen sc1\n\t" \
  "s_add_u32 %[so], %[so], 1024\n\t" \
  "v_exp_f32 v200, v200\n\t" \
  "v_exp_f32 v201, v201\n\t" \
  "v_exp_f32 v202, v202\n\t" \
  "v_smfmac_f32_32x32x32_bf16 %[oa], v[232:235], v[192:199], v248\n\t" \
  "v_exp_f32 v203, v203\n\t" \
  "v_exp_f32 v204, v204\n\t" \
  "v_exp_f32 v205, v205\n\t" \
  "v_exp_f32 v206, v206\n\t" \
  "v_exp_f32 v207, v207\n\t" \
  "v_exp_f32 v208, v208\n\t" \
  "v_exp_f32 v209, v209\n\t" \
  "v_smfmac_f32_32x32x32_bf16 %[oa], v[240:243], v[200:207], v248\n\t" \
  "v_exp_f32 v210, v210\n\t" \
  "v_exp_f32 v211, v211\n\t" \
  "v_exp_f32 v212, v212\n\t" \
  "v_mfma_f32_32x32x16_bf16 v[192:207], v[228:231], %[qa], 0\n\t" \
  "v_exp_f32 v213, v213\n\t" \
  "v_exp_f32 v214, v214\n\t" \
  "v_exp_f32 v215, v215\n\t" \
  "buffer_load_dwordx4 v[132:135], %[vo], %[rs], %[so] offen sc1\n\t" \
  "s_add_u32 %[so], %[so], 1024\n\t" \
  "v_exp_f32 v216, v216\n\t" \
  "v_exp_f32 v217, v217\n\t" \
  "v_exp_f32 v218, v218\n\t" \
  "v_smfmac_f32_32x32x32_bf16 %[ob], v[232:235], v[208:215], v248\n\t" \
  "v_exp_f32 v219, v219\n\t" \
  "v_exp_f32 v220, v220\n\t" \
  "v_exp_f32 v221, v221\n\t" \
  "buffer_load_dwordx4 %[vo], %[rw], %[wo] offen lds\n\t" \
  "s_add_u32 %[wo], %[wo], 262144\n\t" \
  "v_exp_f32 v222, v222\n\t" \
  "v_exp_f32 v223, v223\n\t" \
  "s_waitcnt lgkmcnt(0)\n\t" \
  "v_exp_f32 v192, v192\n\t" \
  "v_exp_f32 v193, v193\n\t" \
  "v_smfmac_f32_32x32x32_bf16 %[ob], v[240:243], v[216:223], v248\n\t" \
  "v_exp_f32 v194, v194\n\t" \
  "v_exp_f32 v195, v195\n\t" \
  "v_exp_f32 v196, v196\n\t" \
  "v_mfma_f32_32x32x16_bf16 v[208:223], v[228:231], %[qb], 0\n\t" \
  "ds_read_b128 v[228:231], %[ka] offset:12480\n\t" \
  "ds_read_b128 v[232:235], %[va0] offset:8320\n\t" \
  "ds_read_b128 v[240:243], %[va1] offset:8320\n\t" \
  "v_exp_f32 v197, v197\n\t" \
  "v_exp_f32 v198, v198\n\t" \
  "v_exp_f32 v199, v199\n\t" \
  "buffer_load_dwordx4 v[136:139], %[vo], %[rs], %[so] offen sc1\n\t" \
  "s_add_u32 %[so], %[so], 1024\n\t" \
  "v_exp_f32 v200, v200\n\t" \
  "v_exp_f32 v201, v201\n\t" \
  "v_exp_f32 v202, v202\n\t" \
  "v_smfmac_f32_32x32x32_bf16 %[oa], v[236:239], v[192:199], v248\n\t" \
  "v_exp_f32 v203, v203\n\t" \
  "v_exp_f32 v204, v204\n\t" \
  "v_exp_f32 v205, v205\n\t" \
  "v_exp_f32 v206, v206\n\t" \
  "v_exp_f32 v207, v207\n\t" \
  "v_exp_f32 v208, v208\n\t" \
  "v_exp_f32 v209, v209\n\t" \
  "v_smfmac_f32_32x32x32_bf16 %[oa], v[244:247], v[200:207], v248\n\t" \
  "v_exp_f32 v210, v210\n\t" \
  "v_exp_f32 v211, v211\n\t" \
  "v_exp_f32 v212, v212\n\t" \
  "v_mfma_f32_32x32x16_bf16 v[192:207], v[224:227], %[qa], 0\n\t" \
  "v_exp_f32 v213, v213\n\t" \
  "v_exp_f32 v214, v214\n\t" \
  "v_exp_f32 v215, v215\n\t" \
  "buffer_load_dwordx4 v[140:143], %[vo], %[rs], %[so] offen sc1\n\t" \
  "s_add_u32 %[so], %[so], 1024\n\t" \
  "v_exp_f32 v216, v216\n\t" \
  "v_exp_f32 v217, v217\n\t" \
  "v_exp_f32 v218, v218\n\t" \
  "v_smfmac_f32_32x32x32_bf16 %[ob], v[236:239], v[208:215], v248\n\t" \
  "v_exp_f32 v219, v219\n\t" \
  "v_exp_f32 v220, v220\n\t" \
  "v_exp_f32 v221, v221\n\t" \
  "buffer_load_dwordx4 %[vo], %[rw], %[wo] offen lds\n\t" \
  "s_add_u32 %[wo], %[wo], 262144\n\t" \
  "v_exp_f32 v222, v222\n\t" \
  "v_exp_f32 v223, v223\n\t" \
  "s_waitcnt lgkmcnt(0)\n\t" \
  "v_exp_f32 v192, v192\n\t" \
  "v_exp_f32 v193, v193\n\t" \
  "v_smfmac_f32_32x32x32_bf16 %[ob], v[244:247], v[216:223], v248\n\t" \
  "v_exp_f32 v194, v194\n\t" \
  "v_exp_f32 v195, v195\n\t" \
  "v_exp_f32 v196, v196\n\t" \
  "v_mfma_f32_32x32x16_bf16 v[208:223], v[224:227], %[qb], 0\n\t" \
  "ds_read_b128 v[224:227], %[ka] offset:16640\n\t" \
  "ds_read_b128 v[236:239], %[va0] offset:12480\n\t" \
  "ds_read_b128 v[244:247], %[va1] offset:12480\n\t" \
  "v_exp_f32 v197, v197\n\t" \
  "v_exp_f32 v198, v198\n\t" \
  "v_exp_f32 v199, v199\n\t" \
  "buffer_load_dwordx4 v[144:147], %[vo], %[rs], %[so] offen sc1\n\t" \
  "s_add_u32 %[so], %[so], 1024\n\t" \
  "v_exp_f32 v200, v200\n\t" \
  "v_exp_f32 v201, v201\n\t" \
  "v_exp_f32 v202, v202\n\t" \
  "v_smfmac_f32_32x32x32_bf16 %[oa], v[232:235], v[192:199], v248\n\t" \
  "v_exp_f32 v203, v203\n\t" \
  "v_exp_f32 v204, v204\n\t" \
  "v_exp_f32 v205, v205\n\t" \
  "v_exp_f32 v206, v206\n\t" \
  "v_exp_f32 v207, v207\n\t" \
  "v_exp_f32 v208, v208\n\t" \
  "v_exp_f32 v209, v209\n\t" \
  "v_smfmac_f32_32x32x32_bf16 %[oa], v[240:243], v[200:207], v248\n\t" \
  "v_exp_f32 v210, v210\n\t" \
  "v_exp_f32 v211, v211\n\t" \
  "v_exp_f32 v212, v212\n\t" \
  "v_mfma_f32_32x32x16_bf16 v[192:207], v[228:231], %[qa], 0\n\t" \
  "v_exp_f32 v213, v213\n\t" \
  "v_exp_f32 v214, v214\n\t" \
  "v_exp_f32 v215, v215\n\t" \
  "buffer_load_dwordx4 v[148:151], %[vo], %[rs], %[so] offen sc1\n\t" \
  "s_add_u32 %[so], %[so], 1024\n\t" \
  "v_exp_f32 v216, v216\n\t" \
  "v_exp_f32 v217, v217\n\t" \
  "v_exp_f32 v218, v218\n\t" \
  "v_smfmac_f32_32x32x32_bf16 %[ob], v[232:235], v[208:215], v248\n\t" \
  "v_exp_f32 v219, v219\n\t" \
  "v_exp_f32 v220, v220\n\t" \
  "v_exp_f32 v221, v221\n\t" \
  "buffer_load_dwordx4 %[vo], %[rw], %[wo] offen lds\n\t" \
  "s_add_u32 %[wo], %[wo], 262144\n\t" \
  "v_exp_f32 v222, v222\n\t" \
  "v_exp_f32 v223, v223\n\t" \
  "s_waitcnt lgkmcnt(0)\n\t" \
  "v_exp_f32 v192, v192\n\t" \
  "v_exp_f32 v193, v193\n\t" \
  "v_smfmac_f32_32x32x32_bf16 %[ob], v[240:243], v[216:223], v248\n\t" \
  "v_exp_f32 v194, v194\n\t" \
  "v_exp_f32 v195, v195\n\t" \
  "v_exp_f32 v196, v196\n\t" \
  "v_mfma_f32_32x32x16_bf16 v[208:223], v[228:231], %[qb], 0\n\t" \
  "ds_read_b128 v[228:231], %[ka] offset:20800\n\t" \
  "ds_read_b128 v[232:235], %[va0] offset:16640\n\t" \
  "ds_read_b128 v[240:243], %[va1] offset:16640\n\t" \
  "v_exp_f32 v197, v197\n\t" \
  "v_exp_f32 v198, v198\n\t" \
  "v_exp_f32 v199, v199\n\t" \
  "buffer_load_dwordx4 v[152:155], %[vo], %[rs], %[so] offen sc1\n\t" \
  "s_add_u32 %[so], %[so], 1024\n\t" \
  "v_exp_f32 v200, v200\n\t" \
  "v_exp_f32 v201, v201\n\t" \
  "v_exp_f32 v202, v202\n\t" \
  "v_smfmac_f32_32x32x32_bf16 %[oa], v[236:239], v[192:199], v248\n\t" \
  "v_exp_f32 v203, v203\n\t" \
  "v_exp_f32 v204, v204\n\t" \
  "v_exp_f32 v205, v205\n\t" \
  "v_exp_f32 v206, v206\n\t" \
  "v_exp_f32 v207, v207\n\t" \
  "v_exp_f32 v208, v208\n\t" \
  "v_exp_f32 v209, v209\n\t" \
  "v_smfmac_f32_32x32x32_bf16 %[oa], v[244:247], v[200:207], v248\n\t" \
  "v_exp_f32 v210, v210\n\t" \
  "v_exp_f32 v211, v211\n\t" \
  "v_exp_f32 v212, v212\n\t" \
  "v_mfma_f32_32x32x16_bf16 v[192:207], v[224:227], %[qa], 0\n\t" \
  "v_exp_f32 v213, v213\n\t" \
  "v_exp_f32 v214, v214\n\t" \
  "v_exp_f32 v215, v215\n\t" \
  "buffer_load_dwordx4 v[156:159], %[vo], %[rs], %[so] offen sc1\n\t" \
  "s_add_u32 %[so], %[so], 1024\n\t" \
  "v_exp_f32 v216, v216\n\t" \
  "v_exp_f32 v217, v217\n\t" \
  "v_exp_f32 v218, v218\n\t" \
  "v_smfmac_f32_32x32x32_bf16 %[ob], v[236:239], v[208:215], v248\n\t" \
  "v_exp_f32 v219, v219\n\t" \
  "v_exp_f32 v220, v220\n\t" \
  "v_exp_f32 v221, v221\n\t" \
  "buffer_load_dwordx4 %[vo], %[rw], %[wo] offen lds\n\t" \
  "s_add_u32 %[wo], %[wo], 262144\n\t" \
  "v_exp_f32 v222, v222\n\t" \
  "v_exp_f32 v223, v223\n\t" \
  "s_waitcnt lgkmcnt(0)\n\t" \
  "v_exp_f32 v192, v192\n\t" \
  "v_exp_f32 v193, v193\n\t" \
  "v_smfmac_f32_32x32x32_bf16 %[ob], v[244:247], v[216:223], v248\n\t" \
  "v_exp_f32 v194, v194\n\t" \
  "v_exp_f32 v195, v195\n\t" \
  "v_exp_f32 v196, v196\n\t" \
  "v_mfma_f32_32x32x16_bf16 v[208:223], v[224:227], %[qb], 0\n\t" \
  "ds_read_b128 v[224:227], %[ka] offset:24960\n\t" \
  "ds_read_b128 v[236:239], %[va0] offset:20800\n\t" \
  "ds_read_b128 v[244:247], %[va1] offset:20800\n\t" \
  "v_exp_f32 v197, v197\n\t" \
  "v_exp_f32 v198, v198\n\t" \
  "v_exp_f32 v199, v199\n\t" \
  "buffer_load_dwordx4 v[160:163], %[vo], %[rs], %[so] offen sc1\n\t" \
  "s_add_u32 %[so], %[so], 1024\n\t" \
  "v_exp_f32 v200, v200\n\t" \
  "v_exp_f32 v201, v201\n\t" \
  "v_exp_f32 v202, v202\n\t" \
  "v_smfmac_f32_32x32x32_bf16 %[oa], v[232:235], v[192:199], v248\n\t" \
  "v_exp_f32 v203, v203\n\t" \
  "v_exp_f32 v204, v204\n\t" \
  "v_exp_f32 v205, v205\n\t" \
  "v_exp_f32 v206, v206\n\t" \
  "v_exp_f32 v207, v207\n\t" \
  "v_exp_f32 v208, v208\n\t" \
  "v_exp_f32 v209, v209\n\t" \
  "v_smfmac_f32_32x32x32_bf16 %[oa], v[240:243], v[200:207], v248\n\t" \
  "v_exp_f32 v210, v210\n\t" \
  "v_exp_f32 v211, v211\n\t" \
  "v_exp_f32 v212, v212\n\t" \
  "v_mfma_f32_32x32x16_bf16 v[192:207], v[228:231], %[qa], 0\n\t" \
  "v_exp_f32 v213, v213\n\t" \
  "v_exp_f32 v214, v214\n\t" \
  "v_exp_f32 v215, v215\n\t" \
  "s_mov_b32 %[so], %[p2]\n\t" \
  "buffer_load_dwordx4 v[164:167], %[vo], %[rs], %[so] offen sc1\n\t" \
  "s_add_u32 %[so], %[so], 1024\n\t" \
  "v_exp_f32 v216, v216\n\t" \
  "v_exp_f32 v217, v217\n\t" \
  "v_exp_f32 v218, v218\n\t" \
  "v_smfmac_f32_32x32x32_bf16 %[ob], v[232:235], v[208:215], v248\n\t" \
  "v_exp_f32 v219, v219\n\t" \
  "v_exp_f32 v220, v220\n\t" \
  "v_exp_f32 v221, v221\n\t" \
  "buffer_load_dwordx4 %[vo], %[rw], %[wo] offen lds\n\t" \
  "s_add_u32 %[wo], %[wo], 262144\n\t" \
  "v_exp_f32 v222, v222\n\t" \
  "v_exp_f32 v223, v223\n\t" \
  "s_waitcnt lgkmcnt(0)\n\t" \
  "v_exp_f32 v192, v192\n\t" \
  "v_exp_f32 v193, v193\n\t" \
  "v_smfmac_f32_32x32x32_bf16 %[ob], v[240:243], v[216:223], v248\n\t" \
  "v_exp_f32 v194, v194\n\t" \
  "v_exp_f32 v195, v195\n\t" \
  "v_exp_f32 v196, v196\n\t" \
  "v_mfma_f32_32x32x16_bf16 v[208:223], v[228:231], %[qb], 0\n\t" \
  "ds_read_b128 v[228:231], %[ka] offset:29120\n\t" \
  "ds_read_b128 v[232:235], %[va0] offset:24960\n\t" \
  "ds_read_b128 v[240:243], %[va1] offset:24960\n\t" \
  "v_exp_f32 v197, v197\n\t" \
  "v_exp_f32 v198, v198\n\t" \
  "v_exp_f32 v199, v199\n\t" \
  "buffer_load_dwordx4 v[168:171], %[vo], %[rs], %[so] offen sc1\n\t" \
  "s_add_u32 %[so], %[so], 1024\n\t" \
  "v_exp_f32 v200, v200\n\t" \
  "v_exp_f32 v201, v201\n\t" \
  "v_exp_f32 v202, v202\n\t" \
  "v_smfmac_f32_32x32x32_bf16 %[oa], v[236:239], v[192:199], v248\n\t" \
  "v_exp_f32 v203, v203\n\t" \
  "v_exp_f32 v204, v204\n\t" \
  "v_exp_f32 v205, v205\n\t" \
  "v_exp_f32 v206, v206\n\t" \
  "v_exp_f32 v207, v207\n\t" \
  "v_exp_f32 v208, v208\n\t" \
  "v_exp_f32 v209, v209\n\t" \
  "v_smfmac_f32_32x32x32_bf16 %[oa], v[244:247], v[200:207], v248\n\t" \
  "v_exp_f32 v210, v210\n\t" \
  "v_exp_f32 v211, v211\n\t" \
  "v_exp_f32 v212, v212\n\t" \
  "v_mfma_f32_32x32x16_bf16 v[192:207], v[224:227], %[qa], 0\n\t" \
  "v_exp_f32 v213, v213\n\t" \
  "v_exp_f32 v214, v214\n\t" \
  "v_exp_f32 v215, v215\n\t" \
  "buffer_load_dwordx4 v[172:175], %[vo], %[rs], %[so] offen sc1\n\t" \
  "s_add_u32 %[so], %[so], 1024\n\t" \
  "v_exp_f32 v216, v216\n\t" \
  "v_exp_f32 v217, v217\n\t" \
  "v_exp_f32 v218, v218\n\t" \
  "v_smfmac_f32_32x32x32_bf16 %[ob], v[236:239], v[208:215], v248\n\t" \
  "v_exp_f32 v219, v219\n\t" \
  "v_exp_f32 v220, v220\n\t" \
  "v_exp_f32 v221, v221\n\t" \
  "buffer_load_dwordx4 %[vo], %[rw], %[wo] offen lds\n\t" \
  "s_add_u32 %[wo], %[wo], 262144\n\t" \
  "v_exp_f32 v222, v222\n\t" \
  "v_exp_f32 v223, v223\n\t" \
  "s_waitcnt lgkmcnt(0)\n\t" \
  "v_exp_f32 v192, v192\n\t" \
  "v_exp_f32 v193, v193\n\t" \
  "v_smfmac_f32_32x32x32_bf16 %[ob], v[244:247], v[216:223], v248\n\t" \
  "v_exp_f32 v194, v194\n\t" \
  "v_exp_f32 v195, v195\n\t" \
  "v_exp_f32 v196, v196\n\t" \
  "v_mfma_f32_32x32x16_bf16 v[208:223], v[224:227], %[qb], 0\n\t" \
  "ds_read_b128 v[224:227], %[ka] offset:33280\n\t" \
  "ds_read_b128 v[236:239], %[va0] offset:29120\n\t" \
  "ds_read_b128 v[244:247], %[va1] offset:29120\n\t" \
  "v_exp_f32 v197, v197\n\t" \
  "v_exp_f32 v198, v198\n\t" \
  "v_exp_f32 v199, v199\n\t" \
  "buffer_load_dwordx4 v[176:179], %[vo], %[rs], %[so] offen sc1\n\t" \
  "s_add_u32 %[so], %[so], 1024\n\t" \
  "v_exp_f32 v200, v200\n\t" \
  "v_exp_f32 v201, v201\n\t" \
  "v_exp_f32 v202, v202\n\t" \
  "v_smfmac_f32_32x32x32_bf16 %[oa], v[232:235], v[192:199], v248\n\t" \
  "v_exp_f32 v203, v203\n\t" \
  "v_exp_f32 v204, v204\n\t" \
  "v_exp_f32 v205, v205\n\t" \
  "v_exp_f32 v206, v206\n\t" \
  "v_exp_f32 v207, v207\n\t" \
  "v_exp_f32 v208, v208\n\t" \
  "v_exp_f32 v209, v209\n\t" \
  "v_smfmac_f32_32x32x32_bf16 %[oa], v[240:243], v[200:207], v248\n\t" \
  "v_exp_f32 v210, v210\n\t" \
  "v_exp_f32 v211, v211\n\t" \
  "v_exp_f32 v212, v212\n\t" \
  "v_mfma_f32_32x32x16_bf16 v[192:207], v[228:231], %[qa], 0\n\t" \
  "v_exp_f32 v213, v213\n\t" \
  "v_exp_f32 v214, v214\n\t" \
  "v_exp_f32 v215, v215\n\t" \
  "buffer_load_dwordx4 v[180:183], %[vo], %[rs], %[so] offen sc1\n\t" \
  "s_add_u32 %[so], %[so], 1024\n\t" \
  "v_exp_f32 v216, v216\n\t" \
  "v_exp_f32 v217, v217\n\t" \
  "v_exp_f32 v218, v218\n\t" \
  "v_smfmac_f32_32x32x32_bf16 %[ob], v[232:235], v[208:215], v248\n\t" \
  "v_exp_f32 v219, v219\n\t" \
  "v_exp_f32 v220, v220\n\t" \
  "v_exp_f32 v221, v221\n\t" \
  "buffer_load_dwordx4 %[vo], %[rw], %[wo] offen lds\n\t" \
  "s_add_u32 %[wo], %[wo], 262144\n\t" \
  "v_exp_f32 v222, v222\n\t" \
  "v_exp_f32 v223, v223\n\t" \
  "s_waitcnt lgkmcnt(0)\n\t" \
  "v_exp_f32 v192, v192\n\t" \
  "v_exp_f32 v193, v193\n\t" \
  "v_smfmac_f32_32x32x32_bf16 %[ob], v[240:243], v[216:223], v248\n\t" \
  "v_exp_f32 v194, v194\n\t" \
  "v_exp_f32 v195, v195\n\t" \
  "v_exp_f32 v196, v196\n\t" \
  "v_mfma_f32_32x32x16_bf16 v[208:223], v[228:231], %[qb], 0\n\t" \
  "ds_read_b128 v[228:231], %[ka] offset:37440\n\t" \
  "ds_read_b128 v[232:235], %[va0] offset:33280\n\t" \
  "ds_read_b128 v[240:243], %[va1] offset:33280\n\t" \
  "v_exp_f32 v197, v197\n\t" \
  "v_exp_f32 v198, v198\n\t" \
  "v_exp_f32 v199, v199\n\t" \
  "buffer_load_dwordx4 v[184:187], %[vo], %[rs], %[so] offen sc1\n\t" \
  "s_add_u32 %[so], %[so], 1024\n\t" \
  "v_exp_f32 v200, v200\n\t" \
  "v_exp_f32 v201, v201\n\t" \
  "v_exp_f32 v202, v202\n\t" \
  "v_smfmac_f32_32x32x32_bf16 %[oa], v[236:239], v[192:199], v248\n\t" \
  "v_exp_f32 v203, v203\n\t" \
  "v_exp_f32 v204, v204\n\t" \
  "v_exp_f32 v205, v205\n\t" \
  "v_exp_f32 v206, v206\n\t" \
  "v_exp_f32 v207, v207\n\t" \
  "v_exp_f32 v208, v208\n\t" \
  "v_exp_f32 v209, v209\n\t" \
  "v_smfmac_f32_32x32x32_bf16 %[oa], v[244:247], v[200:207], v248\n\t" \
  "v_exp_f32 v210, v210\n\t" \
  "v_exp_f32 v211, v211\n\t" \
  "v_exp_f32 v212, v212\n\t" \
  "v_mfma_f32_32x32x16_bf16 v[192:207], v[224:227], %[qa], 0\n\t" \
  "v_exp_f32 v213, v213\n\t" \
  "v_exp_f32 v214, v214\n\t" \
  "v_exp_f32 v215, v215\n\t" \
  "buffer_load_dwordx4 v[188:191], %[vo], %[rs], %[so] offen sc1\n\t" \
  "s_add_u32 %[so], %[so], 1024\n\t" \
  "v_exp_f32 v216, v216\n\t" \
  "v_exp_f32 v217, v217\n\t" \
  "v_exp_f32 v218, v218\n\t" \
  "v_smfmac_f32_32x32x32_bf16 %[ob], v[236:239], v[208:215], v248\n\t" \
  "v_exp_f32 v219, v219\n\t" \
  "v_exp_f32 v220, v220\n\t" \
  "v_exp_f32 v221, v221\n\t" \
  "v_exp_f32 v222, v222\n\t" \
  "v_exp_f32 v223, v223\n\t" \
  "v_add_u32 %[ka], 33280, %[ka]\n\t" \
  "v_add_u32 %[va0], 33280, %[va0]\n\t" \
  "v_add_u32 %[va1], 33280, %[va1]\n\t" \
  "1:\n\t" \
  "s_waitcnt lgkmcnt(0)\n\t" \
  "v_exp_f32 v192, v192\n\t" \
  "v_exp_f32 v193, v193\n\t" \
  "v_smfmac_f32_32x32x32_bf16 %[ob], v[244:247], v[216:223], v248\n\t" \
  "v_exp_f32 v194, v194\n\t" \
  "v_exp_f32 v195, v195\n\t" \
  "v_exp_f32 v196, v196\n\t" \
  "v_mfma_f32_32x32x16_bf16 v[208:223], v[224:227], %[qb], 0\n\t" \
  "ds_read_b128 v[224:227], %[ka] offset:8320\n\t" \
  "ds_read_b128 v[236:239], %[va0] offset:4160\n\t" \
  "ds_read_b128 v[244:247], %[va1] offset:4160\n\t" \
  "v_exp_f32 v197, v197\n\t" \
  "v_exp_f32 v198, v198\n\t" \
  "v_exp_f32 v199, v199\n\t" \
  "v_exp_f32 v200, v200\n\t" \
  "v_exp_f32 v201, v201\n\t" \
  "v_exp_f32 v202, v202\n\t" \
  "v_smfmac_f32_32x32x32_bf16 %[oa], v[232:235], v[192:199], v248\n\t" \
  "v_exp_f32 v203, v203\n\t" \
  "v_exp_f32 v204, v204\n\t" \
  "v_exp_f32 v205, v205\n\t" \
  "v_exp_f32 v206, v206\n\t" \
  "v_exp_f32 v207, v207\n\t" \
  "v_exp_f32 v208, v208\n\t" \
  "v_exp_f32 v209, v209\n\t" \
  "v_smfmac_f32_32x32x32_bf16 %[oa], v[240:243], v[200:207], v248\n\t" \
  "v_exp_f32 v210, v210\n\t" \
  "v_exp_f32 v211, v211\n\t" \
  "v_exp_f32 v212, v212\n\t" \
  "v_mfma_f32_32x32x16_bf16 v[192:207], v[228:231], %[qa], 0\n\t" \
  "v_exp_f32 v213, v213\n\t" \
  "v_exp_f32 v214, v214\n\t" \
  "v_exp_f32 v215, v215\n\t" \
  "v_exp_f32 v216, v216\n\t" \
  "v_exp_f32 v217, v217\n\t" \
  "v_exp_f32 v218, v218\n\t" \
  "v_smfmac_f32_32x32x32_bf16 %[ob], v[232:235], v[208:215], v248\n\t" \
  "v_exp_f32 v219, v219\n\t" \
  "v_exp_f32 v220, v220\n\t" \
  "v_exp_f32 v221, v221\n\t" \
  "v_exp_f32 v222, v222\n\t" \
  "v_exp_f32 v223, v223\n\t" \
  "s_waitcnt lgkmcnt(0)\n\t" \
  "v_exp_f32 v192, v192\n\t" \
  "v_exp_f32 v193, v193\n\t" \
  "v_smfmac_f32_32x32x32_bf16 %[ob], v[240:243], v[216:223], v248\n\t" \
  "v_exp_f32 v194, v194\n\t" \
  "v_exp_f32 v195, v195\n\t" \
  "v_exp_f32 v196, v196\n\t" \
  "v_mfma_f32_32x32x16_bf16 v[208:223], v[228:231], %[qb], 0\n\t" \
  "ds_read_b128 v[228:231], %[ka] offset:12480\n\t" \
  "ds_read_b128 v[232:235], %[va0] offset:8320\n\t" \
  "ds_read_b128 v[240:243], %[va1] offset:8320\n\t" \
  "v_exp_f32 v197, v197\n\t" \
  "v_exp_f32 v198, v198\n\t" \
  "v_exp_f32 v199, v199\n\t" \
  "v_exp_f32 v200, v200\n\t" \
  "v_exp_f32 v201, v201\n\t" \
  "v_exp_f32 v202, v202\n\t" \
  "v_smfmac_f32_32x32x32_bf16 %[oa], v[236:239], v[192:199], v248\n\t" \
  "v_exp_f32 v203, v203\n\t" \
  "v_exp_f32 v204, v204\n\t" \
  "v_exp_f32 v205, v205\n\t" \
  "v_exp_f32 v206, v206\n\t" \
  "v_exp_f32 v207, v207\n\t" \
  "v_exp_f32 v208, v208\n\t" \
  "v_exp_f32 v209, v209\n\t" \
  "v_smfmac_f32_32x32x32_bf16 %[oa], v[244:247], v[200:207], v248\n\t" \
  "v_exp_f32 v210, v210\n\t" \
  "v_exp_f32 v211, v211\n\t" \
  "v_exp_f32 v212, v212\n\t" \
  "v_mfma_f32_32x32x16_bf16 v[192:207], v[224:227], %[qa], 0\n\t" \
  "v_exp_f32 v213, v213\n\t" \
  "v_exp_f32 v214, v214\n\t" \
  "v_exp_f32 v215, v215\n\t" \
  "v_exp_f32 v216, v216\n\t" \
  "v_exp_f32 v217, v217\n\t" \
  "v_exp_f32 v218, v218\n\t" \
  "v_smfmac_f32_32x32x32_bf16 %[ob], v[236:239], v[208:215], v248\n\t" \
  "v_exp_f32 v219, v219\n\t" \
  "v_exp_f32 v220, v220\n\t" \
  "v_exp_f32 v221, v221\n\t" \
  "v_exp_f32 v222, v222\n\t" \
  "v_exp_f32 v223, v223\n\t" \
  "s_waitcnt lgkmcnt(0)\n\t" \
  "v_exp_f32 v192, v192\n\t" \
  "v_exp_f32 v193, v193\n\t" \
  "v_smfmac_f32_32x32x32_bf16 %[ob], v[244:247], v[216:223], v248\n\t" \
  "v_exp_f32 v194, v194\n\t" \
  "v_exp_f32 v195, v195\n\t" \
  "v_exp_f32 v196, v196\n\t" \
  "v_mfma_f32_32x32x16_bf16 v[208:223], v[224:227], %[qb], 0\n\t" \
  "ds_read_b128 v[224:227], %[ka] offset:16640\n\t" \
  "ds_read_b128 v[236:239], %[va0] offset:12480\n\t" \
  "ds_read_b128 v[244:247], %[va1] offset:12480\n\t" \
  "v_exp_f32 v197, v197\n\t" \
  "v_exp_f32 v198, v198\n\t" \
  "v_exp_f32 v199, v199\n\t" \
  "v_exp_f32 v200, v200\n\t" \
  "v_exp_f32 v201, v201\n\t" \
  "v_exp_f32 v202, v202\n\t" \
  "v_smfmac_f32_32x32x32_bf16 %[oa], v[232:235], v[192:199], v248\n\t" \
  "v_exp_f32 v203, v203\n\t" \
  "v_exp_f32 v204, v204\n\t" \
  "v_exp_f32 v205, v205\n\t" \
  "v_exp_f32 v206, v206\n\t" \
  "v_exp_f32 v207, v207\n\t" \
  "v_exp_f32 v208, v208\n\t" \
  "v_exp_f32 v209, v209\n\t" \
  "v_smfmac_f32_32x32x32_bf16 %[oa], v[240:243], v[200:207], v248\n\t" \
  "v_exp_f32 v210, v210\n\t" \
  "v_exp_f32 v211, v211\n\t" \
  "v_exp_f32 v212, v212\n\t" \
  "v_mfma_f32_32x32x16_bf16 v[192:207], v[228:231], %[qa], 0\n\t" \
  "v_exp_f32 v213, v213\n\t" \
  "v_exp_f32 v214, v214\n\t" \
  "v_exp_f32 v215, v215\n\t" \
  "v_exp_f32 v216, v216\n\t" \
  "v_exp_f32 v217, v217\n\t" \
  "v_exp_f32 v218, v218\n\t" \
  "v_smfmac_f32_32x32x32_bf16 %[ob], v[232:235], v[208:215], v248\n\t" \
  "v_exp_f32 v219, v219\n\t" \
  "v_exp_f32 v220, v220\n\t" \
  "v_exp_f32 v221, v221\n\t" \
  "v_exp_f32 v222, v222\n\t" \
  "v_exp_f32 v223, v223\n\t" \
  "s_waitcnt lgkmcnt(0)\n\t" \
  "v_exp_f32 v192, v192\n\t" \
  "v_exp_f32 v193, v193\n\t" \
  "v_smfmac_f32_32x32x32_bf16 %[ob], v[240:243], v[216:223], v248\n\t" \
  "v_exp_f32 v194, v194\n\t" \
  "v_exp_f32 v195, v195\n\t" \
  "v_exp_f32 v196, v196\n\t" \
  "v_mfma_f32_32x32x16_bf16 v[208:223], v[228:231], %[qb], 0\n\t" \
  "ds_read_b128 v[228:231], %[ka] offset:20800\n\t" \
  "ds_read_b128 v[232:235], %[va0] offset:16640\n\t" \
  "ds_read_b128 v[240:243], %[va1] offset:16640\n\t" \
  "v_exp_f32 v197, v197\n\t" \
  "v_exp_f32 v198, v198\n\t" \
  "v_exp_f32 v199, v199\n\t" \
  "v_exp_f32 v200, v200\n\t" \
  "v_exp_f32 v201, v201\n\t" \
  "v_exp_f32 v202, v202\n\t" \
  "v_smfmac_f32_32x32x32_bf16 %[oa], v[236:239], v[192:199], v248\n\t" \
  "v_exp_f32 v203, v203\n\t" \
  "v_exp_f32 v204, v204\n\t" \
  "v_exp_f32 v205, v205\n\t" \
  "v_exp_f32 v206, v206\n\t" \
  "v_exp_f32 v207, v207\n\t" \
  "v_exp_f32 v208, v208\n\t" \
  "v_exp_f32 v209, v209\n\t" \
  "v_smfmac_f32_32x32x32_bf16 %[oa], v[244:247], v[200:207], v248\n\t" \
  "v_exp_f32 v210, v210\n\t" \
  "v_exp_f32 v211, v211\n\t" \
  "v_exp_f32 v212, v212\n\t" \
  "v_mfma_f32_32x32x16_bf16 v[192:207], v[224:227], %[qa], 0\n\t" \
  "v_exp_f32 v213, v213\n\t" \
  "v_exp_f32 v214, v214\n\t" \
  "v_exp_f32 v215, v215\n\t" \
  "v_exp_f32 v216, v216\n\t" \
  "v_exp_f32 v217, v217\n\t" \
  "v_exp_f32 v218, v218\n\t" \
  "v_smfmac_f32_32x32x32_bf16 %[ob], v[236:239], v[208:215], v248\n\t" \
  "v_exp_f32 v219, v219\n\t" \
  "v_exp_f32 v220, v220\n\t" \
  "v_exp_f32 v221, v221\n\t" \
  "v_exp_f32 v222, v222\n\t" \
  "v_exp_f32 v223, v223\n\t" \
  "v_add_u32 %[ka], 16640, %[ka]\n\t" \
  "v_add_u32 %[va0], 16640, %[va0]\n\t" \
  "v_add_u32 %[va1], 16640, %[va1]\n\t" \
  "s_add_u32 %[cnt], %[cnt], 1\n\t" \
  "s_cmp_lg_u32 %[cnt], 2\n\t" \
  "s_cbranch_scc1 1b\n\t" \
  "s_waitcnt vmcnt(0) lgkmcnt(0)\n\t" \
  "v_smfmac_f32_32x32x32_bf16 %[ob], v[244:247], v[216:223], v248\n\t" \
  "s_mov_b32 m0, %[keep]\n\t" \
  "s_nop 11\n\t" \
  "s_branch 9f\n\t" \
  "8:\n\t" \
  "s_mov_b32 %[so], %[p0]\n\t" \
  "buffer_load_dwordx4 v[64:67], %[vo], %[rs], %[so] offen sc1\n\t" \
  "s_add_u32 %[so], %[so], 1024\n\t" \
  "buffer_load_dwordx4 v[68:71], %[vo], %[rs], %[so] offen sc1\n\t" \
  "s_add_u32 %[so], %[so], 1024\n\t" \
  "buffer_load_dwordx4 v[72:75], %[vo], %[rs], %[so] offen sc1\n\t" \
  "s_add_u32 %[so], %[so], 1024\n\t" \
  "buffer_load_dwordx4 v[76:79], %[vo], %[rs], %[so] offen sc1\n\t" \
  "s_add_u32 %[so], %[so], 1024\n\t" \
  "buffer_load_dwordx4 v[80:83], %[vo], %[rs], %[so] offen sc1\n\t" \
  "s_add_u32 %[so], %[so], 1024\n\t" \
  "buffer_load_dwordx4 v[84:87], %[vo], %[rs], %[so] offen sc1\n\t" \
  "s_add_u32 %[so], %[so], 1024\n\t" \
  "buffer_load_dwordx4 v[88:91], %[vo], %[rs], %[so] offen sc1\n\t" \
  "s_add_u32 %[so], %[so], 1024\n\t" \
  "buffer_load_dwordx4 v[92:95], %[vo], %[rs], %[so] offen sc1\n\t" \
  "s_add_u32 %[so], %[so], 1024\n\t" \
  "buffer_load_dwordx4 v[96:99], %[vo], %[rs], %[so] offen sc1\n\t" \
  "s_add_u32 %[so], %[so], 1024\n\t" \
  "buffer_load_dwordx4 v[100:103], %[vo], %[rs], %[so] offen sc1\n\t" \
  "s_add_u32 %[so], %[so], 1024\n\t" \
  "buffer_load_dwordx4 v[104:107], %[vo], %[rs], %[so] offen sc1\n\t" \
  "s_add_u32 %[so], %[so], 1024\n\t" \
  "buffer_load_dwordx4 v[108:111], %[vo], %[rs], %[so] offen sc1\n\t" \
  "s_add_u32 %[so], %[so], 1024\n\t" \
  "buffer_load_dwordx4 v[112:115], %[vo], %[rs], %[so] offen sc1\n\t" \
  "s_add_u32 %[so], %[so], 1024\n\t" \
  "buffer_load_dwordx4 v[116:119], %[vo], %[rs], %[so] offen sc1\n\t" \
  "s_add_u32 %[so], %[so], 1024\n\t" \
  "buffer_load_dwordx4 v[120:123], %[vo], %[rs], %[so] offen sc1\n\t" \
  "s_add_u32 %[so], %[so], 1024\n\t" \
  "buffer_load_dwordx4 v[124:127], %[vo], %[rs], %[so] offen sc1\n\t" \
  "s_add_u32 %[so], %[so], 1024\n\t" \
  "s_mov_b32 %[so], %[p1]\n\t" \
  "buffer_load_dwordx4 v[128:131], %[vo], %[rs], %[so] offen sc1\n\t" \
  "s_add_u32 %[so], %[so], 1024\n\t" \
  "buffer_load_dwordx4 v[132:135], %[vo], %[rs], %[so] offen sc1\n\t" \
  "s_add_u32 %[so], %[so], 1024\n\t" \
  "buffer_load_dwordx4 v[136:139], %[vo], %[rs], %[so] offen sc1\n\t" \
  "s_add_u32 %[so], %[so], 1024\n\t" \
  "buffer_load_dwordx4 v[140:143], %[vo], %[rs], %[so] offen sc1\n\t" \
  "s_add_u32 %[so], %[so], 1024\n\t" \
  "buffer_load_dwordx4 v[144:147], %[vo], %[rs], %[so] offen sc1\n\t" \
  "s_add_u32 %[so], %[so], 1024\n\t" \
  "buffer_load_dwordx4 v[148:151], %[vo], %[rs], %[so] offen sc1\n\t" \
  "s_add_u32 %[so], %[so], 1024\n\t" \
  "buffer_load_dwordx4 v[152:155], %[vo], %[rs], %[so] offen sc1\n\t" \
  "s_add_u32 %[so], %[so], 1024\n\t" \
  "buffer_load_dwordx4 v[156:159], %[vo], %[rs], %[so] offen sc1\n\t" \
  "s_add_u32 %[so], %[so], 1024\n\t" \
  "buffer_load_dwordx4 v[160:163], %[vo], %[rs], %[so] offen sc1\n\t" \
  "s_add_u32 %[so], %[so], 1024\n\t" \
  "s_mov_b32 %[so], %[p2]\n\t" \
  "buffer_load_dwordx4 v[164:167], %[vo], %[rs], %[so] offen sc1\n\t" \
  "s_add_u32 %[so], %[so], 1024\n\t" \
  "buffer_load_dwordx4 v[168:171], %[vo], %[rs], %[so] offen sc1\n\t" \
  "s_add_u32 %[so], %[so], 1024\n\t" \
  "buffer_load_dwordx4 v[172:175], %[vo], %[rs], %[so] offen sc1\n\t" \
  "s_add_u32 %[so], %[so], 1024\n\t" \
  "buffer_load_dwordx4 v[176:179], %[vo], %[rs], %[so] offen sc1\n\t" \
  "s_add_u32 %[so], %[so], 1024\n\t" \
  "buffer_load_dwordx4 v[180:183], %[vo], %[rs], %[so] offen sc1\n\t" \
  "s_add_u32 %[so], %[so], 1024\n\t" \
  "buffer_load_dwordx4 v[184:187], %[vo], %[rs], %[so] offen sc1\n\t" \
  "s_add_u32 %[so], %[so], 1024\n\t" \
  "buffer_load_dwordx4 v[188:191], %[vo], %[rs], %[so] offen sc1\n\t" \
  "s_add_u32 %[so], %[so], 1024\n\t" \
  "s_waitcnt vmcnt(0)\n\t" \
  "9:\n\t"
#define ATTN_FAST_CLOBBERS "v192", "v193", "v194", "v195", "v196", "v197", "v198", "v199", "v200", "v201", "v202", "v203", "v204", "v205", "v206", "v207", "v208", "v209", "v210", "v211", "v212", "v213", "v214", "v215", "v216", "v217", "v218", "v219", "v220", "v221", "v222", "v223", "v224", "v225", "v226", "v227", "v228", "v229", "v230", "v231", "v232", "v233", "v234", "v235", "v236", "v237", "v238", "v239", "v240", "v241", "v242", "v243", "v244", "v245", "v246", "v247", "v248", "v249", "v250", "v251", "vcc"

template <bool HELPER> __device__ __forceinline__ void attn_main(const Params &p, const Ctx &c0, int layer, f32x16 (&o)[2], bool &fast, v16u (&pre)[8] STAMP_ARGS) {
  const Ctx c = fresh(c0);
  const int head = 4 * c.bx + (c.bj >> 3), qb = c.bj & 7, l = lane_id(), h = l >> 5, tt = qb * 8 + 2 * (c.w & 3);
  STAMP(layer, PH_ATT, 0);
  if (!HELPER && c.w == 0) {
    const int hp = head >> 1, cq = hp / 3, ck = (16 + hp) / 3, cv = (32 + hp) / 3;
    poll(p, c, layer * 5 + PH_QKV, l < 16 ? ck * 16 + l : l < 32 ? cv * 16 + (l & 15) : l < 34 ? cq * 16 + 2 * qb + (l & 1) : -1);
  }
  BAR();
  STAMP(layer, PH_ATT, 1);
  v4u qa, qb2; float kn;
  const unsigned skv = OFF_KV + layer * 4 * MB + head * 131072 + c.w * 2048;
  {
    const unsigned sq = OFF_QF + layer * 2 * MB + (head * 64 + tt) * 1024, sk = OFF_KN + layer * 8192 + head * 256;
    asm volatile("buffer_load_dwordx4 %0, %3, %5, %6 offen sc1\n\tbuffer_load_dwordx4 %1, %3, %5, %6 offen offset:1024 sc1\n\t"
                 "buffer_load_dword %2, %4, %5, %7 offen sc1"
                 : "=&v"(qa), "=&v"(qb2), "=&v"(kn) : "v"(l * 16), "v"(l * 4), "s"(c.rws), "s"(sq), "s"(sk) : "memory");
    glds<true>(c.rws, l * 16, skv, c.lds0 + c.w * KVP);
    glds<true>(c.rws, l * 16, skv + 1024, c.lds0 + c.w * KVP + 1024);
    if (l < 16) { unsigned one = (l & 1) ? 0u : ONES2; OPAQUE_V(one); *(LAS v4u *)(c.lds + (8 * c.w + (l >> 1)) * KVP + 2048 + (l & 1) * 16) = v4u{one, one, one, one}; }
    asm volatile("s_waitcnt vmcnt(2)" : "+v"(qa), "+v"(qb2), "+v"(kn) :: "memory");
  }
  float km = max32(kn);
  km = __builtin_fmaxf(__builtin_bit_cast(float, __builtin_amdgcn_readlane(__builtin_bit_cast(int, km), 31)),
                       __builtin_bit_cast(float, __builtin_amdgcn_readlane(__builtin_bit_cast(int, km), 63)));
  const float qn = halfsum2(dot2(qa.w, qa.w, dot2(qa.z, qa.z, dot2(qa.y, qa.y, dot2(qa.x, qa.x, 0.f)))),
                            dot2(qb2.w, qb2.w, dot2(qb2.z, qb2.z, dot2(qb2.y, qb2.y, dot2(qb2.x, qb2.x, 0.f)))));
  const unsigned kb0 = c.lds0 + (HELPER ? KVP : 0);
  unsigned ka = kb0 + l * 16;
  unsigned va0 = kb0 + ((l & 16) ? ((l & 15) ? 2064 : 2048) : 1024 + h * 256 + (l & 15) * 16), va1 = va0 + ((l & 16) ? 0 : 512);
  fast = rfl(__all(qn * km <= 4096.f));
  asm volatile("" : "=v"(o[0]), "=v"(o[1]));
  unsigned p0, p1, p2;
  if (!HELPER) { p0 = OFF_WTO + (2 * (c.bj & 7) * 32 + (c.w >> 1) * 16) * 1024; p1 = p0 + 32768; p2 = p1 + 9 * 1024; }
  else { p0 = OFF_WT1 + up_nt0(c.bj & 15) * NTP + 25 * (c.w - 4) * 1024; p1 = p0 + 16384; p2 = OFF_WT2 + ((2 * (c.bj & 7) + (c.w & 1)) * 86 + ((c.w - 4) >> 1) * 43) * 1024; }
  asm volatile("s_waitcnt vmcnt(0) lgkmcnt(0)\n\ts_barrier" ::: "memory");
  STAMP(layer, PH_ATT, 4);
  if (HELPER && c.w == 4 && layer == 0)
    __builtin_amdgcn_raw_buffer_store_b64(v2u{(unsigned)c.tag, (unsigned)(c.tag >> 32)}, c.rws, (unsigned)l << 26,
                                          OFF_FLAGS + (SLOT_PREP * 256 + c.bj * 8 + c.bx) * FSTR, SC1);
  const int si = (c.w & 3) * 64 + l;
  if (!fast) {
#pragma unroll
    for (int i = 1; i < 8; ++i) {
      glds<true>(c.rws, l * 16, skv + i * 16384, c.lds0 + (8 * i + c.w) * KVP);
      glds<true>(c.rws, l * 16, skv + i * 16384 + 1024, c.lds0 + (8 * i + c.w) * KVP + 1024);
    }
    poll(p, c, SLOT_PREP, si);
    asm volatile("s_waitcnt vmcnt(0)\n\ts_barrier\n\ts_barrier\n\ts_barrier" ::: "memory");
    int idx = 0xdddd;
    OPAQUE_V(idx);
#pragma unroll
    for (int j = 0; j < 2; ++j) {
      const v4u q = j ? qb2 : qa;
      f32x16 acc = {};
      float m = -__builtin_inff();
#pragma unroll 1
      for (int t = 0; t < 32; ++t) {
        const v4u kf = *(const LAS v4u *)(uintptr_t)(ka + t * 2 * KVP);
        const v4u v0 = *(const LAS v4u *)(uintptr_t)(va0 + t * 2 * KVP), v1 = *(const LAS v4u *)(uintptr_t)(va1 + t * 2 * KVP);
        f32x16 s = mfma(kf, q, f32x16{});
        float tm = s[0];
#pragma unroll
        for (int r = 1; r < 16; ++r) tm = __builtin_fmaxf(tm, s[r]);
        auto sw = __builtin_amdgcn_permlane32_swap(__builtin_bit_cast(unsigned, tm), __builtin_bit_cast(unsigned, tm), false, false);
        const unsigned sw0 = sw[0], sw1 = sw[1];
        const float mn = __builtin_fmaxf(m, __builtin_fmaxf(__uint_as_float(sw0), __uint_as_float(sw1)));
        const float al = __builtin_amdgcn_exp2f(m - mn);
        m = mn;
        v8u e0, e1;
#pragma unroll
        for (int r = 0; r < 8; ++r) {
          e0[r] = __float_as_uint(__builtin_amdgcn_exp2f(s[r] - mn));
          e1[r] = __float_as_uint(__builtin_amdgcn_exp2f(s[8 + r] - mn));
        }
#pragma unroll
        for (int r = 0; r < 16; ++r) acc[r] *= al;
        acc = __builtin_amdgcn_smfmac_f32_32x32x32_bf16(__builtin_bit_cast(bf16x8, v0), __builtin_bit_cast(bf16x16, e0), acc, idx, 0, 0);
        acc = __builtin_amdgcn_smfmac_f32_32x32x32_bf16(__builtin_bit_cast(bf16x8, v1), __builtin_bit_cast(bf16x16, e1), acc, idx, 0, 0);
      }
      acc[9] = m;
      o[j] = acc;
    }
  }
  unsigned cnt = rfl(fast), keep, so = rfl(skv + 16384), m0v = rfl(c.lds0 + (8 + c.w) * KVP), wo = rfl(OFF_WT2 + (c.bj * 8 + c.w) * 1024);
  const rsrc_t rw = mkrsrc(p.ws, layer == 0 ? WS_BYTES : OFF_WTQ);
  asm volatile(ATTN_FAST_ASM
               : [oa] "+v"(o[0]), [ob] "+v"(o[1]), [cnt] "+s"(cnt), [keep] "=&s"(keep), [so] "+s"(so), [m0v] "+s"(m0v), [wo] "+s"(wo),
                 [ka] "+v"(ka), [va0] "+v"(va0), [va1] "+v"(va1),
                 "=&{v[64:79]}"(pre[0]), "=&{v[80:95]}"(pre[1]), "=&{v[96:111]}"(pre[2]), "=&{v[112:127]}"(pre[3]),
                 "=&{v[128:143]}"(pre[4]), "=&{v[144:159]}"(pre[5]), "=&{v[160:175]}"(pre[6]), "=&{v[176:191]}"(pre[7])
               : [qa] "v"(qa), [qb] "v"(qb2), [vo] "v"(l * 16), [rs] "s"(c.rws), [rw] "s"(rw), [dummy] "s"(rfl(c.lds0 + L_DUMMY)),
                 [p0] "s"(rfl(p0)), [p1] "s"(rfl(p1)), [p2] "s"(rfl(p2)), [fo] "v"(OFF_FLAGS + (SLOT_PREP * 256 + si) * FSTR), [tag] "s"(rfl64(c.tag))
               : "memory", "scc", ATTN_FAST_CLOBBERS);
  STAMP(layer, PH_ATT, 2);
}
__device__ __forceinline__ void attn_send(const Ctx &c0, const f32x16 (&o)[2]) {
  const Ctx c = fresh(c0);
  const int l = lane_id();
  LAS float *xp = (LAS float *)(c.lds + L_APART + c.w * 5120);
#pragma unroll
  for (int j = 0; j < 2; ++j) {
    *(LAS f32x4 *)(xp + j * 640 + l * 4) = f32x4{o[j][0], o[j][1], o[j][2], o[j][3]};
    *(LAS f32x4 *)(xp + j * 640 + 256 + l * 4) = f32x4{o[j][4], o[j][5], o[j][6], o[j][7]};
    *(LAS v2u *)(xp + j * 640 + 512 + l * 2) = v2u{__float_as_uint(o[j][8]), __float_as_uint(o[j][9])};
  }
}
__device__ __forceinline__ void attn_finish(const Ctx &c0, int layer, const f32x16 (&o)[2], bool fast STAMP_ARGS) {
  const Ctx c = fresh(c0);
  const int head = 4 * c.bx + (c.bj >> 3), qb = c.bj & 7, l = lane_id(), tt = qb * 8 + 2 * (c.w & 3);
  const LAS float *xp = (const LAS float *)(c.lds + L_APART + (c.w & 3) * 5120);
  BAR();
#pragma unroll
  for (int j = 0; j < 2; ++j) {
    const f32x4 a0 = *(const LAS f32x4 *)(xp + j * 640 + l * 4), a1 = *(const LAS f32x4 *)(xp + j * 640 + 256 + l * 4);
    const v2u a2 = *(const LAS v2u *)(xp + j * 640 + 512 + l * 2);
    float f0 = 1.f, f1 = 1.f;
    if (!fast) {
      const float m1 = __uint_as_float(a2.y), mn = __builtin_fmaxf(o[j][9], m1);
      f0 = __builtin_amdgcn_exp2f(o[j][9] - mn); f1 = __builtin_amdgcn_exp2f(m1 - mn);
    }
    const float lp = o[j][8] * f0 + __uint_as_float(a2.x) * f1;
    const float inv = __builtin_amdgcn_rcpf(halfsum2(lp, lp));
    f0 *= inv; f1 *= inv;
    st16(v4u{pk2(o[j][0] * f0 + a0[0] * f1, o[j][1] * f0 + a0[1] * f1), pk2(o[j][2] * f0 + a0[2] * f1, o[j][3] * f0 + a0[3] * f1),
             pk2(o[j][4] * f0 + a1[0] * f1, o[j][5] * f0 + a1[1] * f1), pk2(o[j][6] * f0 + a1[2] * f1, o[j][7] * f0 + a1[3] * f1)},
         c.rws, l * 16, OFF_OF + layer * 2 * MB + ((tt + j) * 32 + head) * 1024);
  }
  STAMP(layer, PH_ATT, 5);
}

__global__ __launch_bounds__(512, 2) void fused_kernel(Params p) {
  extern __shared__ __attribute__((aligned(16))) char smem[];
  Ctx c;
  c.rws = mkrsrc(p.ws, WS_BYTES);
  c.lds = (LAS char *)smem;
  c.lds0 = (unsigned)(uintptr_t)c.lds;
  c.w = __builtin_amdgcn_readfirstlane(threadIdx.x >> 6);
  c.bx = blockIdx.x & 7; c.bj = blockIdx.x >> 3;
  c.tag = 0;
  if (c.w == 0) *cnt_ptr(c, lane_id()) = 0;
  if (CODEWARM && c.w == 7) {
    u64 pc; int len;
    asm volatile("s_getpc_b64 %0\n.Lwarm_from:\n\ts_mov_b32 %1, .Lwarm_to-.Lwarm_from\n\ts_max_i32 %1, %1, 0" : "=s"(pc), "=s"(len) :: "scc");
    const rsrc_t rc = mkrsrc((const void *)(pc & ~(u64)15), (unsigned)len & ~15u);
    glds<false>(rc, lane_id() * 16, c.bj * 2048, c.lds0 + L_JUNK);
    glds<false>(rc, lane_id() * 16, c.bj * 2048 + 1024, c.lds0 + L_JUNK);
  }
  BAR();
  if (c.w >= 4) {
    helpers_start(p, c STAMP_PASS);
    c.tag = rfl64(c.tag);
#pragma unroll 1
    for (int layer = 0; layer < 2; ++layer) {
      f32x16 o[2];
      bool fast;
      v16u pre[8];
      attn_main<true>(p, c, layer, o, fast, pre STAMP_PASS);
      attn_finish(c, layer, o, fast STAMP_PASS);
      helpers_chain(p, c, layer, pre STAMP_PASS);
    }
  } else {
    ln_gemm<PH_QKV, true>(p, c, 0 STAMP_PASS);
    c.tag = rfl64(c.tag);
#pragma unroll 1
    for (int layer = 0; layer < 2; ++layer) {
      f32x16 o[2];
      bool fast;
      v16u pre[8];
      attn_main<false>(p, c, layer, o, fast, pre STAMP_PASS);
      attn_send(c, o);
      res_gemm<PH_WO>(p, c, layer, pre STAMP_PASS);
      ln_gemm<PH_UP, false>(p, c, layer STAMP_PASS);
      res_gemm<PH_DOWN>(p, c, layer, pre STAMP_PASS);
      if (layer == 0) ln_gemm<PH_QKV, false>(p, c, 1 STAMP_PASS);
    }
  }
  if (CODEWARM) asm volatile(".Lwarm_to:");
  if (blockIdx.x == 0 && c.w == 0 && lane_id() == 0) __hip_atomic_store((GAS u64 *)(p.ws + OFF_EPOCH), c.tag, __ATOMIC_RELAXED, __HIP_MEMORY_SCOPE_AGENT);
}

extern "C" void kernel_launch(void *const *d_in, const int *in_sizes, int n_in, void *d_out, int out_size, void *d_ws, size_t ws_size,
                              hipStream_t stream) {
  Params p;
  p.x = d_in[0]; p.wqkv = d_in[1]; p.wo = d_in[2]; p.w1 = d_in[4]; p.b1 = d_in[5]; p.w2 = d_in[6];
  p.out = d_out; p.ws = (char *)d_ws;
  (void)hipFuncSetAttribute((const void *)fused_kernel, hipFuncAttributeMaxDynamicSharedMemorySize, LDS_BYTES);
  fused_kernel<<<256, 512, LDS_BYTES, stream>>>(p);
}
```

```cpp
#include <hip/hip_runtime.h>
#include <hip/hip_bf16.h>
#include <stdint.h>
#include <type_traits>


#define LAS __attribute__((address_space(3)))
#define GAS __attribute__((address_space(1)))
typedef unsigned v4u __attribute__((ext_vector_type(4)));
typedef unsigned v2u __attribute__((ext_vector_type(2)));
typedef float f32x2 __attribute__((ext_vector_type(2)));
typedef float f32x4 __attribute__((ext_vector_type(4)));
typedef float f32x16 __attribute__((ext_vector_type(16)));
typedef __bf16 bf16x2 __attribute__((ext_vector_type(2)));
typedef __bf16 bf16x8 __attribute__((ext_vector_type(8)));
typedef __bf16 bf16x16 __attribute__((ext_vector_type(16)));
typedef short s16x4 __attribute__((ext_vector_type(4)));
typedef unsigned v8u __attribute__((ext_vector_type(8)));
typedef unsigned v16u __attribute__((ext_vector_type(16)));
typedef __amdgpu_buffer_rsrc_t rsrc_t;
typedef unsigned long long u64;

constexpr unsigned MB = 1u << 20;
constexpr unsigned OFF_EPOCH = 0;
constexpr unsigned OFF_QF = MB;
constexpr unsigned OFF_KV = OFF_QF + 4 * MB;
constexpr unsigned OFF_OF = OFF_KV + 8 * MB;
constexpr unsigned OFF_X1 = OFF_OF + 4 * MB;
constexpr unsigned OFF_X2 = OFF_X1 + 4 * MB;
constexpr unsigned OFF_HF = OFF_X2 + 2 * MB;
constexpr unsigned HF_LAYER = 6 * MB;
constexpr unsigned OFF_ST = OFF_HF + 2 * HF_LAYER;
constexpr unsigned OFF_ST0 = OFF_ST + 3 * 262144;
constexpr unsigned OFF_WT1 = OFF_ST + MB;
constexpr unsigned OFF_WTO = OFF_WT1 + 43 * 33 * 1024, OFF_WT2 = OFF_WTO + 16 * 32 * 1024, OFF_WTQ = OFF_WT2 + 16 * 86 * 1024;
constexpr unsigned OFF_FLAGS = OFF_WTQ + 48 * 33 * 1024;
constexpr unsigned WS_BYTES = OFF_FLAGS + 11 * 256 * 128;
constexpr unsigned LDS_BYTES = 163840;
constexpr unsigned NTP = 33 * 1024;
constexpr unsigned L_VSCR = 3 * NTP;
constexpr unsigned L_PREP = L_VSCR + 4 * 4608;
constexpr unsigned L_XCH = 25 * 4096;
constexpr unsigned L_APART = 136 * 1024;
constexpr unsigned L_DUMMY = 135168;
constexpr unsigned L_CNT = 159744;
constexpr unsigned L_JUNK = L_CNT + 1024;
constexpr unsigned KVP = 2080;
constexpr int SC1 = 16;
constexpr int FSTR = 128;
constexpr int D_ATT = 18, D_WO = 18, D_UP = 13, D_DN = 17, POLL_SLEEP = 3;
enum { PH_QKV = 0, PH_ATT = 1, PH_WO = 2, PH_UP = 3, PH_DOWN = 4, SLOT_PREP = 10 };

struct Params {
  const void *x, *wqkv, *wo, *w1, *b1, *w2;
  void *out;
  char *ws;
};

struct Ctx {
  rsrc_t rws;
  LAS char *lds;
  unsigned lds0;
  int w;
  int bx, bj;
  u64 tag;
};

#define SB() __builtin_amdgcn_sched_barrier(0)
#define OPAQUE_V(x) asm volatile("" : "+v"(x))
__device__ __forceinline__ Ctx fresh(const Ctx &c0) { Ctx c = c0; asm volatile("" : "+s"(c.w), "+s"(c.bx), "+s"(c.bj), "+s"(c.lds0), "+s"(c.tag)); return c; }
#define BAR() asm volatile("s_waitcnt lgkmcnt(0)\n\ts_barrier" ::: "memory")
#define VMWAIT(n) asm volatile("s_waitcnt vmcnt(" #n ")" ::: "memory")
__device__ __forceinline__ unsigned rfl(unsigned v) { return (unsigned)__builtin_amdgcn_readfirstlane((int)v); }
__device__ __forceinline__ u64 rfl64(u64 v) { return (u64)rfl((unsigned)v) | ((u64)rfl((unsigned)(v >> 32)) << 32); }
__device__ __forceinline__ rsrc_t mkrsrc(const void *p, unsigned bytes) { return __builtin_amdgcn_make_buffer_rsrc((void *)p, (short)0, (int)bytes, 0x00020000); }
__device__ __forceinline__ int lane_id() { int l; asm volatile("v_mbcnt_lo_u32_b32 %0, -1, 0\n\tv_mbcnt_hi_u32_b32 %0, -1, %0" : "=v"(l)); return l; }
__device__ __forceinline__ f32x16 mfma(v4u a, v4u b, f32x16 c) {
  return __builtin_amdgcn_mfma_f32_32x32x16_bf16(__builtin_bit_cast(bf16x8, a), __builtin_bit_cast(bf16x8, b), c, 0, 0, 0);
}
__device__ __forceinline__ unsigned pk2(float lo, float hi) { bf16x2 v = {(__bf16)lo, (__bf16)hi}; return __builtin_bit_cast(unsigned, v); }
__device__ __forceinline__ float dot2(unsigned a, unsigned b, float c) {
  return __builtin_amdgcn_fdot2_f32_bf16(__builtin_bit_cast(bf16x2, a), __builtin_bit_cast(bf16x2, b), c, false);
}
constexpr unsigned ONES2 = 0x3f803f80u;
__device__ __forceinline__ float bf_lo(unsigned u) { return __builtin_bit_cast(float, u << 16); }
__device__ __forceinline__ float bf_hi(unsigned u) { return __builtin_bit_cast(float, u & 0xffff0000u); }
__device__ __forceinline__ float halfsum2(float a, float b) {
  auto r = __builtin_amdgcn_permlane32_swap(__builtin_bit_cast(unsigned, a), __builtin_bit_cast(unsigned, b), false, false);
  const unsigned r0 = r[0], r1 = r[1];
  return __uint_as_float(r0) + __uint_as_float(r1);
}
template <int CTRL, int RMASK> __device__ __forceinline__ float dppmax(float v) {
  const int o = __builtin_amdgcn_update_dpp(0, __builtin_bit_cast(int, v), CTRL, RMASK, 0xf, true);
  return __builtin_fmaxf(v, __builtin_bit_cast(float, o));
}
template <int CTRL, int RMASK> __device__ __forceinline__ float dppadd(float v) {
  return v + __builtin_bit_cast(float, __builtin_amdgcn_update_dpp(0, __builtin_bit_cast(int, v), CTRL, RMASK, 0xf, true));
}
__device__ __forceinline__ float sum32(float v) {
  v = dppadd<0x111, 0xf>(v); v = dppadd<0x112, 0xf>(v); v = dppadd<0x114, 0xf>(v); v = dppadd<0x118, 0xf>(v);
  return dppadd<0x142, 0xa>(v);
}
__device__ __forceinline__ float max32(float v) {
  v = dppmax<0x111, 0xf>(v); v = dppmax<0x112, 0xf>(v); v = dppmax<0x114, 0xf>(v); v = dppmax<0x118, 0xf>(v);
  return dppmax<0x142, 0xa>(v);
}
template <bool DEV> __device__ __forceinline__ void glds(rsrc_t rs, unsigned voff, unsigned soff, unsigned ldsaddr) {
  unsigned keep;
  if constexpr (DEV)
    asm volatile("s_mov_b32 %0, m0\n\ts_mov_b32 m0, %1\n\ts_nop 2\n\tbuffer_load_dwordx4 %2, %3, %4 offen sc1 lds\n\ts_mov_b32 m0, %0"
                 : "=&s"(keep) : "s"(ldsaddr), "v"(voff), "s"(rs), "s"(soff) : "memory");
  else
    asm volatile("s_mov_b32 %0, m0\n\ts_mov_b32 m0, %1\n\ts_nop 2\n\tbuffer_load_dwordx4 %2, %3, %4 offen lds\n\ts_mov_b32 m0, %0"
                 : "=&s"(keep) : "s"(ldsaddr), "v"(voff), "s"(rs), "s"(soff) : "memory");
}
__device__ __forceinline__ v2u trd(const LAS char *p) {
  return __builtin_bit_cast(v2u, __builtin_amdgcn_ds_read_tr16_b64_v4i16((LAS s16x4 *)p));
}
__device__ __forceinline__ v4u trfrag(const LAS char *p0, const LAS char *p1) { const v2u a = trd(p0), b = trd(p1); return v4u{a.x, a.y, b.x, b.y}; }
__device__ __forceinline__ v4u ld16(rsrc_t rs, unsigned voff, unsigned soff) { return __builtin_amdgcn_raw_buffer_load_b128(rs, voff, soff, SC1); }
template <int AUX = SC1> __device__ __forceinline__ void st16(v4u v, rsrc_t rs, unsigned voff, unsigned soff) {
  __builtin_amdgcn_raw_buffer_store_b128(v, rs, voff, soff, AUX);
  asm volatile("s_nop 1" :: "v"(v) : "memory");
}


__device__ __forceinline__ LAS unsigned *cnt_ptr(const Ctx &c, int i) { return (LAS unsigned *)(c.lds + L_CNT) + i; }
template <bool NORMS = false> __device__ __forceinline__ void publish(const Ctx &c, int slot, int idx) {
  unsigned old, t1;
  asm volatile("v_mov_b32 %0, %2\n\tv_mov_b32 %1, 1\n\ts_waitcnt vmcnt(0)\n\ts_mov_b64 exec, 1\n\tds_add_rtn_u32 %0, %0, %1\n\t"
               "s_mov_b64 exec, -1\n\ts_waitcnt lgkmcnt(0)" : "=&v"(old), "=&v"(t1) : "s"(c.lds0 + L_CNT + 4 * slot) : "memory");
  if (rfl(old) == 3) {
    const int l = lane_id();
    v2u v = {(unsigned)c.tag, (unsigned)(c.tag >> 32)};
    if (NORMS) { const unsigned n = *cnt_ptr(c, 63 + (slot ? 8 : 0) + (l & 7)); if (l) v = v2u{n, (unsigned)c.tag}; }
    __builtin_amdgcn_raw_buffer_store_b64(v, c.rws, (unsigned)(l / (FSTR / 8)) << 26 | l * 8, OFF_FLAGS + (slot * 256 + idx) * FSTR, SC1);
  }
}
template <int DELAY> __device__ __forceinline__ void poll(const Ctx &c, int slot, int idx  ) {
  v2u v; unsigned n;
  asm volatile("v_cmp_le_i32 vcc, 0, %2\n\ts_mov_b32 %1, 0x100000\n\ts_sleep %6\n\ts_mov_b64 exec, vcc\n"
               "1:\n\tbuffer_load_dwordx2 %0, %2, %3, %4 offen sc1\n\ts_waitcnt vmcnt(0)\n\tv_cmp_ne_u64 vcc, %5, %0\n\ts_cbranch_vccz 2f\n\t"
               "s_mov_b64 exec, vcc\n\ts_sleep %7\n\ts_sub_u32 %1, %1, 1\n\ts_cbranch_scc0 1b\n2:\n\ts_mov_b64 exec, -1"
               : "=&v"(v), "=&s"(n) : "v"(idx * FSTR), "s"(c.rws), "s"(rfl(OFF_FLAGS + slot * 256 * FSTR)), "s"(c.tag), "n"(DELAY), "n"(POLL_SLEEP) : "memory", "vcc", "scc");
}
__device__ __forceinline__ void lds_arrive(const Ctx &c, int i) {
  unsigned t0, t1;
  asm volatile("v_mov_b32 %0, %2\n\tv_mov_b32 %1, 1\n\ts_mov_b64 exec, 1\n\tds_add_u32 %0, %1\n\ts_mov_b64 exec, -1"
               : "=&v"(t0), "=&v"(t1) : "s"(c.lds0 + L_CNT + 4 * i) : "memory");
}
template <int SLEEP = 1> __device__ __forceinline__ void lds_wait(const Ctx &c, int i, unsigned n) {
  unsigned t0, t1;
  asm volatile("v_mov_b32 %0, %2\n1:\n\tds_read_b32 %1, %0\n\ts_waitcnt lgkmcnt(0)\n\tv_cmp_gt_u32 vcc, %3, %1\n\ts_cbranch_vccz 2f\n\ts_sleep %4\n\ts_branch 1b\n2:"
               : "=&v"(t0), "=&v"(t1) : "s"(rfl(c.lds0 + L_CNT + 4 * i)), "s"(rfl(n)), "n"(SLEEP) : "memory", "vcc");
}

__device__ __forceinline__ void lds_peek(const Ctx &c, int i, unsigned &a, unsigned &v) {
  asm volatile("v_mov_b32 %0, %2\n\tds_read_b32 %1, %0" : "=&v"(a), "=&v"(v) : "s"(c.lds0 + L_CNT + 4 * i) : "memory");
}
__device__ __forceinline__ void lds_check(unsigned a, unsigned &v, unsigned n) {
  asm volatile("1:\n\tv_cmp_gt_u32 vcc, %2, %0\n\ts_cbranch_vccz 2f\n\ts_sleep 1\n\tds_read_b32 %0, %1\n\ts_waitcnt lgkmcnt(0)\n\ts_branch 1b\n2:" : "+v"(v) : "v"(a), "s"(n) : "memory", "vcc");
}
__device__ __forceinline__ int up_nt0(int cb) { return cb < 11 ? 3 * cb : 33 + 2 * (cb - 11); }
__device__ __forceinline__ v2u aug_quad(float cs, unsigned bias_bits) {
  const unsigned hi = pk2(cs, 0.f) & 0xffffu;
  return v2u{pk2(cs, cs - bf_lo(hi)), hi | (bias_bits << 16)};
}
template <int I> __device__ __forceinline__ v4u frag(const v16u (&pre)[8]) {
  return __builtin_shufflevector(pre[I >> 2], pre[I >> 2], 4 * (I & 3), 4 * (I & 3) + 1, 4 * (I & 3) + 2, 4 * (I & 3) + 3);
}
template <int N, class F> __device__ __forceinline__ void static_for(F f) {
  if constexpr (N > 0) { static_for<N - 1>(f); f(std::integral_constant<int, N - 1>{}); }
}
template <bool FIRST> __device__ __forceinline__ void ln_tile(const Ctx &c, int &rb, int &cb) {
  if (FIRST) { rb = 8 * (c.bx >> 2) + (c.bj >> 2); cb = 4 * (c.bx & 3) + (c.bj & 3); }
  else { rb = 2 * c.bx + (c.bj >> 4); cb = c.bj & 15; }
}

__device__ __forceinline__ void helpers_start(const Params &p, Ctx &c) {
  const int l = lane_id(), hj = c.w - 4, id = blockIdx.x, h = l >> 5;
  int rb, cb;
  ln_tile<true>(c, rb, cb);
  v4u r0, r1; v2u ep;
  asm volatile("s_nop 4\n\tbuffer_load_dwordx4 %0, %3, %4, %5 offen\n\tbuffer_load_dwordx4 %1, %3, %4, %5 offen offset:1024\n\t"
               "buffer_load_dwordx2 %2, off, %6, 0 sc1"
               : "=&v"(r0), "=&v"(r1), "=&v"(ep) : "v"(l * 16), "s"(mkrsrc(p.x, 2048 * 1024)), "s"((id * 8 + 2 * hj) * 1024), "s"(c.rws) : "memory");
  const rsrc_t rq = mkrsrc(p.wqkv, 512 * 3072);
  unsigned voff[3];
#pragma unroll
  for (int i = 0; i < 3; ++i) { const int u = 64 * i + l, row = (u * 43691) >> 19; voff[i] = row * 3072 + (u - 12 * row) * 16; }
#define STG(i) _Pragma("unroll") for (int t = 0; t < 3; ++t) glds<false>(rq, voff[t], (4 * (i) + hj) * 16 * 3072 + cb * 192, c.lds0 + (4 * (i) + hj) * 3072 + t * 1024)
  STG(0); STG(1); STG(2); STG(3);
  asm volatile("s_waitcnt vmcnt(12)" : "+v"(r0), "+v"(r1), "+v"(ep) :: "memory");
  c.tag = rfl64(((u64)ep.y << 32 | ep.x) + 1);
  const float t1 = sum32(halfsum2(dot2(r0.w, ONES2, dot2(r0.z, ONES2, dot2(r0.y, ONES2, dot2(r0.x, ONES2, 0.f)))),
                                  dot2(r1.w, ONES2, dot2(r1.z, ONES2, dot2(r1.y, ONES2, dot2(r1.x, ONES2, 0.f))))));
  const float t2 = sum32(halfsum2(dot2(r0.w, r0.w, dot2(r0.z, r0.z, dot2(r0.y, r0.y, dot2(r0.x, r0.x, 0.f)))),
                                  dot2(r1.w, r1.w, dot2(r1.z, r1.z, dot2(r1.y, r1.y, dot2(r1.x, r1.x, 0.f))))));
  st16(v4u{__float_as_uint(t1), (unsigned)c.tag, __float_as_uint(t2), (unsigned)c.tag + (unsigned)(c.tag >> 32)}, c.rws, (unsigned)(~l & 31) << 26 | h * 16,
       OFF_ST0 + (id * 8 + 2 * hj) * 16);
  const unsigned g1 = (unsigned)(47 - id) >> 31, g2 = (unsigned)(91 - id) >> 31, g3 = (unsigned)(107 - id) >> 31, g4 = (unsigned)(155 - id) >> 31;
  auto pick = [&](u64 a, u64 b, u64 cc, u64 d) { return a + g1 * (b - a) + g2 * (cc - b) + g3 * (d - cc); };
  const void *sw = (const void *)rfl64(pick((u64)p.wqkv, (u64)p.w1, (u64)p.wo, (u64)p.w2));
  const unsigned pitch = rfl(pick(3072, 2752, 1024, 1024)), rows = rfl(512 + g3 * 864 - g4 * 1376), rel = id - (unsigned)pick(0, 48, 92, 108);
  const unsigned np = rfl(g3 ? rel & 7 : rel >> 1), kb0 = rfl(g3 ? (rel >> 3) * 16 : (rel & 1) * 16);
  const unsigned dst = rfl(pick(OFF_WTQ, OFF_WT1, OFF_WTO, OFF_WT2)), tp = rfl(pick(NTP, NTP, 32768, 86 * 1024));
  const v4u ones = {ONES2, ONES2, ONES2, ONES2};
  const LAS char *blk = c.lds + hj * 3072 + (8 * h + ((l & 15) >> 2)) * 192 + ((l >> 4) & 1) * 32 + (l & 3) * 8;
  f32x16 cs[3] = {};
#define CSUM(i) _Pragma("unroll") for (int u = 0; u < 6; ++u) { const LAS char *q = blk + (2 * (i) + u / 3) * 12288 + (u % 3) * 64; cs[u % 3] = mfma(ones, trfrag(q, q + 768), cs[u % 3]); }
  VMWAIT(7); lds_arrive(c, 52);
  STG(4); STG(5);
  CSUM(0);
  VMWAIT(6); lds_arrive(c, 53);
  STG(6); STG(7);
  CSUM(1);
  VMWAIT(6); lds_arrive(c, 54);
  {
    const rsrc_t rw = mkrsrc(sw, rows * pitch);
    const unsigned vo = (l >> 3) * pitch + ((l & 7) ^ ((l >> 2) & 4)) * 16;
#pragma unroll
    for (int i = 0; i < 8; ++i) glds<false>(rw, vo, (kb0 * 16 + 64 * hj + 8 * i) * pitch + np * 128, c.lds0 + L_PREP + (8 * hj + i) * 1024);
  }
  __builtin_amdgcn_s_setprio(2);
  VMWAIT(8); lds_arrive(c, 55);
  CSUM(2);
  CSUM(3);
#undef CSUM
#undef STG
#pragma unroll
  for (int t = 0; t < 3; ++t)
    *(LAS v2u *)(c.lds + 32 * 3072 + t * 1024 + ((l & 31) + 32 * (hj >> 1)) * 16 + (hj & 1) * 8) = aug_quad(cs[t][0], 0);
  lds_arrive(c, 56);
  __builtin_amdgcn_s_setprio(0);
  if (id < 156) {
    const unsigned nt = 2 * np + (hj & 1), kb = kb0 + 8 * (hj >> 1), q = (kb >> 3) & 3;
    unsigned bias = 0;
    if (id >= 48 && id < 92 && q < 2 && nt < 43) bias = ((const GAS unsigned short *)p.b1)[nt * 32 + (l & 31)];
    VMWAIT(0); lds_arrive(c, 58);
    lds_wait(c, 58, 4);
    const LAS char *sp = c.lds + L_PREP + (hj >> 1) * 16384 + (4 * h + ((l & 15) >> 2)) * 128 + (((hj & 1) * 64) ^ ((l & 8) << 3)) + ((l >> 4) & 1) * 32 + (l & 3) * 8;
    f32x16 ss = {};
    v4u f[8];
#pragma unroll
    for (int i = 0; i < 8; ++i) { f[i] = trfrag(sp + i * 2048, sp + i * 2048 + 1024); ss = mfma(ones, f[i], ss); }
    v2u aq = aug_quad(ss[0], bias), aq2 = {};
    {
      LAS v2u *ax = (LAS v2u *)(c.lds + L_JUNK + (hj & 1) * 512) + l;
      if (hj >> 1) { *ax = aq; lds_arrive(c, 60 + (hj & 1)); } else { lds_wait(c, 60 + hj, 1); aq2 = *ax; }
    }
    OPAQUE_V(aq);
    const unsigned d0 = dst + nt * tp, off = id >= 48 && id < 92 && nt == 43 ? 1u << 31 : 0;
    if (id >= 48 && id < 108) lds_wait(c, PH_QKV, 4);
#pragma unroll
    for (int i = 0; i < 8; ++i) st16(f[i], c.rws, (kb + i < 86 ? off : 1u << 31) | l * 16, d0 + (kb + i) * 1024);
    if (id < 92 && hj < 2) st16(v4u{aq.x, aq.y, aq2.x, aq2.y}, c.rws, off | (unsigned)h << 30 | (l + 32 * (q >> 1)) * 16, d0 + 32768);
  }
}

__device__ __forceinline__ void store_stats(const Ctx &c, const unsigned (&o)[8], int l, unsigned st_tt, int g) {
  float s1 = 0.f, s2 = 0.f, s1b = 0.f, s2b = 0.f;
#pragma unroll
  for (int i = 0; i < 8; i += 2) { s1 = dot2(o[i], ONES2, s1); s2 = dot2(o[i], o[i], s2); s1b = dot2(o[i + 1], ONES2, s1b); s2b = dot2(o[i + 1], o[i + 1], s2b); }
  const int sv = (int)__float_as_uint(halfsum2(s1 + s1b, s2 + s2b));
  __builtin_amdgcn_raw_buffer_store_b32(sv, c.rws, l * 4, st_tt + g * 256, SC1);
}

template <int PH, bool FIRST> __device__ __forceinline__ void ln_gemm(const Params &p, Ctx &c0, int layer) {
  Ctx c = fresh(c0);
  int rb, cb;
  ln_tile<FIRST>(c, rb, cb);
  const int nt0 = PH == PH_QKV ? 3 * cb : up_nt0(cb);
  const int l = lane_id(), h = l >> 5, tt = rb * 4 + c.w;
  constexpr int PD = 10;
  v4u a[32];
  float st[16], rstd = 0.f, sp = 0.f, nm = 0.f;
  const unsigned base = (PH == PH_QKV ? OFF_X2 : OFF_X1 + layer * 2 * MB) + tt * 32768;
#define LDA(ks) a[ks] = ld16(c.rws, l * 16 + ((ks) & 3) * 1024, base + ((ks) >> 2) * 4096)
  [[maybe_unused]] LAS char *sb = c.lds + L_VSCR + c.w * 4608;
  [[maybe_unused]] const unsigned w0 = (l >> 3) * 128 + ((l & 7) ^ (l >> 4)) * 16, r0 = (l & 31) * 128 + (h ^ ((l >> 1) & 7)) * 16;
  [[maybe_unused]] u64 e = 0;
  [[maybe_unused]] v4u g = {};
  [[maybe_unused]] unsigned pka = 0, pkv = 0;
  [[maybe_unused]] const rsrc_t rx = mkrsrc(p.x, 2048 * 1024);
#define LDX(ks) a[ks] = __builtin_amdgcn_raw_buffer_load_b128(rx, (l >> 3) * 1024 + (l & 7) * 16 + ((ks) >> 2) * 128, tt * 32768 + ((ks) & 3) * 8192, 0)
#define XW(ch, i) *(LAS v4u *)(sb + (i) * 1024 + (w0 ^ (((i) & 1) * 64))) = a[4 * (ch) + (i)]
#define XR(ch, i) a[4 * (ch) + (i)] = *(const LAS v4u *)(sb + (r0 ^ ((i) * 32)))
  if constexpr (FIRST) {
#pragma unroll
    for (int ks = 0; ks < 32; ++ks) LDX(ks);
    e = __hip_atomic_load((GAS u64 *)(p.ws + OFF_EPOCH), __ATOMIC_RELAXED, __HIP_MEMORY_SCOPE_AGENT) + 1;
    SB();
#pragma unroll
    for (int ch = 0; ch < 8; ++ch) {
#pragma unroll
      for (int i = 0; i < 4; ++i) XW(ch, i);
      asm volatile("" ::: "memory");
#pragma unroll
      for (int i = 0; i < 4; ++i) XR(ch, i);
      asm volatile("" ::: "memory");
    }
    lds_wait(c, 52, 4);
  } else {
    if (c.w == 0) poll<D_UP>(c, PH == PH_QKV ? PH_DOWN : layer * 5 + PH_WO, l < 16 ? rb * 16 + l : -1);
    BAR();
#pragma unroll
    for (int ks = 0; ks < PD; ++ks) LDA(ks);
  }
  SB();
  const LAS char *b0 = c.lds + (FIRST ? (8 * h + ((l & 15) >> 2)) * 192 + ((l >> 4) & 1) * 32 + (l & 3) * 8 : l * 16);
  const LAS char *b2 = b0 + (FIRST ? 20 * 3072 : 2 * NTP - 4096);
  OPAQUE_V(b2);
  f32x16 acc[3] = {};
  v4u wf[3][3], aug = {};
  auto WF = [&](int ks, int nt) -> v4u {
    if (FIRST) {
      if (ks == 32) return *(const LAS v4u *)(c.lds + 32 * 3072 + nt * 1024 + l * 16);
      const LAS char *q = (ks >= 20 ? b2 + (ks - 20) * 3072 : b0 + ks * 3072) + nt * 64;
      return trfrag(q, q + 768);
    }
    return *(const LAS v4u *)(nt == 2 ? b2 + (ks + 4) * 1024 : nt == 1 && ks > 30 ? b2 + (ks - 29) * 1024 : b0 + nt * NTP + ks * 1024);
  };
#pragma unroll
  for (int ks = 0; ks < 2; ++ks)
#pragma unroll
    for (int nt = 0; nt < 3; ++nt) wf[ks][nt] = WF(ks, nt);
  SB();
#pragma unroll
  for (int ks = 0; ks < 33; ++ks) {
#pragma unroll
    for (int nt = 0; nt < 3; ++nt) {
      if (FIRST && nt == 0 && (ks & 7) == 4) lds_peek(c, 53 + (ks >> 3), pka, pkv);
      if (FIRST && nt == 1 && (ks & 7) == 6) { lds_check(pka, pkv, 4); SB(); }
      if (FIRST && nt == 1 && (ks & 7) == 6) wf[(ks + 2) % 3][0] = WF(ks + 2, 0);
      if (ks + 2 < 33 && !(FIRST && nt == 0 && (ks & 7) == 6)) wf[(ks + 2) % 3][nt] = WF(ks + 2, nt);
      acc[nt] = mfma(wf[ks % 3][nt], ks < 32 ? a[ks] : aug, acc[nt]);
      if constexpr (FIRST) {
        if (ks == 12 && nt == 0) g = ld16(c.rws, (l & 31) * 16, OFF_ST0 + tt * 512);
        if (ks == 27 && nt == 0) {
          c0.tag = c.tag = rfl64(e);
          if (__builtin_expect(!__all(g.y == (unsigned)c.tag && g.w == (unsigned)c.tag + (unsigned)(c.tag >> 32)), 0)) {
            OPAQUE_V(acc[0]); OPAQUE_V(acc[1]); OPAQUE_V(acc[2]);
#pragma unroll 1
            for (unsigned spins = 0; spins < (1u << 20); ++spins) {
              asm volatile("" ::: "memory");
              g = ld16(c.rws, (l & 31) * 16, OFF_ST0 + tt * 512);
              if (__all(g.y == (unsigned)c.tag && g.w == (unsigned)c.tag + (unsigned)(c.tag >> 32))) break;
            }
          }
          sp = __uint_as_float(h ? g.z : g.x);
        }
      } else {
        if (nt == 1 && ks + PD < 32) LDA(ks + PD);
        if (PH == PH_UP && nt == 2 && ks + PD == 31) lds_arrive(c, 40 + layer * 5 + PH);
        if (nt == 2 && ks >= 8 && ks < 16) {
#pragma unroll
          for (int g = 2 * (ks - 8); g < 2 * (ks - 7); ++g)
            st[g] = __uint_as_float(__builtin_amdgcn_raw_buffer_load_b32(c.rws, l * 4 + g * 256, OFF_ST + (PH == PH_UP ? layer : 2) * 262144 + tt * 4096, SC1));
        }
        if (ks == 27) { if (nt == 0) sp = (st[0] + st[1]) + (st[2] + st[3]); else sp += (st[4 * nt] + st[4 * nt + 1]) + (st[4 * nt + 2] + st[4 * nt + 3]); OPAQUE_V(sp); }
        if (ks == 28 && nt == 0) { sp += (st[12] + st[13]) + (st[14] + st[15]); OPAQUE_V(sp); }
      }
      if (ks == 28 && nt == 1) {
        auto sw = __builtin_amdgcn_permlane32_swap(__float_as_uint(sp), __float_as_uint(sp), false, false);
        const unsigned u1 = sw[0], u2 = sw[1];
        nm = __uint_as_float(u1) * (-1.f / 512.f); sp = __uint_as_float(u2) * (1.f / 512.f);
        OPAQUE_V(nm); OPAQUE_V(sp);
      }
      if (ks == 28 && nt == 2) { sp = __builtin_fmaxf(sp - nm * nm, 0.f) + 1e-5f; rstd = __builtin_amdgcn_rsqf(sp); OPAQUE_V(sp); OPAQUE_V(rstd); }
      if (ks == 29 && nt == 0) { sp *= rstd; aug.x = pk2(nm, nm); aug.y = pk2(0.f, sp); OPAQUE_V(sp); OPAQUE_V(aug); }
      if (ks == 29 && nt == 1) { aug.w = pk2(nm - bf_lo(aug.x), sp - bf_hi(aug.y)); aug.y |= aug.w & 0xffffu; aug.z = aug.x; OPAQUE_V(aug); }
      SB();
    }
  }
#undef LDA
#undef XW
#undef XR
#undef LDX
  if (PH == PH_UP) lds_arrive(c, 16 + layer * 5 + PH);
  if constexpr (PH == PH_QKV) {
    auto scaled = [&](int nt, float rs, v4u &wa, v4u &wb) {
      unsigned o[8];
#pragma unroll
      for (int i = 0; i < 8; ++i) { const f32x2 v = f32x2{acc[nt][2 * i], acc[nt][2 * i + 1]} * rs; o[i] = pk2(v.x, v.y); }
      wa = v4u{o[0], o[1], o[2], o[3]}; wb = v4u{o[4], o[5], o[6], o[7]};
    };
    auto qk = [&](int nt) {
      const int NT = nt0 + nt, head = 2 * (NT & 15);
      const bool isq = NT < 16;
      v4u wa, wb;
      scaled(nt, isq ? rstd * (0.25f * 1.4426950408889634f) : rstd, wa, wb);
      const unsigned so = isq ? OFF_QF + layer * 2 * MB + (head * 64 + tt) * 1024 : OFF_KV + layer * 4 * MB + (head * 64 + tt) * 2048;
      st16(wa, c.rws, l * 16, so); st16(wb, c.rws, l * 16, so + (isq ? 65536 : 131072));
      const float na = dot2(wa.w, wa.w, dot2(wa.z, wa.z, dot2(wa.y, wa.y, dot2(wa.x, wa.x, 0.f))));
      const float nb = dot2(wb.w, wb.w, dot2(wb.z, wb.z, dot2(wb.y, wb.y, dot2(wb.x, wb.x, 0.f))));
      const unsigned km = __float_as_uint(max32(halfsum2(na, nb)));
      if ((l & 31) == 31) __hip_atomic_fetch_max(cnt_ptr(c, 64 + layer * 8 + 2 * nt + h), km, __ATOMIC_RELAXED, __HIP_MEMORY_SCOPE_WORKGROUP);
    };
    LAS char *vs = c.lds + L_VSCR + c.w * 4608;
    auto vput = [&](int nt) {
      v4u wa, wb;
      scaled(nt, rstd, wa, wb);
      *(LAS v4u *)(vs + (nt & 1) * 2304 + (l & 31) * 32 + h * 16) = wa;
      *(LAS v4u *)(vs + (nt & 1) * 2304 + 1152 + (l & 31) * 32 + h * 16) = wb;
      asm volatile("" ::: "memory");
    };
    auto vget = [&](int nt) {
      const LAS char *rp = vs + (nt & 1) * 2304 + ((l >> 4) & 1) * 1152 + (8 * h + ((l & 15) >> 2)) * 32 + (l & 3) * 8;
      const v4u f0 = trfrag(rp, rp + 128), f1 = trfrag(rp + 512, rp + 640);
      const int i = l & 15, d = (i & 3) | ((i & 4) << 1) | ((i & 8) >> 1);
      const unsigned vo = ((l >> 4) & 1) * 131072 + h * 256 + d * 16, so = OFF_KV + layer * 4 * MB + (2 * ((nt0 + nt) & 15) * 64 + tt) * 2048 + 1024;
      st16(f0, c.rws, vo, so); st16(f1, c.rws, vo, so + 512);
    };
    if (cb < 10) { qk(0); qk(1); qk(2); }
    else if (cb == 10) { vput(2); qk(0); vget(2); qk(1); }
    else { vput(0); vput(1); vget(0); vput(2); vget(1); vget(2); }
  } else {
#pragma unroll
    for (int nt = 0; nt < 3; ++nt) {
      const int NT = nt0 + nt;
      unsigned o[8];
      const float rl = rstd * 1.4426950408889634f;
      const f32x2 one = {1.f, 1.f}, rs2 = {rstd, rstd}, rm2 = {-2.f * rstd, -2.f * rstd};
#pragma unroll
      for (int i = 0; i < 8; ++i) {
        const f32x2 x = {acc[nt][2 * i], acc[nt][2 * i + 1]}, hl = x * rl;
        const f32x2 t = f32x2{__builtin_amdgcn_exp2f(hl.x), __builtin_amdgcn_exp2f(hl.y)} + one;
        const f32x2 d = __builtin_elementwise_fma(t, t, one);
        const f32x2 v = x * __builtin_elementwise_fma(f32x2{__builtin_amdgcn_rcpf(d.x), __builtin_amdgcn_rcpf(d.y)}, rm2, rs2);
        o[i] = pk2(v.x, v.y);
      }
      if (nt < 2 || cb < 11) {
        const unsigned so = OFF_HF + layer * HF_LAYER + (tt * 86 + 2 * NT) * 1024;
        st16(v4u{o[0], o[1], o[2], o[3]}, c.rws, l * 16, so); st16(v4u{o[4], o[5], o[6], o[7]}, c.rws, l * 16, so + 1024);
      }
    }
  }
  publish<PH == PH_QKV>(c, layer * 5 + PH, PH == PH_QKV ? cb * 16 + rb : rb * 16 + cb);
}

template <int PH> __device__ __forceinline__ void res_gemm(const Params &p, const Ctx &c0, int layer, const v16u (&pre)[8]) {
  const Ctx c = fresh(c0);
  constexpr int KB = PH == PH_WO ? 32 : 86, KH = KB / 2;
  const int rb = 4 * c.bx + (c.bj >> 3), cb = c.bj & 7;
  const int l = lane_id(), h = l >> 5, kh = c.w >> 1, tt = rb * 2 + (c.w & 1);
  const unsigned fm = (tt * 32 + cb * 4 + kh * 2) * 1024;
  v4u res[2];
  auto load_res = [&]() {
    if (PH == PH_WO && layer == 0) {
      const rsrc_t rx = mkrsrc((const void *)rfl64((u64)p.x), 2048 * 1024);
      const unsigned voff = (l & 31) * 1024 + h * 8, so = tt * 32768 + cb * 128 + kh * 64;
      const v2u r0 = __builtin_amdgcn_raw_buffer_load_b64(rx, voff, so, 0), r1 = __builtin_amdgcn_raw_buffer_load_b64(rx, voff + 16, so, 0);
      const v2u r2 = __builtin_amdgcn_raw_buffer_load_b64(rx, voff + 32, so, 0), r3 = __builtin_amdgcn_raw_buffer_load_b64(rx, voff + 48, so, 0);
      res[0] = v4u{r0.x, r0.y, r1.x, r1.y}; res[1] = v4u{r2.x, r2.y, r3.x, r3.y};
    } else {
      const unsigned so = (PH == PH_WO ? OFF_X2 : OFF_X1 + layer * 2 * MB) + fm;
      res[0] = ld16(c.rws, l * 16, so); res[1] = ld16(c.rws, l * 16 + 1024, so);
    }
  };
  load_res();
  if (PH == PH_WO) BAR();
  if (c.w == 0) { if (PH == PH_WO) { lds_wait(c, layer * 5 + PH_ATT, 4); poll<D_WO>(c, layer * 5 + PH_ATT, l < 32 ? (rb >> 2) * 32 + l : -1); } else poll<D_DN>(c, layer * 5 + PH_UP, l < 16 ? (rb >> 1) * 16 + l : -1); }
  if constexpr (PH == PH_WO) { if (c.w == 0) lds_arrive(c, 57); else lds_wait(c, 57, layer + 1); } else BAR();
  v4u a[KH];
  const unsigned base = (PH == PH_WO ? OFF_OF + layer * 2 * MB : OFF_HF + layer * HF_LAYER) + (tt * KB + kh * KH) * 1024;
#define LDA(ks) a[ks] = ld16(c.rws, l * 16 + ((ks) & 3) * 1024, base + ((ks) >> 2) * 4096)
  constexpr int PD = PH == PH_WO ? 16 : 13;
#pragma unroll
  for (int ks = 0; ks < PD; ++ks) LDA(ks);
  if (PH == PH_WO) lds_arrive(c, 40 + layer * 5 + PH);
  SB();
  f32x16 acc[2] = {};
  if constexpr (PH == PH_WO) {
    static_for<16>([&](auto I) {
      constexpr int ks = I.value;
      acc[0] = mfma(frag<ks>(pre), a[ks], acc[0]);
      if constexpr (PD + 2 * ks < KH) LDA(PD + 2 * ks);
      SB();
      acc[1] = mfma(frag<16 + ks>(pre), a[ks], acc[1]);
      if constexpr (PD + 2 * ks + 1 < KH) LDA(PD + 2 * ks + 1);
      SB();
    });
  } else {
    const LAS char *b0 = c.lds + kh * 2048 + l * 16, *b1 = b0 + 65536, *b2 = b0 + 131072;
    OPAQUE_V(b1); OPAQUE_V(b2);
    v4u wf[4][2];
#define SLOT(ks) ((ks) < 39 ? (ks) : (ks) - 39)
#define WF(ks, nt) (*(const LAS v4u *)((SLOT(ks) >= 32 ? b2 : SLOT(ks) >= 16 ? b1 : b0) + (SLOT(ks) & 15) * 4096 + (nt) * 1024))
#pragma unroll
    for (int ks = 0; ks < 3; ++ks)
#pragma unroll
      for (int nt = 0; nt < 2; ++nt) wf[ks][nt] = WF(ks, nt);
    SB();
#pragma unroll
    for (int ks = 0; ks < KH; ++ks) {
      if (ks == 12 || ks == 36) { asm volatile("s_barrier" ::: "memory"); SB(); }
#pragma unroll
      for (int nt = 0; nt < 2; ++nt) {
        if (ks + 3 < KH) wf[(ks + 3) % 4][nt] = WF(ks + 3, nt);
        acc[nt] = mfma(wf[ks % 4][nt], a[ks], acc[nt]);
        if (nt == 1 && PD + ks < KH) LDA(PD + ks);
        if (nt == 1 && PD + ks == KH - 1) lds_arrive(c, 40 + layer * 5 + PH);
        SB();
      }
    }
#undef WF
#undef SLOT
    lds_arrive(c, 16 + layer * 5 + PH);
  }
#undef LDA
  constexpr unsigned XCH = L_XCH;
  const unsigned tok = layer * 2 + (PH == PH_WO ? 1 : 2);
  {
    const f32x16 s = kh ? acc[0] : acc[1];
    LAS f32x4 *xw = (LAS f32x4 *)(c.lds + XCH + c.w * 4096 + l * 16);
#pragma unroll
    for (int g = 0; g < 4; ++g) xw[g * 64] = f32x4{s[4 * g], s[4 * g + 1], s[4 * g + 2], s[4 * g + 3]};
  }
  lds_arrive(c, 32 + c.w);
  const f32x16 m = kh ? acc[1] : acc[0];
  f32x2 own[8];
#pragma unroll
  for (int i = 0; i < 8; ++i) { const unsigned r = res[i >> 2][i & 3]; own[i] = f32x2{m[2 * i], m[2 * i + 1]} + f32x2{bf_lo(r), bf_hi(r)}; OPAQUE_V(own[i]); }
  lds_wait<0>(c, 32 + (c.w ^ 2), tok);
  const LAS f32x4 *xr = (const LAS f32x4 *)(c.lds + XCH + (c.w ^ 2) * 4096 + l * 16);
  unsigned o[8];
#pragma unroll
  for (int g = 0; g < 4; ++g) {
    const f32x4 q = xr[g * 64];
    const f32x2 u = own[2 * g] + f32x2{q[0], q[1]}, v = own[2 * g + 1] + f32x2{q[2], q[3]};
    o[2 * g] = pk2(u.x, u.y); o[2 * g + 1] = pk2(v.x, v.y);
  }
  if (PH == PH_DOWN && layer == 1) {
    const rsrc_t ro = mkrsrc((const void *)rfl64((u64)p.out), 2048 * 1024);
#pragma unroll
    for (int t = 0; t < 2; ++t) {
      auto s0 = __builtin_amdgcn_permlane32_swap(o[4 * t], o[4 * t + 2], false, false);
      auto s1 = __builtin_amdgcn_permlane32_swap(o[4 * t + 1], o[4 * t + 3], false, false);
      st16<0>(v4u{s0[0], s1[0], s0[1], s1[1]}, ro, (l & 31) * 1024 + h * 16 + t * 32, tt * 32768 + cb * 128 + kh * 64);
    }
  } else {
    const unsigned so = (PH == PH_WO ? OFF_X1 + layer * 2 * MB : OFF_X2) + fm;
    st16(v4u{o[0], o[1], o[2], o[3]}, c.rws, l * 16, so); st16(v4u{o[4], o[5], o[6], o[7]}, c.rws, l * 16 + 1024, so);
    store_stats(c, o, l, OFF_ST + (PH == PH_WO ? layer : 2) * 262144 + tt * 4096, 2 * cb + kh);
    publish(c, layer * 5 + PH, rb * 8 + cb);
  }
}

__device__ __forceinline__ void helpers_chain(const Params &p, const Ctx &c0, int layer, const v16u (&pre)[8]) {
  const Ctx c = fresh(c0);
  const int l = lane_id(), hj = c.w - 4;
  LAS char *lw = c.lds + l * 16;
  publish(c, layer * 5 + PH_ATT, (c.bj & 7) * 32 + 4 * c.bx + (c.bj >> 3));
  static_for<25>([&](auto I) { *(LAS v4u *)(lw + (25 * hj + I.value) * 1024) = frag<I.value>(pre); });
  const unsigned src = OFF_WT2 + ((2 * (c.bj & 7) + (hj & 1)) * 86 + (hj >> 1) * 43) * 1024;
  v4u w2[22];
#define LDW(i) w2[i] = ld16(c.rws, l * 16, src + ((i) < 18 ? 7 + (i) : 21 + (i)) * 1024)
  lds_wait(c, 40 + layer * 5 + PH_WO, 4);
#pragma unroll
  for (int i = 0; i < 16; ++i) LDW(i);
  BAR();
  lds_wait(c, 40 + layer * 5 + PH_UP, 4);
#pragma unroll
  for (int i = 16; i < 22; ++i) LDW(i);
#pragma unroll
  for (int ks = 25; ks < 39; ++ks) glds<true>(c.rws, l * 16, src + ks * 1024, c.lds0 + ks * 4096 + hj * 1024);
#undef LDW
  lds_wait(c, 16 + layer * 5 + PH_UP, 4);
  static_for<7>([&](auto I) { *(LAS v4u *)(lw + I.value * 4096 + hj * 1024) = frag<25 + I.value>(pre); });
#pragma unroll
  for (int i = 0; i < 18; ++i) *(LAS v4u *)(lw + (7 + i) * 4096 + hj * 1024) = w2[i];
  VMWAIT(0);
  BAR();
  BAR();
#pragma unroll
  for (int i = 0; i < 4; ++i) *(LAS v4u *)(lw + i * 4096 + hj * 1024) = w2[18 + i];
  if (layer == 0) {
    v4u wq[25];
    const unsigned sq = OFF_WTQ + 3 * (c.bj & 15) * NTP + 25 * hj * 1024;
    asm volatile("s_waitcnt lgkmcnt(0)" ::: "memory");
    lds_wait(c, 40 + PH_DOWN, 4);
#pragma unroll
    for (int i = 0; i < 6; ++i) wq[i] = ld16(c.rws, l * 16, sq + i * 1024);
    BAR();
#pragma unroll
    for (int i = 6; i < 25; ++i) wq[i] = ld16(c.rws, l * 16, sq + i * 1024);
    lds_wait(c, 16 + PH_DOWN, 4);
#pragma unroll
    for (int i = 0; i < 25; ++i) *(LAS v4u *)(lw + (25 * hj + i) * 1024) = wq[i];
    BAR();
  } else BAR();
}

#define ATTN_FAST_ASM \
  "s_cmp_eq_u32 %[cnt], 0\n\t" \
  "s_cbranch_scc1 8f\n\t" \
  "s_mov_b32 %[keep], m0\n\t" \
  "v_mov_b32 v248, 0xdddd\n\t" \
  "ds_read_b128 v[224:227], %[ka]\n\t" \
  "ds_read_b128 v[232:235], %[va0]\n\t" \
  "ds_read_b128 v[240:243], %[va1]\n\t" \
  "ds_read_b128 v[228:231], %[ka] offset:4160\n\t" \
  "v_mov_b32 v244, 0\n\t" \
  "v_mov_b32 v245, 0\n\t" \
  "v_mov_b32 v246, 0\n\t" \
  "v_mov_b32 v247, 0\n\t" \
  "s_mov_b32 %[cnt], 0\n\t" \
  "s_nop 0\n\t" \
  "v_mfma_f32_32x32x16_bf16 %[oa], v[244:247], v[244:247], 0\n\t" \
  "v_mfma_f32_32x32x16_bf16 %[ob], v[244:247], v[244:247], 0\n\t" \
  "v_mfma_f32_32x32x16_bf16 v[208:223], v[244:247], v[244:247], 0\n\t" \
  "s_waitcnt lgkmcnt(3)\n\t" \
  "v_mfma_f32_32x32x16_bf16 v[192:207], v[224:227], %[qa], 0\n\t" \
  "s_nop 11\n\t" \
  "s_waitcnt lgkmcnt(0)\n\t" \
  "v_exp_f32 v192, v192\n\t" \
  "v_exp_f32 v193, v193\n\t" \
  "v_smfmac_f32_32x32x32_bf16 %[ob], v[244:247], v[216:223], v248\n\t" \
  "v_exp_f32 v194, v194\n\t" \
  "v_exp_f32 v195, v195\n\t" \
  "v_exp_f32 v196, v196\n\t" \
  "v_mfma_f32_32x32x16_bf16 v[208:223], v[224:227], %[qb], 0\n\t" \
  "ds_read_b128 v[224:227], %[ka] offset:8320\n\t" \
  "ds_read_b128 v[236:239], %[va0] offset:4160\n\t" \
  "ds_read_b128 v[244:247], %[va1] offset:4160\n\t" \
  "v_exp_f32 v197, v197\n\t" \
  "v_exp_f32 v198, v198\n\t" \
  "v_exp_f32 v199, v199\n\t" \
  "s_mov_b32 m0, %[m0v]\n\t" \
  "s_add_u32 %[m0v], %[m0v], 16640\n\t" \
  "buffer_load_dwordx4 %[vo], %[rs], %[so] offen sc1 lds\n\t" \
  "v_exp_f32 v200, v200\n\t" \
  "v_exp_f32 v201, v201\n\t" \
  "v_exp_f32 v202, v202\n\t" \
  "v_smfmac_f32_32x32x32_bf16 %[oa], v[232:235], v[192:199], v248\n\t" \
  "v_exp_f32 v203, v203\n\t" \
  "v_exp_f32 v204, v204\n\t" \
  "v_exp_f32 v205, v205\n\t" \
  "buffer_load_dwordx4 %[vo], %[rs], %[so] offen offset:1024 sc1 lds\n\t" \
  "s_add_u32 %[so], %[so], 16384\n\t" \
  "v_exp_f32 v206, v206\n\t" \
  "v_exp_f32 v207, v207\n\t" \
  "v_exp_f32 v208, v208\n\t" \
  "v_exp_f32 v209, v209\n\t" \
  "v_smfmac_f32_32x32x32_bf16 %[oa], v[240:243], v[200:207], v248\n\t" \
  "v_exp_f32 v210, v210\n\t" \
  "v_exp_f32 v211, v211\n\t" \
  "v_exp_f32 v212, v212\n\t" \
  "v_mfma_f32_32x32x16_bf16 v[192:207], v[228:231], %[qa], 0\n\t" \
  "v_exp_f32 v213, v213\n\t" \
  "v_exp_f32 v214, v214\n\t" \
  "v_exp_f32 v215, v215\n\t" \
  "s_mov_b32 m0, %[m0v]\n\t" \
  "s_add_u32 %[m0v], %[m0v], 16640\n\t" \
  "buffer_load_dwordx4 %[vo], %[rs], %[so] offen sc1 lds\n\t" \
  "v_exp_f32 v216, v216\n\t" \
  "v_exp_f32 v217, v217\n\t" \
  "v_exp_f32 v218, v218\n\t" \
  "v_smfmac_f32_32x32x32_bf16 %[ob], v[232:235], v[208:215], v248\n\t" \
  "v_exp_f32 v219, v219\n\t" \
  "v_exp_f32 v220, v220\n\t" \
  "v_exp_f32 v221, v221\n\t" \
  "buffer_load_dwordx4 %[vo], %[rs], %[so] offen offset:1024 sc1 lds\n\t" \
  "s_add_u32 %[so], %[so], 16384\n\t" \
  "v_exp_f32 v222, v222\n\t" \
  "v_exp_f32 v223, v223\n\t" \
  "s_waitcnt lgkmcnt(0)\n\t" \
  "v_exp_f32 v192, v192\n\t" \
  "v_exp_f32 v193, v193\n\t" \
  "v_smfmac_f32_32x32x32_bf16 %[ob], v[240:243], v[216:223], v248\n\t" \
  "v_exp_f32 v194, v194\n\t" \
  "v_exp_f32 v195, v195\n\t" \
  "v_exp_f32 v196, v196\n\t" \
  "v_mfma_f32_32x32x16_bf16 v[208:223], v[228:231], %[qb], 0\n\t" \
  "ds_read_b128 v[228:231], %[ka] offset:12480\n\t" \
  "ds_read_b128 v[232:235], %[va0] offset:8320\n\t" \
  "ds_read_b128 v[240:243], %[va1] offset:8320\n\t" \
  "v_exp_f32 v197, v197\n\t" \
  "v_exp_f32 v198, v198\n\t" \
  "v_exp_f32 v199, v199\n\t" \
  "s_mov_b32 m0, %[m0v]\n\t" \
  "s_add_u32 %[m0v], %[m0v], 16640\n\t" \
  "buffer_load_dwordx4 %[vo], %[rs], %[so] offen sc1 lds\n\t" \
  "v_exp_f32 v200, v200\n\t" \
  "v_exp_f32 v201, v201\n\t" \
  "v_exp_f32 v202, v202\n\t" \
  "v_smfmac_f32_32x32x32_bf16 %[oa], v[236:239], v[192:199], v248\n\t" \
  "v_exp_f32 v203, v203\n\t" \
  "v_exp_f32 v204, v204\n\t" \
  "v_exp_f32 v205, v205\n\t" \
  "buffer_load_dwordx4 %[vo], %[rs], %[so] offen offset:1024 sc1 lds\n\t" \
  "s_add_u32 %[so], %[so], 16384\n\t" \
  "v_exp_f32 v206, v206\n\t" \
  "v_exp_f32 v207, v207\n\t" \
  "v_exp_f32 v208, v208\n\t" \
  "v_exp_f32 v209, v209\n\t" \
  "v_smfmac_f32_32x32x32_bf16 %[oa], v[244:247], v[200:207], v248\n\t" \
  "v_exp_f32 v210, v210\n\t" \
  "v_exp_f32 v211, v211\n\t" \
  "v_exp_f32 v212, v212\n\t" \
  "v_mfma_f32_32x32x16_bf16 v[192:207], v[224:227], %[qa], 0\n\t" \
  "v_exp_f32 v213, v213\n\t" \
  "v_exp_f32 v214, v214\n\t" \
  "v_exp_f32 v215, v215\n\t" \
  "s_mov_b32 m0, %[m0v]\n\t" \
  "s_add_u32 %[m0v], %[m0v], 16640\n\t" \
  "buffer_load_dwordx4 %[vo], %[rs], %[so] offen sc1 lds\n\t" \
  "v_exp_f32 v216, v216\n\t" \
  "v_exp_f32 v217, v217\n\t" \
  "v_exp_f32 v218, v218\n\t" \
  "v_smfmac_f32_32x32x32_bf16 %[ob], v[236:239], v[208:215], v248\n\t" \
  "v_exp_f32 v219, v219\n\t" \
  "v_exp_f32 v220, v220\n\t" \
  "v_exp_f32 v221, v221\n\t" \
  "buffer_load_dwordx4 %[vo], %[rs], %[so] offen offset:1024 sc1 lds\n\t" \
  "s_add_u32 %[so], %[so], 16384\n\t" \
  "v_exp_f32 v222, v222\n\t" \
  "v_exp_f32 v223, v223\n\t" \
  "s_waitcnt vmcnt(6)\n\t" \
  "s_barrier\n\t" \
  "s_waitcnt lgkmcnt(0)\n\t" \
  "v_exp_f32 v192, v192\n\t" \
  "v_exp_f32 v193, v193\n\t" \
  "v_smfmac_f32_32x32x32_bf16 %[ob], v[244:247], v[216:223], v248\n\t" \
  "v_exp_f32 v194, v194\n\t" \
  "v_exp_f32 v195, v195\n\t" \
  "v_exp_f32 v196, v196\n\t" \
  "v_mfma_f32_32x32x16_bf16 v[208:223], v[224:227], %[qb], 0\n\t" \
  "ds_read_b128 v[224:227], %[ka] offset:16640\n\t" \
  "ds_read_b128 v[236:239], %[va0] offset:12480\n\t" \
  "ds_read_b128 v[244:247], %[va1] offset:12480\n\t" \
  "v_exp_f32 v197, v197\n\t" \
  "v_exp_f32 v198, v198\n\t" \
  "v_exp_f32 v199, v199\n\t" \
  "s_mov_b32 m0, %[m0v]\n\t" \
  "s_add_u32 %[m0v], %[m0v], 16640\n\t" \
  "buffer_load_dwordx4 %[vo], %[rs], %[so] offen sc1 lds\n\t" \
  "v_exp_f32 v200, v200\n\t" \
  "v_exp_f32 v201, v201\n\t" \
  "v_exp_f32 v202, v202\n\t" \
  "v_smfmac_f32_32x32x32_bf16 %[oa], v[232:235], v[192:199], v248\n\t" \
  "v_exp_f32 v203, v203\n\t" \
  "v_exp_f32 v204, v204\n\t" \
  "v_exp_f32 v205, v205\n\t" \
  "buffer_load_dwordx4 %[vo], %[rs], %[so] offen offset:1024 sc1 lds\n\t" \
  "s_add_u32 %[so], %[so], 16384\n\t" \
  "v_exp_f32 v206, v206\n\t" \
  "v_exp_f32 v207, v207\n\t" \
  "v_exp_f32 v208, v208\n\t" \
  "v_exp_f32 v209, v209\n\t" \
  "v_smfmac_f32_32x32x32_bf16 %[oa], v[240:243], v[200:207], v248\n\t" \
  "v_exp_f32 v210, v210\n\t" \
  "v_exp_f32 v211, v211\n\t" \
  "v_exp_f32 v212, v212\n\t" \
  "v_mfma_f32_32x32x16_bf16 v[192:207], v[228:231], %[qa], 0\n\t" \
  "v_exp_f32 v213, v213\n\t" \
  "v_exp_f32 v214, v214\n\t" \
  "v_exp_f32 v215, v215\n\t" \
  "s_mov_b32 m0, %[m0v]\n\t" \
  "s_add_u32 %[m0v], %[m0v], 16640\n\t" \
  "buffer_load_dwordx4 %[vo], %[rs], %[so] offen sc1 lds\n\t" \
  "v_exp_f32 v216, v216\n\t" \
  "v_exp_f32 v217, v217\n\t" \
  "v_exp_f32 v218, v218\n\t" \
  "v_smfmac_f32_32x32x32_bf16 %[ob], v[232:235], v[208:215], v248\n\t" \
  "v_exp_f32 v219, v219\n\t" \
  "v_exp_f32 v220, v220\n\t" \
  "v_exp_f32 v221, v221\n\t" \
  "buffer_load_dwordx4 %[vo], %[rs], %[so] offen offset:1024 sc1 lds\n\t" \
  "s_add_u32 %[so], %[so], 16384\n\t" \
  "v_exp_f32 v222, v222\n\t" \
  "v_exp_f32 v223, v223\n\t" \
  "s_waitcnt lgkmcnt(0)\n\t" \
  "v_exp_f32 v192, v192\n\t" \
  "v_exp_f32 v193, v193\n\t" \
  "v_smfmac_f32_32x32x32_bf16 %[ob], v[240:243], v[216:223], v248\n\t" \
  "v_exp_f32 v194, v194\n\t" \
  "v_exp_f32 v195, v195\n\t" \
  "v_exp_f32 v196, v196\n\t" \
  "v_mfma_f32_32x32x16_bf16 v[208:223], v[228:231], %[qb], 0\n\t" \
  "ds_read_b128 v[228:231], %[ka] offset:20800\n\t" \
  "ds_read_b128 v[232:235], %[va0] offset:16640\n\t" \
  "ds_read_b128 v[240:243], %[va1] offset:16640\n\t" \
  "v_exp_f32 v197, v197\n\t" \
  "v_exp_f32 v198, v198\n\t" \
  "v_exp_f32 v199, v199\n\t" \
  "s_mov_b32 m0, %[m0v]\n\t" \
  "s_add_u32 %[m0v], %[m0v], 16640\n\t" \
  "buffer_load_dwordx4 %[vo], %[rs], %[so] offen sc1 lds\n\t" \
  "v_exp_f32 v200, v200\n\t" \
  "v_exp_f32 v201, v201\n\t" \
  "v_exp_f32 v202, v202\n\t" \
  "v_smfmac_f32_32x32x32_bf16 %[oa], v[236:239], v[192:199], v248\n\t" \
  "v_exp_f32 v203, v203\n\t" \
  "v_exp_f32 v204, v204\n\t" \
  "v_exp_f32 v205, v205\n\t" \
  "buffer_load_dwordx4 %[vo], %[rs], %[so] offen offset:1024 sc1 lds\n\t" \
  "s_add_u32 %[so], %[so], 16384\n\t" \
  "v_exp_f32 v206, v206\n\t" \
  "v_exp_f32 v207, v207\n\t" \
  "v_exp_f32 v208, v208\n\t" \
  "v_exp_f32 v209, v209\n\t" \
  "v_smfmac_f32_32x32x32_bf16 %[oa], v[244:247], v[200:207], v248\n\t" \
  "v_exp_f32 v210, v210\n\t" \
  "v_exp_f32 v211, v211\n\t" \
  "v_exp_f32 v212, v212\n\t" \
  "v_mfma_f32_32x32x16_bf16 v[192:207], v[224:227], %[qa], 0\n\t" \
  "v_exp_f32 v213, v213\n\t" \
  "v_exp_f32 v214, v214\n\t" \
  "v_exp_f32 v215, v215\n\t" \
  "v_exp_f32 v216, v216\n\t" \
  "v_exp_f32 v217, v217\n\t" \
  "v_exp_f32 v218, v218\n\t" \
  "v_smfmac_f32_32x32x32_bf16 %[ob], v[236:239], v[208:215], v248\n\t" \
  "v_exp_f32 v219, v219\n\t" \
  "v_exp_f32 v220, v220\n\t" \
  "v_exp_f32 v221, v221\n\t" \
  "v_exp_f32 v222, v222\n\t" \
  "v_exp_f32 v223, v223\n\t" \
  "s_waitcnt vmcnt(10)\n\t" \
  "s_barrier\n\t" \
  "s_waitcnt lgkmcnt(0)\n\t" \
  "v_exp_f32 v192, v192\n\t" \
  "v_exp_f32 v193, v193\n\t" \
  "v_smfmac_f32_32x32x32_bf16 %[ob], v[244:247], v[216:223], v248\n\t" \
  "v_exp_f32 v194, v194\n\t" \
  "v_exp_f32 v195, v195\n\t" \
  "v_exp_f32 v196, v196\n\t" \
  "v_mfma_f32_32x32x16_bf16 v[208:223], v[224:227], %[qb], 0\n\t" \
  "ds_read_b128 v[224:227], %[ka] offset:24960\n\t" \
  "ds_read_b128 v[236:239], %[va0] offset:20800\n\t" \
  "ds_read_b128 v[244:247], %[va1] offset:20800\n\t" \
  "v_exp_f32 v197, v197\n\t" \
  "v_exp_f32 v198, v198\n\t" \
  "v_exp_f32 v199, v199\n\t" \
  "v_exp_f32 v200, v200\n\t" \
  "v_exp_f32 v201, v201\n\t" \
  "v_exp_f32 v202, v202\n\t" \
  "v_smfmac_f32_32x32x32_bf16 %[oa], v[232:235], v[192:199], v248\n\t" \
  "v_exp_f32 v203, v203\n\t" \
  "v_exp_f32 v204, v204\n\t" \
  "v_exp_f32 v205, v205\n\t" \
  "v_exp_f32 v206, v206\n\t" \
  "v_exp_f32 v207, v207\n\t" \
  "v_exp_f32 v208, v208\n\t" \
  "v_exp_f32 v209, v209\n\t" \
  "v_smfmac_f32_32x32x32_bf16 %[oa], v[240:243], v[200:207], v248\n\t" \
  "v_exp_f32 v210, v210\n\t" \
  "v_exp_f32 v211, v211\n\t" \
  "v_exp_f32 v212, v212\n\t" \
  "v_mfma_f32_32x32x16_bf16 v[192:207], v[228:231], %[qa], 0\n\t" \
  "v_exp_f32 v213, v213\n\t" \
  "v_exp_f32 v214, v214\n\t" \
  "v_exp_f32 v215, v215\n\t" \
  "v_exp_f32 v216, v216\n\t" \
  "v_exp_f32 v217, v217\n\t" \
  "v_exp_f32 v218, v218\n\t" \
  "v_smfmac_f32_32x32x32_bf16 %[ob], v[232:235], v[208:215], v248\n\t" \
  "v_exp_f32 v219, v219\n\t" \
  "v_exp_f32 v220, v220\n\t" \
  "v_exp_f32 v221, v221\n\t" \
  "v_exp_f32 v222, v222\n\t" \
  "v_exp_f32 v223, v223\n\t" \
  "s_waitcnt lgkmcnt(0)\n\t" \
  "v_exp_f32 v192, v192\n\t" \
  "v_exp_f32 v193, v193\n\t" \
  "v_smfmac_f32_32x32x32_bf16 %[ob], v[240:243], v[216:223], v248\n\t" \
  "v_exp_f32 v194, v194\n\t" \
  "v_exp_f32 v195, v195\n\t" \
  "v_exp_f32 v196, v196\n\t" \
  "v_mfma_f32_32x32x16_bf16 v[208:223], v[228:231], %[qb], 0\n\t" \
  "ds_read_b128 v[228:231], %[ka] offset:29120\n\t" \
  "ds_read_b128 v[232:235], %[va0] offset:24960\n\t" \
  "ds_read_b128 v[240:243], %[va1] offset:24960\n\t" \
  "v_exp_f32 v197, v197\n\t" \
  "v_exp_f32 v198, v198\n\t" \
  "v_exp_f32 v199, v199\n\t" \
  "v_exp_f32 v200, v200\n\t" \
  "v_exp_f32 v201, v201\n\t" \
  "v_exp_f32 v202, v202\n\t" \
  "v_smfmac_f32_32x32x32_bf16 %[oa], v[236:239], v[192:199], v248\n\t" \
  "v_exp_f32 v203, v203\n\t" \
  "v_exp_f32 v204, v204\n\t" \
  "v_exp_f32 v205, v205\n\t" \
  "v_exp_f32 v206, v206\n\t" \
  "v_exp_f32 v207, v207\n\t" \
  "v_exp_f32 v208, v208\n\t" \
  "v_exp_f32 v209, v209\n\t" \
  "v_smfmac_f32_32x32x32_bf16 %[oa], v[244:247], v[200:207], v248\n\t" \
  "v_exp_f32 v210, v210\n\t" \
  "v_exp_f32 v211, v211\n\t" \
  "v_exp_f32 v212, v212\n\t" \
  "v_mfma_f32_32x32x16_bf16 v[192:207], v[224:227], %[qa], 0\n\t" \
  "v_exp_f32 v213, v213\n\t" \
  "v_exp_f32 v214, v214\n\t" \
  "v_exp_f32 v215, v215\n\t" \
  "v_exp_f32 v216, v216\n\t" \
  "v_exp_f32 v217, v217\n\t" \
  "v_exp_f32 v218, v218\n\t" \
  "v_smfmac_f32_32x32x32_bf16 %[ob], v[236:239], v[208:215], v248\n\t" \
  "v_exp_f32 v219, v219\n\t" \
  "v_exp_f32 v220, v220\n\t" \
  "v_exp_f32 v221, v221\n\t" \
  "v_exp_f32 v222, v222\n\t" \
  "v_exp_f32 v223, v223\n\t" \
  "s_waitcnt lgkmcnt(0)\n\t" \
  "v_exp_f32 v192, v192\n\t" \
  "v_exp_f32 v193, v193\n\t" \
  "v_smfmac_f32_32x32x32_bf16 %[ob], v[244:247], v[216:223], v248\n\t" \
  "v_exp_f32 v194, v194\n\t" \
  "v_exp_f32 v195, v195\n\t" \
  "v_exp_f32 v196, v196\n\t" \
  "v_mfma_f32_32x32x16_bf16 v[208:223], v[224:227], %[qb], 0\n\t" \
  "ds_read_b128 v[224:227], %[ka] offset:33280\n\t" \
  "ds_read_b128 v[236:239], %[va0] offset:29120\n\t" \
  "ds_read_b128 v[244:247], %[va1] offset:29120\n\t" \
  "v_exp_f32 v197, v197\n\t" \
  "v_exp_f32 v198, v198\n\t" \
  "v_exp_f32 v199, v199\n\t" \
  "buffer_load_dwordx2 v[250:251], %[fo], %[rs], 0 offen sc1\n\t" \
  "v_exp_f32 v200, v200\n\t" \
  "v_exp_f32 v201, v201\n\t" \
  "v_exp_f32 v202, v202\n\t" \
  "v_smfmac_f32_32x32x32_bf16 %[oa], v[232:235], v[192:199], v248\n\t" \
  "v_exp_f32 v203, v203\n\t" \
  "v_exp_f32 v204, v204\n\t" \
  "v_exp_f32 v205, v205\n\t" \
  "v_exp_f32 v206, v206\n\t" \
  "v_exp_f32 v207, v207\n\t" \
  "v_exp_f32 v208, v208\n\t" \
  "v_exp_f32 v209, v209\n\t" \
  "v_smfmac_f32_32x32x32_bf16 %[oa], v[240:243], v[200:207], v248\n\t" \
  "v_exp_f32 v210, v210\n\t" \
  "v_exp_f32 v211, v211\n\t" \
  "v_exp_f32 v212, v212\n\t" \
  "v_mfma_f32_32x32x16_bf16 v[192:207], v[228:231], %[qa], 0\n\t" \
  "v_exp_f32 v213, v213\n\t" \
  "v_exp_f32 v214, v214\n\t" \
  "v_exp_f32 v215, v215\n\t" \
  "v_exp_f32 v216, v216\n\t" \
  "v_exp_f32 v217, v217\n\t" \
  "v_exp_f32 v218, v218\n\t" \
  "v_smfmac_f32_32x32x32_bf16 %[ob], v[232:235], v[208:215], v248\n\t" \
  "v_exp_f32 v219, v219\n\t" \
  "v_exp_f32 v220, v220\n\t" \
  "v_exp_f32 v221, v221\n\t" \
  "v_exp_f32 v222, v222\n\t" \
  "v_exp_f32 v223, v223\n\t" \
  "s_waitcnt lgkmcnt(0)\n\t" \
  "v_exp_f32 v192, v192\n\t" \
  "v_exp_f32 v193, v193\n\t" \
  "v_smfmac_f32_32x32x32_bf16 %[ob], v[240:243], v[216:223], v248\n\t" \
  "v_exp_f32 v194, v194\n\t" \
  "v_exp_f32 v195, v195\n\t" \
  "v_exp_f32 v196, v196\n\t" \
  "v_mfma_f32_32x32x16_bf16 v[208:223], v[228:231], %[qb], 0\n\t" \
  "ds_read_b128 v[228:231], %[ka] offset:37440\n\t" \
  "ds_read_b128 v[232:235], %[va0] offset:33280\n\t" \
  "ds_read_b128 v[240:243], %[va1] offset:33280\n\t" \
  "v_exp_f32 v197, v197\n\t" \
  "v_exp_f32 v198, v198\n\t" \
  "v_exp_f32 v199, v199\n\t" \
  "v_exp_f32 v200, v200\n\t" \
  "v_exp_f32 v201, v201\n\t" \
  "v_exp_f32 v202, v202\n\t" \
  "v_smfmac_f32_32x32x32_bf16 %[oa], v[236:239], v[192:199], v248\n\t" \
  "v_exp_f32 v203, v203\n\t" \
  "v_exp_f32 v204, v204\n\t" \
  "v_exp_f32 v205, v205\n\t" \
  "v_exp_f32 v206, v206\n\t" \
  "v_exp_f32 v207, v207\n\t" \
  "v_exp_f32 v208, v208\n\t" \
  "v_exp_f32 v209, v209\n\t" \
  "v_smfmac_f32_32x32x32_bf16 %[oa], v[244:247], v[200:207], v248\n\t" \
  "v_exp_f32 v210, v210\n\t" \
  "v_exp_f32 v211, v211\n\t" \
  "v_exp_f32 v212, v212\n\t" \
  "v_mfma_f32_32x32x16_bf16 v[192:207], v[224:227], %[qa], 0\n\t" \
  "v_exp_f32 v213, v213\n\t" \
  "v_exp_f32 v214, v214\n\t" \
  "v_exp_f32 v215, v215\n\t" \
  "v_exp_f32 v216, v216\n\t" \
  "v_exp_f32 v217, v217\n\t" \
  "v_exp_f32 v218, v218\n\t" \
  "v_smfmac_f32_32x32x32_bf16 %[ob], v[236:239], v[208:215], v248\n\t" \
  "v_exp_f32 v219, v219\n\t" \
  "v_exp_f32 v220, v220\n\t" \
  "v_exp_f32 v221, v221\n\t" \
  "v_exp_f32 v222, v222\n\t" \
  "v_exp_f32 v223, v223\n\t" \
  "v_add_u32 %[ka], 33280, %[ka]\n\t" \
  "v_add_u32 %[va0], 33280, %[va0]\n\t" \
  "v_add_u32 %[va1], 33280, %[va1]\n\t" \
  "s_mov_b32 m0, %[dummy]\n\t" \
  "s_mov_b32 %[so], 0x100000\n\t" \
  "5:\n\t" \
  "s_waitcnt vmcnt(0)\n\t" \
  "v_cmp_ne_u64 vcc, %[tag], v[250:251]\n\t" \
  "s_cbranch_vccz 6f\n\t" \
  "buffer_load_dwordx2 v[250:251], %[fo], %[rs], 0 offen sc1\n\t" \
  "s_sub_u32 %[so], %[so], 1\n\t" \
  "s_cbranch_scc0 5b\n\t" \
  "6:\n\t" \
  "s_barrier\n\t" \
  "s_waitcnt lgkmcnt(0)\n\t" \
  "v_exp_f32 v192, v192\n\t" \
  "v_exp_f32 v193, v193\n\t" \
  "v_smfmac_f32_32x32x32_bf16 %[ob], v[244:247], v[216:223], v248\n\t" \
  "v_exp_f32 v194, v194\n\t" \
  "v_exp_f32 v195, v195\n\t" \
  "v_exp_f32 v196, v196\n\t" \
  "v_mfma_f32_32x32x16_bf16 v[208:223], v[224:227], %[qb], 0\n\t" \
  "ds_read_b128 v[224:227], %[ka] offset:8320\n\t" \
  "ds_read_b128 v[236:239], %[va0] offset:4160\n\t" \
  "ds_read_b128 v[244:247], %[va1] offset:4160\n\t" \
  "v_exp_f32 v197, v197\n\t" \
  "v_exp_f32 v198, v198\n\t" \
  "v_exp_f32 v199, v199\n\t" \
  "s_mov_b32 %[so], %[p0]\n\t" \
  "buffer_load_dwordx4 v[64:67], %[vo], %[rs], %[so] offen offset:0 sc1\n\t" \
  "v_exp_f32 v200, v200\n\t" \
  "v_exp_f32 v201, v201\n\t" \
  "v_exp_f32 v202, v202\n\t" \
  "v_smfmac_f32_32x32x32_bf16 %[oa], v[232:235], v[192:199], v248\n\t" \
  "v_exp_f32 v203, v203\n\t" \
  "v_exp_f32 v204, v204\n\t" \
  "v_exp_f32 v205, v205\n\t" \
  "v_exp_f32 v206, v206\n\t" \
  "v_exp_f32 v207, v207\n\t" \
  "v_exp_f32 v208, v208\n\t" \
  "v_exp_f32 v209, v209\n\t" \
  "v_smfmac_f32_32x32x32_bf16 %[oa], v[240:243], v[200:207], v248\n\t" \
  "v_exp_f32 v210, v210\n\t" \
  "v_exp_f32 v211, v211\n\t" \
  "v_exp_f32 v212, v212\n\t" \
  "v_mfma_f32_32x32x16_bf16 v[192:207], v[228:231], %[qa], 0\n\t" \
  "v_exp_f32 v213, v213\n\t" \
  "v_exp_f32 v214, v214\n\t" \
  "v_exp_f32 v215, v215\n\t" \
  "buffer_load_dwordx4 v[68:71], %[vo], %[rs], %[so] offen offset:1024 sc1\n\t" \
  "v_exp_f32 v216, v216\n\t" \
  "v_exp_f32 v217, v217\n\t" \
  "v_exp_f32 v218, v218\n\t" \
  "v_smfmac_f32_32x32x32_bf16 %[ob], v[232:235], v[208:215], v248\n\t" \
  "v_exp_f32 v219, v219\n\t" \
  "v_exp_f32 v220, v220\n\t" \
  "v_exp_f32 v221, v221\n\t" \
  "buffer_load_dwordx4 %[vo], %[rw], %[wo] offen offset:0 lds\n\t" \
  "v_exp_f32 v222, v222\n\t" \
  "v_exp_f32 v223, v223\n\t" \
  "s_waitcnt lgkmcnt(0)\n\t" \
  "v_exp_f32 v192, v192\n\t" \
  "v_exp_f32 v193, v193\n\t" \
  "v_smfmac_f32_32x32x32_bf16 %[ob], v[240:243], v[216:223], v248\n\t" \
  "v_exp_f32 v194, v194\n\t" \
  "v_exp_f32 v195, v195\n\t" \
  "v_exp_f32 v196, v196\n\t" \
  "v_mfma_f32_32x32x16_bf16 v[208:223], v[228:231], %[qb], 0\n\t" \
  "ds_read_b128 v[228:231], %[ka] offset:12480\n\t" \
  "ds_read_b128 v[232:235], %[va0] offset:8320\n\t" \
  "ds_read_b128 v[240:243], %[va1] offset:8320\n\t" \
  "v_exp_f32 v197, v197\n\t" \
  "v_exp_f32 v198, v198\n\t" \
  "v_exp_f32 v199, v199\n\t" \
  "buffer_load_dwordx4 v[72:75], %[vo], %[rs], %[so] offen offset:2048 sc1\n\t" \
  "v_exp_f32 v200, v200\n\t" \
  "v_exp_f32 v201, v201\n\t" \
  "v_exp_f32 v202, v202\n\t" \
  "v_smfmac_f32_32x32x32_bf16 %[oa], v[236:239], v[192:199], v248\n\t" \
  "v_exp_f32 v203, v203\n\t" \
  "v_exp_f32 v204, v204\n\t" \
  "v_exp_f32 v205, v205\n\t" \
  "v_exp_f32 v206, v206\n\t" \
  "v_exp_f32 v207, v207\n\t" \
  "v_exp_f32 v208, v208\n\t" \
  "v_exp_f32 v209, v209\n\t" \
  "v_smfmac_f32_32x32x32_bf16 %[oa], v[244:247], v[200:207], v248\n\t" \
  "v_exp_f32 v210, v210\n\t" \
  "v_exp_f32 v211, v211\n\t" \
  "v_exp_f32 v212, v212\n\t" \
  "v_mfma_f32_32x32x16_bf16 v[192:207], v[224:227], %[qa], 0\n\t" \
  "v_exp_f32 v213, v213\n\t" \
  "v_exp_f32 v214, v214\n\t" \
  "v_exp_f32 v215, v215\n\t" \
  "buffer_load_dwordx4 v[76:79], %[vo], %[rs], %[so] offen offset:3072 sc1\n\t" \
  "v_exp_f32 v216, v216\n\t" \
  "v_exp_f32 v217, v217\n\t" \
  "v_exp_f32 v218, v218\n\t" \
  "v_smfmac_f32_32x32x32_bf16 %[ob], v[236:239], v[208:215], v248\n\t" \
  "v_exp_f32 v219, v219\n\t" \
  "v_exp_f32 v220, v220\n\t" \
  "v_exp_f32 v221, v221\n\t" \
  "v_exp_f32 v222, v222\n\t" \
  "v_exp_f32 v223, v223\n\t" \
  "s_waitcnt lgkmcnt(0)\n\t" \
  "v_exp_f32 v192, v192\n\t" \
  "v_exp_f32 v193, v193\n\t" \
  "v_smfmac_f32_32x32x32_bf16 %[ob], v[244:247], v[216:223], v248\n\t" \
  "v_exp_f32 v194, v194\n\t" \
  "v_exp_f32 v195, v195\n\t" \
  "v_exp_f32 v196, v196\n\t" \
  "v_mfma_f32_32x32x16_bf16 v[208:223], v[224:227], %[qb], 0\n\t" \
  "ds_read_b128 v[224:227], %[ka] offset:16640\n\t" \
  "ds_read_b128 v[236:239], %[va0] offset:12480\n\t" \
  "ds_read_b128 v[244:247], %[va1] offset:12480\n\t" \
  "v_exp_f32 v197, v197\n\t" \
  "v_exp_f32 v198, v198\n\t" \
  "v_exp_f32 v199, v199\n\t" \
  "s_add_u32 %[so], %[so], 4096\n\t" \
  "buffer_load_dwordx4 v[80:83], %[vo], %[rs], %[so] offen offset:0 sc1\n\t" \
  "v_exp_f32 v200, v200\n\t" \
  "v_exp_f32 v201, v201\n\t" \
  "v_exp_f32 v202, v202\n\t" \
  "v_smfmac_f32_32x32x32_bf16 %[oa], v[232:235], v[192:199], v248\n\t" \
  "v_exp_f32 v203, v203\n\t" \
  "v_exp_f32 v204, v204\n\t" \
  "v_exp_f32 v205, v205\n\t" \
  "buffer_load_dwordx4 %[vo], %[rw], %[wo] offen offset:1024 lds\n\t" \
  "v_exp_f32 v206, v206\n\t" \
  "v_exp_f32 v207, v207\n\t" \
  "v_exp_f32 v208, v208\n\t" \
  "v_exp_f32 v209, v209\n\t" \
  "v_smfmac_f32_32x32x32_bf16 %[oa], v[240:243], v[200:207], v248\n\t" \
  "v_exp_f32 v210, v210\n\t" \
  "v_exp_f32 v211, v211\n\t" \
  "v_exp_f32 v212, v212\n\t" \
  "v_mfma_f32_32x32x16_bf16 v[192:207], v[228:231], %[qa], 0\n\t" \
  "v_exp_f32 v213, v213\n\t" \
  "v_exp_f32 v214, v214\n\t" \
  "v_exp_f32 v215, v215\n\t" \
  "buffer_load_dwordx4 v[84:87], %[vo], %[rs], %[so] offen offset:1024 sc1\n\t" \
  "v_exp_f32 v216, v216\n\t" \
  "v_exp_f32 v217, v217\n\t" \
  "v_exp_f32 v218, v218\n\t" \
  "v_smfmac_f32_32x32x32_bf16 %[ob], v[232:235], v[208:215], v248\n\t" \
  "v_exp_f32 v219, v219\n\t" \
  "v_exp_f32 v220, v220\n\t" \
  "v_exp_f32 v221, v221\n\t" \
  "v_exp_f32 v222, v222\n\t" \
  "v_exp_f32 v223, v223\n\t" \
  "s_waitcnt lgkmcnt(0)\n\t" \
  "v_exp_f32 v192, v192\n\t" \
  "v_exp_f32 v193, v193\n\t" \
  "v_smfmac_f32_32x32x32_bf16 %[ob], v[240:243], v[216:223], v248\n\t" \
  "v_exp_f32 v194, v194\n\t" \
  "v_exp_f32 v195, v195\n\t" \
  "v_exp_f32 v196, v196\n\t" \
  "v_mfma_f32_32x32x16_bf16 v[208:223], v[228:231], %[qb], 0\n\t" \
  "ds_read_b128 v[228:231], %[ka] offset:20800\n\t" \
  "ds_read_b128 v[232:235], %[va0] offset:16640\n\t" \
  "ds_read_b128 v[240:243], %[va1] offset:16640\n\t" \
  "v_exp_f32 v197, v197\n\t" \
  "v_exp_f32 v198, v198\n\t" \
  "v_exp_f32 v199, v199\n\t" \
  "buffer_load_dwordx4 v[88:91], %[vo], %[rs], %[so] offen offset:2048 sc1\n\t" \
  "v_exp_f32 v200, v200\n\t" \
  "v_exp_f32 v201, v201\n\t" \
  "v_exp_f32 v202, v202\n\t" \
  "v_smfmac_f32_32x32x32_bf16 %[oa], v[236:239], v[192:199], v248\n\t" \
  "v_exp_f32 v203, v203\n\t" \
  "v_exp_f32 v204, v204\n\t" \
  "v_exp_f32 v205, v205\n\t" \
  "v_exp_f32 v206, v206\n\t" \
  "v_exp_f32 v207, v207\n\t" \
  "v_exp_f32 v208, v208\n\t" \
  "v_exp_f32 v209, v209\n\t" \
  "v_smfmac_f32_32x32x32_bf16 %[oa], v[244:247], v[200:207], v248\n\t" \
  "v_exp_f32 v210, v210\n\t" \
  "v_exp_f32 v211, v211\n\t" \
  "v_exp_f32 v212, v212\n\t" \
  "v_mfma_f32_32x32x16_bf16 v[192:207], v[224:227], %[qa], 0\n\t" \
  "v_exp_f32 v213, v213\n\t" \
  "v_exp_f32 v214, v214\n\t" \
  "v_exp_f32 v215, v215\n\t" \
  "buffer_load_dwordx4 v[92:95], %[vo], %[rs], %[so] offen offset:3072 sc1\n\t" \
  "v_exp_f32 v216, v216\n\t" \
  "v_exp_f32 v217, v217\n\t" \
  "v_exp_f32 v218, v218\n\t" \
  "v_smfmac_f32_32x32x32_bf16 %[ob], v[236:239], v[208:215], v248\n\t" \
  "v_exp_f32 v219, v219\n\t" \
  "v_exp_f32 v220, v220\n\t" \
  "v_exp_f32 v221, v221\n\t" \
  "buffer_load_dwordx4 %[vo], %[rw], %[wo] offen offset:2048 lds\n\t" \
  "v_exp_f32 v222, v222\n\t" \
  "v_exp_f32 v223, v223\n\t" \
  "s_waitcnt lgkmcnt(0)\n\t" \
  "v_exp_f32 v192, v192\n\t" \
  "v_exp_f32 v193, v193\n\t" \
  "v_smfmac_f32_32x32x32_bf16 %[ob], v[244:247], v[216:223], v248\n\t" \
  "v_exp_f32 v194, v194\n\t" \
  "v_exp_f32 v195, v195\n\t" \
  "v_exp_f32 v196, v196\n\t" \
  "v_mfma_f32_32x32x16_bf16 v[208:223], v[224:227], %[qb], 0\n\t" \
  "ds_read_b128 v[224:227], %[ka] offset:24960\n\t" \
  "ds_read_b128 v[236:239], %[va0] offset:20800\n\t" \
  "ds_read_b128 v[244:247], %[va1] offset:20800\n\t" \
  "v_exp_f32 v197, v197\n\t" \
  "v_exp_f32 v198, v198\n\t" \
  "v_exp_f32 v199, v199\n\t" \
  "s_add_u32 %[so], %[so], 4096\n\t" \
  "buffer_load_dwordx4 v[96:99], %[vo], %[rs], %[so] offen offset:0 sc1\n\t" \
  "v_exp_f32 v200, v200\n\t" \
  "v_exp_f32 v201, v201\n\t" \
  "v_exp_f32 v202, v202\n\t" \
  "v_smfmac_f32_32x32x32_bf16 %[oa], v[232:235], v[192:199], v248\n\t" \
  "v_exp_f32 v203, v203\n\t" \
  "v_exp_f32 v204, v204\n\t" \
  "v_exp_f32 v205, v205\n\t" \
  "v_exp_f32 v206, v206\n\t" \
  "v_exp_f32 v207, v207\n\t" \
  "v_exp_f32 v208, v208\n\t" \
  "v_exp_f32 v209, v209\n\t" \
  "v_smfmac_f32_32x32x32_bf16 %[oa], v[240:243], v[200:207], v248\n\t" \
  "v_exp_f32 v210, v210\n\t" \
  "v_exp_f32 v211, v211\n\t" \
  "v_exp_f32 v212, v212\n\t" \
  "v_mfma_f32_32x32x16_bf16 v[192:207], v[228:231], %[qa], 0\n\t" \
  "v_exp_f32 v213, v213\n\t" \
  "v_exp_f32 v214, v214\n\t" \
  "v_exp_f32 v215, v215\n\t" \
  "buffer_load_dwordx4 v[100:103], %[vo], %[rs], %[so] offen offset:1024 sc1\n\t" \
  "v_exp_f32 v216, v216\n\t" \
  "v_exp_f32 v217, v217\n\t" \
  "v_exp_f32 v218, v218\n\t" \
  "v_smfmac_f32_32x32x32_bf16 %[ob], v[232:235], v[208:215], v248\n\t" \
  "v_exp_f32 v219, v219\n\t" \
  "v_exp_f32 v220, v220\n\t" \
  "v_exp_f32 v221, v221\n\t" \
  "v_exp_f32 v222, v222\n\t" \
  "v_exp_f32 v223, v223\n\t" \
  "s_waitcnt lgkmcnt(0)\n\t" \
  "v_exp_f32 v192, v192\n\t" \
  "v_exp_f32 v193, v193\n\t" \
  "v_smfmac_f32_32x32x32_bf16 %[ob], v[240:243], v[216:223], v248\n\t" \
  "v_exp_f32 v194, v194\n\t" \
  "v_exp_f32 v195, v195\n\t" \
  "v_exp_f32 v196, v196\n\t" \
  "v_mfma_f32_32x32x16_bf16 v[208:223], v[228:231], %[qb], 0\n\t" \
  "ds_read_b128 v[228:231], %[ka] offset:29120\n\t" \
  "ds_read_b128 v[232:235], %[va0] offset:24960\n\t" \
  "ds_read_b128 v[240:243], %[va1] offset:24960\n\t" \
  "v_exp_f32 v197, v197\n\t" \
  "v_exp_f32 v198, v198\n\t" \
  "v_exp_f32 v199, v199\n\t" \
  "buffer_load_dwordx4 v[104:107], %[vo], %[rs], %[so] offen offset:2048 sc1\n\t" \
  "v_exp_f32 v200, v200\n\t" \
  "v_exp_f32 v201, v201\n\t" \
  "v_exp_f32 v202, v202\n\t" \
  "v_smfmac_f32_32x32x32_bf16 %[oa], v[236:239], v[192:199], v248\n\t" \
  "v_exp_f32 v203, v203\n\t" \
  "v_exp_f32 v204, v204\n\t" \
  "v_exp_f32 v205, v205\n\t" \
  "buffer_load_dwordx4 %[vo], %[rw], %[wo] offen offset:3072 lds\n\t" \
  "v_exp_f32 v206, v206\n\t" \
  "v_exp_f32 v207, v207\n\t" \
  "v_exp_f32 v208, v208\n\t" \
  "v_exp_f32 v209, v209\n\t" \
  "v_smfmac_f32_32x32x32_bf16 %[oa], v[244:247], v[200:207], v248\n\t" \
  "v_exp_f32 v210, v210\n\t" \
  "v_exp_f32 v211, v211\n\t" \
  "v_exp_f32 v212, v212\n\t" \
  "v_mfma_f32_32x32x16_bf16 v[192:207], v[224:227], %[qa], 0\n\t" \
  "v_exp_f32 v213, v213\n\t" \
  "v_exp_f32 v214, v214\n\t" \
  "v_exp_f32 v215, v215\n\t" \
  "buffer_load_dwordx4 v[108:111], %[vo], %[rs], %[so] offen offset:3072 sc1\n\t" \
  "v_exp_f32 v216, v216\n\t" \
  "v_exp_f32 v217, v217\n\t" \
  "v_exp_f32 v218, v218\n\t" \
  "v_smfmac_f32_32x32x32_bf16 %[ob], v[236:239], v[208:215], v248\n\t" \
  "v_exp_f32 v219, v219\n\t" \
  "v_exp_f32 v220, v220\n\t" \
  "v_exp_f32 v221, v221\n\t" \
  "v_exp_f32 v222, v222\n\t" \
  "v_exp_f32 v223, v223\n\t" \
  "s_waitcnt lgkmcnt(0)\n\t" \
  "v_exp_f32 v192, v192\n\t" \
  "v_exp_f32 v193, v193\n\t" \
  "v_smfmac_f32_32x32x32_bf16 %[ob], v[244:247], v[216:223], v248\n\t" \
  "v_exp_f32 v194, v194\n\t" \
  "v_exp_f32 v195, v195\n\t" \
  "v_exp_f32 v196, v196\n\t" \
  "v_mfma_f32_32x32x16_bf16 v[208:223], v[224:227], %[qb], 0\n\t" \
  "ds_read_b128 v[224:227], %[ka] offset:33280\n\t" \
  "ds_read_b128 v[236:239], %[va0] offset:29120\n\t" \
  "ds_read_b128 v[244:247], %[va1] offset:29120\n\t" \
  "v_exp_f32 v197, v197\n\t" \
  "v_exp_f32 v198, v198\n\t" \
  "v_exp_f32 v199, v199\n\t" \
  "s_add_u32 %[so], %[so], 4096\n\t" \
  "buffer_load_dwordx4 v[112:115], %[vo], %[rs], %[so] offen offset:0 sc1\n\t" \
  "v_exp_f32 v200, v200\n\t" \
  "v_exp_f32 v201, v201\n\t" \
  "v_exp_f32 v202, v202\n\t" \
  "v_smfmac_f32_32x32x32_bf16 %[oa], v[232:235], v[192:199], v248\n\t" \
  "v_exp_f32 v203, v203\n\t" \
  "v_exp_f32 v204, v204\n\t" \
  "v_exp_f32 v205, v205\n\t" \
  "v_exp_f32 v206, v206\n\t" \
  "v_exp_f32 v207, v207\n\t" \
  "v_exp_f32 v208, v208\n\t" \
  "v_exp_f32 v209, v209\n\t" \
  "v_smfmac_f32_32x32x32_bf16 %[oa], v[240:243], v[200:207], v248\n\t" \
  "v_exp_f32 v210, v210\n\t" \
  "v_exp_f32 v211, v211\n\t" \
  "v_exp_f32 v212, v212\n\t" \
  "v_mfma_f32_32x32x16_bf16 v[192:207], v[228:231], %[qa], 0\n\t" \
  "v_exp_f32 v213, v213\n\t" \
  "v_exp_f32 v214, v214\n\t" \
  "v_exp_f32 v215, v215\n\t" \
  "buffer_load_dwordx4 v[116:119], %[vo], %[rs], %[so] offen offset:1024 sc1\n\t" \
  "v_exp_f32 v216, v216\n\t" \
  "v_exp_f32 v217, v217\n\t" \
  "v_exp_f32 v218, v218\n\t" \
  "v_smfmac_f32_32x32x32_bf16 %[ob], v[232:235], v[208:215], v248\n\t" \
  "v_exp_f32 v219, v219\n\t" \
  "v_exp_f32 v220, v220\n\t" \
  "v_exp_f32 v221, v221\n\t" \
  "s_add_u32 %[wo], %[wo], 0x100000\n\t" \
  "buffer_load_dwordx4 %[vo], %[rw], %[wo] offen offset:0 lds\n\t" \
  "v_exp_f32 v222, v222\n\t" \
  "v_exp_f32 v223, v223\n\t" \
  "s_waitcnt lgkmcnt(0)\n\t" \
  "v_exp_f32 v192, v192\n\t" \
  "v_exp_f32 v193, v193\n\t" \
  "v_smfmac_f32_32x32x32_bf16 %[ob], v[240:243], v[216:223], v248\n\t" \
  "v_exp_f32 v194, v194\n\t" \
  "v_exp_f32 v195, v195\n\t" \
  "v_exp_f32 v196, v196\n\t" \
  "v_mfma_f32_32x32x16_bf16 v[208:223], v[228:231], %[qb], 0\n\t" \
  "ds_read_b128 v[228:231], %[ka] offset:37440\n\t" \
  "ds_read_b128 v[232:235], %[va0] offset:33280\n\t" \
  "ds_read_b128 v[240:243], %[va1] offset:33280\n\t" \
  "v_exp_f32 v197, v197\n\t" \
  "v_exp_f32 v198, v198\n\t" \
  "v_exp_f32 v199, v199\n\t" \
  "buffer_load_dwordx4 v[120:123], %[vo], %[rs], %[so] offen offset:2048 sc1\n\t" \
  "v_exp_f32 v200, v200\n\t" \
  "v_exp_f32 v201, v201\n\t" \
  "v_exp_f32 v202, v202\n\t" \
  "v_smfmac_f32_32x32x32_bf16 %[oa], v[236:239], v[192:199], v248\n\t" \
  "v_exp_f32 v203, v203\n\t" \
  "v_exp_f32 v204, v204\n\t" \
  "v_exp_f32 v205, v205\n\t" \
  "v_exp_f32 v206, v206\n\t" \
  "v_exp_f32 v207, v207\n\t" \
  "v_exp_f32 v208, v208\n\t" \
  "v_exp_f32 v209, v209\n\t" \
  "v_smfmac_f32_32x32x32_bf16 %[oa], v[244:247], v[200:207], v248\n\t" \
  "v_exp_f32 v210, v210\n\t" \
  "v_exp_f32 v211, v211\n\t" \
  "v_exp_f32 v212, v212\n\t" \
  "v_mfma_f32_32x32x16_bf16 v[192:207], v[224:227], %[qa], 0\n\t" \
  "v_exp_f32 v213, v213\n\t" \
  "v_exp_f32 v214, v214\n\t" \
  "v_exp_f32 v215, v215\n\t" \
  "buffer_load_dwordx4 v[124:127], %[vo], %[rs], %[so] offen offset:3072 sc1\n\t" \
  "v_exp_f32 v216, v216\n\t" \
  "v_exp_f32 v217, v217\n\t" \
  "v_exp_f32 v218, v218\n\t" \
  "v_smfmac_f32_32x32x32_bf16 %[ob], v[236:239], v[208:215], v248\n\t" \
  "v_exp_f32 v219, v219\n\t" \
  "v_exp_f32 v220, v220\n\t" \
  "v_exp_f32 v221, v221\n\t" \
  "v_exp_f32 v222, v222\n\t" \
  "v_exp_f32 v223, v223\n\t" \
  "v_add_u32 %[ka], 33280, %[ka]\n\t" \
  "v_add_u32 %[va0], 33280, %[va0]\n\t" \
  "v_add_u32 %[va1], 33280, %[va1]\n\t" \
  "s_waitcnt lgkmcnt(0)\n\t" \
  "v_exp_f32 v192, v192\n\t" \
  "v_exp_f32 v193, v193\n\t" \
  "v_smfmac_f32_32x32x32_bf16 %[ob], v[244:247], v[216:223], v248\n\t" \
  "v_exp_f32 v194, v194\n\t" \
  "v_exp_f32 v195, v195\n\t" \
  "v_exp_f32 v196, v196\n\t" \
  "v_mfma_f32_32x32x16_bf16 v[208:223], v[224:227], %[qb], 0\n\t" \
  "ds_read_b128 v[224:227], %[ka] offset:8320\n\t" \
  "ds_read_b128 v[236:239], %[va0] offset:4160\n\t" \
  "ds_read_b128 v[244:247], %[va1] offset:4160\n\t" \
  "v_exp_f32 v197, v197\n\t" \
  "v_exp_f32 v198, v198\n\t" \
  "v_exp_f32 v199, v199\n\t" \
  "s_mov_b32 %[so], %[p1]\n\t" \
  "buffer_load_dwordx4 v[128:131], %[vo], %[rs], %[so] offen offset:0 sc1\n\t" \
  "v_exp_f32 v200, v200\n\t" \
  "v_exp_f32 v201, v201\n\t" \
  "v_exp_f32 v202, v202\n\t" \
  "v_smfmac_f32_32x32x32_bf16 %[oa], v[232:235], v[192:199], v248\n\t" \
  "v_exp_f32 v203, v203\n\t" \
  "v_exp_f32 v204, v204\n\t" \
  "v_exp_f32 v205, v205\n\t" \
  "v_exp_f32 v206, v206\n\t" \
  "v_exp_f32 v207, v207\n\t" \
  "v_exp_f32 v208, v208\n\t" \
  "v_exp_f32 v209, v209\n\t" \
  "v_smfmac_f32_32x32x32_bf16 %[oa], v[240:243], v[200:207], v248\n\t" \
  "v_exp_f32 v210, v210\n\t" \
  "v_exp_f32 v211, v211\n\t" \
  "v_exp_f32 v212, v212\n\t" \
  "v_mfma_f32_32x32x16_bf16 v[192:207], v[228:231], %[qa], 0\n\t" \
  "v_exp_f32 v213, v213\n\t" \
  "v_exp_f32 v214, v214\n\t" \
  "v_exp_f32 v215, v215\n\t" \
  "buffer_load_dwordx4 v[132:135], %[vo], %[rs], %[so] offen offset:1024 sc1\n\t" \
  "v_exp_f32 v216, v216\n\t" \
  "v_exp_f32 v217, v217\n\t" \
  "v_exp_f32 v218, v218\n\t" \
  "v_smfmac_f32_32x32x32_bf16 %[ob], v[232:235], v[208:215], v248\n\t" \
  "v_exp_f32 v219, v219\n\t" \
  "v_exp_f32 v220, v220\n\t" \
  "v_exp_f32 v221, v221\n\t" \
  "buffer_load_dwordx4 %[vo], %[rw], %[wo] offen offset:1024 lds\n\t" \
  "v_exp_f32 v222, v222\n\t" \
  "v_exp_f32 v223, v223\n\t" \
  "s_waitcnt lgkmcnt(0)\n\t" \
  "v_exp_f32 v192, v192\n\t" \
  "v_exp_f32 v193, v193\n\t" \
  "v_smfmac_f32_32x32x32_bf16 %[ob], v[240:243], v[216:223], v248\n\t" \
  "v_exp_f32 v194, v194\n\t" \
  "v_exp_f32 v195, v195\n\t" \
  "v_exp_f32 v196, v196\n\t" \
  "v_mfma_f32_32x32x16_bf16 v[208:223], v[228:231], %[qb], 0\n\t" \
  "ds_read_b128 v[228:231], %[ka] offset:12480\n\t" \
  "ds_read_b128 v[232:235], %[va0] offset:8320\n\t" \
  "ds_read_b128 v[240:243], %[va1] offset:8320\n\t" \
  "v_exp_f32 v197, v197\n\t" \
  "v_exp_f32 v198, v198\n\t" \
  "v_exp_f32 v199, v199\n\t" \
  "buffer_load_dwordx4 v[136:139], %[vo], %[rs], %[so] offen offset:2048 sc1\n\t" \
  "v_exp_f32 v200, v200\n\t" \
  "v_exp_f32 v201, v201\n\t" \
  "v_exp_f32 v202, v202\n\t" \
  "v_smfmac_f32_32x32x32_bf16 %[oa], v[236:239], v[192:199], v248\n\t" \
  "v_exp_f32 v203, v203\n\t" \
  "v_exp_f32 v204, v204\n\t" \
  "v_exp_f32 v205, v205\n\t" \
  "v_exp_f32 v206, v206\n\t" \
  "v_exp_f32 v207, v207\n\t" \
  "v_exp_f32 v208, v208\n\t" \
  "v_exp_f32 v209, v209\n\t" \
  "v_smfmac_f32_32x32x32_bf16 %[oa], v[244:247], v[200:207], v248\n\t" \
  "v_exp_f32 v210, v210\n\t" \
  "v_exp_f32 v211, v211\n\t" \
  "v_exp_f32 v212, v212\n\t" \
  "v_mfma_f32_32x32x16_bf16 v[192:207], v[224:227], %[qa], 0\n\t" \
  "v_exp_f32 v213, v213\n\t" \
  "v_exp_f32 v214, v214\n\t" \
  "v_exp_f32 v215, v215\n\t" \
  "buffer_load_dwordx4 v[140:143], %[vo], %[rs], %[so] offen offset:3072 sc1\n\t" \
  "v_exp_f32 v216, v216\n\t" \
  "v_exp_f32 v217, v217\n\t" \
  "v_exp_f32 v218, v218\n\t" \
  "v_smfmac_f32_32x32x32_bf16 %[ob], v[236:239], v[208:215], v248\n\t" \
  "v_exp_f32 v219, v219\n\t" \
  "v_exp_f32 v220, v220\n\t" \
  "v_exp_f32 v221, v221\n\t" \
  "buffer_load_dwordx4 %[vo], %[rw], %[wo] offen offset:2048 lds\n\t" \
  "v_exp_f32 v222, v222\n\t" \
  "v_exp_f32 v223, v223\n\t" \
  "s_waitcnt lgkmcnt(0)\n\t" \
  "v_exp_f32 v192, v192\n\t" \
  "v_exp_f32 v193, v193\n\t" \
  "v_smfmac_f32_32x32x32_bf16 %[ob], v[244:247], v[216:223], v248\n\t" \
  "v_exp_f32 v194, v194\n\t" \
  "v_exp_f32 v195, v195\n\t" \
  "v_exp_f32 v196, v196\n\t" \
  "v_mfma_f32_32x32x16_bf16 v[208:223], v[224:227], %[qb], 0\n\t" \
  "ds_read_b128 v[224:227], %[ka] offset:16640\n\t" \
  "ds_read_b128 v[236:239], %[va0] offset:12480\n\t" \
  "ds_read_b128 v[244:247], %[va1] offset:12480\n\t" \
  "v_exp_f32 v197, v197\n\t" \
  "v_exp_f32 v198, v198\n\t" \
  "v_exp_f32 v199, v199\n\t" \
  "s_add_u32 %[so], %[so], 4096\n\t" \
  "buffer_load_dwordx4 v[144:147], %[vo], %[rs], %[so] offen offset:0 sc1\n\t" \
  "v_exp_f32 v200, v200\n\t" \
  "v_exp_f32 v201, v201\n\t" \
  "v_exp_f32 v202, v202\n\t" \
  "v_smfmac_f32_32x32x32_bf16 %[oa], v[232:235], v[192:199], v248\n\t" \
  "v_exp_f32 v203, v203\n\t" \
  "v_exp_f32 v204, v204\n\t" \
  "v_exp_f32 v205, v205\n\t" \
  "v_exp_f32 v206, v206\n\t" \
  "v_exp_f32 v207, v207\n\t" \
  "v_exp_f32 v208, v208\n\t" \
  "v_exp_f32 v209, v209\n\t" \
  "v_smfmac_f32_32x32x32_bf16 %[oa], v[240:243], v[200:207], v248\n\t" \
  "v_exp_f32 v210, v210\n\t" \
  "v_exp_f32 v211, v211\n\t" \
  "v_exp_f32 v212, v212\n\t" \
  "v_mfma_f32_32x32x16_bf16 v[192:207], v[228:231], %[qa], 0\n\t" \
  "v_exp_f32 v213, v213\n\t" \
  "v_exp_f32 v214, v214\n\t" \
  "v_exp_f32 v215, v215\n\t" \
  "buffer_load_dwordx4 v[148:151], %[vo], %[rs], %[so] offen offset:1024 sc1\n\t" \
  "v_exp_f32 v216, v216\n\t" \
  "v_exp_f32 v217, v217\n\t" \
  "v_exp_f32 v218, v218\n\t" \
  "v_smfmac_f32_32x32x32_bf16 %[ob], v[232:235], v[208:215], v248\n\t" \
  "v_exp_f32 v219, v219\n\t" \
  "v_exp_f32 v220, v220\n\t" \
  "v_exp_f32 v221, v221\n\t" \
  "buffer_load_dwordx4 %[vo], %[rw], %[wo] offen offset:3072 lds\n\t" \
  "v_exp_f32 v222, v222\n\t" \
  "v_exp_f32 v223, v223\n\t" \
  "s_waitcnt lgkmcnt(0)\n\t" \
  "v_exp_f32 v192, v192\n\t" \
  "v_exp_f32 v193, v193\n\t" \
  "v_smfmac_f32_32x32x32_bf16 %[ob], v[240:243], v[216:223], v248\n\t" \
  "v_exp_f32 v194, v194\n\t" \
  "v_exp_f32 v195, v195\n\t" \
  "v_exp_f32 v196, v196\n\t" \
  "v_mfma_f32_32x32x16_bf16 v[208:223], v[228:231], %[qb], 0\n\t" \
  "ds_read_b128 v[228:231], %[ka] offset:20800\n\t" \
  "ds_read_b128 v[232:235], %[va0] offset:16640\n\t" \
  "ds_read_b128 v[240:243], %[va1] offset:16640\n\t" \
  "v_exp_f32 v197, v197\n\t" \
  "v_exp_f32 v198, v198\n\t" \
  "v_exp_f32 v199, v199\n\t" \
  "buffer_load_dwordx4 v[152:155], %[vo], %[rs], %[so] offen offset:2048 sc1\n\t" \
  "v_exp_f32 v200, v200\n\t" \
  "v_exp_f32 v201, v201\n\t" \
  "v_exp_f32 v202, v202\n\t" \
  "v_smfmac_f32_32x32x32_bf16 %[oa], v[236:239], v[192:199], v248\n\t" \
  "v_exp_f32 v203, v203\n\t" \
  "v_exp_f32 v204, v204\n\t" \
  "v_exp_f32 v205, v205\n\t" \
  "v_exp_f32 v206, v206\n\t" \
  "v_exp_f32 v207, v207\n\t" \
  "v_exp_f32 v208, v208\n\t" \
  "v_exp_f32 v209, v209\n\t" \
  "v_smfmac_f32_32x32x32_bf16 %[oa], v[244:247], v[200:207], v248\n\t" \
  "v_exp_f32 v210, v210\n\t" \
  "v_exp_f32 v211, v211\n\t" \
  "v_exp_f32 v212, v212\n\t" \
  "v_mfma_f32_32x32x16_bf16 v[192:207], v[224:227], %[qa], 0\n\t" \
  "v_exp_f32 v213, v213\n\t" \
  "v_exp_f32 v214, v214\n\t" \
  "v_exp_f32 v215, v215\n\t" \
  "buffer_load_dwordx4 v[156:159], %[vo], %[rs], %[so] offen offset:3072 sc1\n\t" \
  "v_exp_f32 v216, v216\n\t" \
  "v_exp_f32 v217, v217\n\t" \
  "v_exp_f32 v218, v218\n\t" \
  "v_smfmac_f32_32x32x32_bf16 %[ob], v[236:239], v[208:215], v248\n\t" \
  "v_exp_f32 v219, v219\n\t" \
  "v_exp_f32 v220, v220\n\t" \
  "v_exp_f32 v221, v221\n\t" \
  "s_add_u32 %[wo], %[wo], 0x100000\n\t" \
  "buffer_load_dwordx4 %[vo], %[rw], %[wo] offen offset:0 lds\n\t" \
  "v_exp_f32 v222, v222\n\t" \
  "v_exp_f32 v223, v223\n\t" \
  "s_waitcnt lgkmcnt(0)\n\t" \
  "v_exp_f32 v192, v192\n\t" \
  "v_exp_f32 v193, v193\n\t" \
  "v_smfmac_f32_32x32x32_bf16 %[ob], v[244:247], v[216:223], v248\n\t" \
  "v_exp_f32 v194, v194\n\t" \
  "v_exp_f32 v195, v195\n\t" \
  "v_exp_f32 v196, v196\n\t" \
  "v_mfma_f32_32x32x16_bf16 v[208:223], v[224:227], %[qb], 0\n\t" \
  "ds_read_b128 v[224:227], %[ka] offset:24960\n\t" \
  "ds_read_b128 v[236:239], %[va0] offset:20800\n\t" \
  "ds_read_b128 v[244:247], %[va1] offset:20800\n\t" \
  "v_exp_f32 v197, v197\n\t" \
  "v_exp_f32 v198, v198\n\t" \
  "v_exp_f32 v199, v199\n\t" \
  "s_add_u32 %[so], %[so], 4096\n\t" \
  "buffer_load_dwordx4 v[160:163], %[vo], %[rs], %[so] offen offset:0 sc1\n\t" \
  "v_exp_f32 v200, v200\n\t" \
  "v_exp_f32 v201, v201\n\t" \
  "v_exp_f32 v202, v202\n\t" \
  "v_smfmac_f32_32x32x32_bf16 %[oa], v[232:235], v[192:199], v248\n\t" \
  "v_exp_f32 v203, v203\n\t" \
  "v_exp_f32 v204, v204\n\t" \
  "v_exp_f32 v205, v205\n\t" \
  "v_exp_f32 v206, v206\n\t" \
  "v_exp_f32 v207, v207\n\t" \
  "v_exp_f32 v208, v208\n\t" \
  "v_exp_f32 v209, v209\n\t" \
  "v_smfmac_f32_32x32x32_bf16 %[oa], v[240:243], v[200:207], v248\n\t" \
  "v_exp_f32 v210, v210\n\t" \
  "v_exp_f32 v211, v211\n\t" \
  "v_exp_f32 v212, v212\n\t" \
  "v_mfma_f32_32x32x16_bf16 v[192:207], v[228:231], %[qa], 0\n\t" \
  "v_exp_f32 v213, v213\n\t" \
  "v_exp_f32 v214, v214\n\t" \
  "v_exp_f32 v215, v215\n\t" \
  "s_mov_b32 %[so], %[p2]\n\t" \
  "buffer_load_dwordx4 v[164:167], %[vo], %[rs], %[so] offen offset:0 sc1\n\t" \
  "v_exp_f32 v216, v216\n\t" \
  "v_exp_f32 v217, v217\n\t" \
  "v_exp_f32 v218, v218\n\t" \
  "v_smfmac_f32_32x32x32_bf16 %[ob], v[232:235], v[208:215], v248\n\t" \
  "v_exp_f32 v219, v219\n\t" \
  "v_exp_f32 v220, v220\n\t" \
  "v_exp_f32 v221, v221\n\t" \
  "buffer_load_dwordx4 %[vo], %[rw], %[wo] offen offset:1024 lds\n\t" \
  "v_exp_f32 v222, v222\n\t" \
  "v_exp_f32 v223, v223\n\t" \
  "s_waitcnt lgkmcnt(0)\n\t" \
  "v_exp_f32 v192, v192\n\t" \
  "v_exp_f32 v193, v193\n\t" \
  "v_smfmac_f32_32x32x32_bf16 %[ob], v[240:243], v[216:223], v248\n\t" \
  "v_exp_f32 v194, v194\n\t" \
  "v_exp_f32 v195, v195\n\t" \
  "v_exp_f32 v196, v196\n\t" \
  "v_mfma_f32_32x32x16_bf16 v[208:223], v[228:231], %[qb], 0\n\t" \
  "ds_read_b128 v[228:231], %[ka] offset:29120\n\t" \
  "ds_read_b128 v[232:235], %[va0] offset:24960\n\t" \
  "ds_read_b128 v[240:243], %[va1] offset:24960\n\t" \
  "v_exp_f32 v197, v197\n\t" \
  "v_exp_f32 v198, v198\n\t" \
  "v_exp_f32 v199, v199\n\t" \
  "buffer_load_dwordx4 v[168:171], %[vo], %[rs], %[so] offen offset:1024 sc1\n\t" \
  "v_exp_f32 v200, v200\n\t" \
  "v_exp_f32 v201, v201\n\t" \
  "v_exp_f32 v202, v202\n\t" \
  "v_smfmac_f32_32x32x32_bf16 %[oa], v[236:239], v[192:199], v248\n\t" \
  "v_exp_f32 v203, v203\n\t" \
  "v_exp_f32 v204, v204\n\t" \
  "v_exp_f32 v205, v205\n\t" \
  "v_exp_f32 v206, v206\n\t" \
  "v_exp_f32 v207, v207\n\t" \
  "v_exp_f32 v208, v208\n\t" \
  "v_exp_f32 v209, v209\n\t" \
  "v_smfmac_f32_32x32x32_bf16 %[oa], v[244:247], v[200:207], v248\n\t" \
  "v_exp_f32 v210, v210\n\t" \
  "v_exp_f32 v211, v211\n\t" \
  "v_exp_f32 v212, v212\n\t" \
  "v_mfma_f32_32x32x16_bf16 v[192:207], v[224:227], %[qa], 0\n\t" \
  "v_exp_f32 v213, v213\n\t" \
  "v_exp_f32 v214, v214\n\t" \
  "v_exp_f32 v215, v215\n\t" \
  "buffer_load_dwordx4 v[172:175], %[vo], %[rs], %[so] offen offset:2048 sc1\n\t" \
  "v_exp_f32 v216, v216\n\t" \
  "v_exp_f32 v217, v217\n\t" \
  "v_exp_f32 v218, v218\n\t" \
  "v_smfmac_f32_32x32x32_bf16 %[ob], v[236:239], v[208:215], v248\n\t" \
  "v_exp_f32 v219, v219\n\t" \
  "v_exp_f32 v220, v220\n\t" \
  "v_exp_f32 v221, v221\n\t" \
  "buffer_load_dwordx4 %[vo], %[rw], %[wo] offen offset:2048 lds\n\t" \
  "v_exp_f32 v222, v222\n\t" \
  "v_exp_f32 v223, v223\n\t" \
  "s_waitcnt lgkmcnt(0)\n\t" \
  "v_exp_f32 v192, v192\n\t" \
  "v_exp_f32 v193, v193\n\t" \
  "v_smfmac_f32_32x32x32_bf16 %[ob], v[244:247], v[216:223], v248\n\t" \
  "v_exp_f32 v194, v194\n\t" \
  "v_exp_f32 v195, v195\n\t" \
  "v_exp_f32 v196, v196\n\t" \
  "v_mfma_f32_32x32x16_bf16 v[208:223], v[224:227], %[qb], 0\n\t" \
  "ds_read_b128 v[224:227], %[ka] offset:33280\n\t" \
  "ds_read_b128 v[236:239], %[va0] offset:29120\n\t" \
  "ds_read_b128 v[244:247], %[va1] offset:29120\n\t" \
  "v_exp_f32 v197, v197\n\t" \
  "v_exp_f32 v198, v198\n\t" \
  "v_exp_f32 v199, v199\n\t" \
  "buffer_load_dwordx4 v[176:179], %[vo], %[rs], %[so] offen offset:3072 sc1\n\t" \
  "v_exp_f32 v200, v200\n\t" \
  "v_exp_f32 v201, v201\n\t" \
  "v_exp_f32 v202, v202\n\t" \
  "v_smfmac_f32_32x32x32_bf16 %[oa], v[232:235], v[192:199], v248\n\t" \
  "v_exp_f32 v203, v203\n\t" \
  "v_exp_f32 v204, v204\n\t" \
  "v_exp_f32 v205, v205\n\t" \
  "v_exp_f32 v206, v206\n\t" \
  "v_exp_f32 v207, v207\n\t" \
  "v_exp_f32 v208, v208\n\t" \
  "v_exp_f32 v209, v209\n\t" \
  "v_smfmac_f32_32x32x32_bf16 %[oa], v[240:243], v[200:207], v248\n\t" \
  "v_exp_f32 v210, v210\n\t" \
  "v_exp_f32 v211, v211\n\t" \
  "v_exp_f32 v212, v212\n\t" \
  "v_mfma_f32_32x32x16_bf16 v[192:207], v[228:231], %[qa], 0\n\t" \
  "v_exp_f32 v213, v213\n\t" \
  "v_exp_f32 v214, v214\n\t" \
  "v_exp_f32 v215, v215\n\t" \
  "s_add_u32 %[so], %[so], 4096\n\t" \
  "buffer_load_dwordx4 v[180:183], %[vo], %[rs], %[so] offen offset:0 sc1\n\t" \
  "v_exp_f32 v216, v216\n\t" \
  "v_exp_f32 v217, v217\n\t" \
  "v_exp_f32 v218, v218\n\t" \
  "v_smfmac_f32_32x32x32_bf16 %[ob], v[232:235], v[208:215], v248\n\t" \
  "v_exp_f32 v219, v219\n\t" \
  "v_exp_f32 v220, v220\n\t" \
  "v_exp_f32 v221, v221\n\t" \
  "buffer_load_dwordx4 %[vo], %[rw], %[wo] offen offset:3072 lds\n\t" \
  "v_exp_f32 v222, v222\n\t" \
  "v_exp_f32 v223, v223\n\t" \
  "s_waitcnt lgkmcnt(0)\n\t" \
  "v_exp_f32 v192, v192\n\t" \
  "v_exp_f32 v193, v193\n\t" \
  "v_smfmac_f32_32x32x32_bf16 %[ob], v[240:243], v[216:223], v248\n\t" \
  "v_exp_f32 v194, v194\n\t" \
  "v_exp_f32 v195, v195\n\t" \
  "v_exp_f32 v196, v196\n\t" \
  "v_mfma_f32_32x32x16_bf16 v[208:223], v[228:231], %[qb], 0\n\t" \
  "ds_read_b128 v[228:231], %[ka] offset:37440\n\t" \
  "ds_read_b128 v[232:235], %[va0] offset:33280\n\t" \
  "ds_read_b128 v[240:243], %[va1] offset:33280\n\t" \
  "v_exp_f32 v197, v197\n\t" \
  "v_exp_f32 v198, v198\n\t" \
  "v_exp_f32 v199, v199\n\t" \
  "buffer_load_dwordx4 v[184:187], %[vo], %[rs], %[so] offen offset:1024 sc1\n\t" \
  "v_exp_f32 v200, v200\n\t" \
  "v_exp_f32 v201, v201\n\t" \
  "v_exp_f32 v202, v202\n\t" \
  "v_smfmac_f32_32x32x32_bf16 %[oa], v[236:239], v[192:199], v248\n\t" \
  "v_exp_f32 v203, v203\n\t" \
  "v_exp_f32 v204, v204\n\t" \
  "v_exp_f32 v205, v205\n\t" \
  "v_exp_f32 v206, v206\n\t" \
  "v_exp_f32 v207, v207\n\t" \
  "v_exp_f32 v208, v208\n\t" \
  "v_exp_f32 v209, v209\n\t" \
  "v_smfmac_f32_32x32x32_bf16 %[oa], v[244:247], v[200:207], v248\n\t" \
  "v_exp_f32 v210, v210\n\t" \
  "v_exp_f32 v211, v211\n\t" \
  "v_exp_f32 v212, v212\n\t" \
  "v_mfma_f32_32x32x16_bf16 v[192:207], v[224:227], %[qa], 0\n\t" \
  "v_exp_f32 v213, v213\n\t" \
  "v_exp_f32 v214, v214\n\t" \
  "v_exp_f32 v215, v215\n\t" \
  "buffer_load_dwordx4 v[188:191], %[vo], %[rs], %[so] offen offset:2048 sc1\n\t" \
  "v_exp_f32 v216, v216\n\t" \
  "v_exp_f32 v217, v217\n\t" \
  "v_exp_f32 v218, v218\n\t" \
  "v_smfmac_f32_32x32x32_bf16 %[ob], v[236:239], v[208:215], v248\n\t" \
  "v_exp_f32 v219, v219\n\t" \
  "v_exp_f32 v220, v220\n\t" \
  "v_exp_f32 v221, v221\n\t" \
  "v_exp_f32 v222, v222\n\t" \
  "v_exp_f32 v223, v223\n\t" \
  "v_add_u32 %[ka], 33280, %[ka]\n\t" \
  "v_add_u32 %[va0], 33280, %[va0]\n\t" \
  "v_add_u32 %[va1], 33280, %[va1]\n\t" \
  "1:\n\t" \
  "s_waitcnt lgkmcnt(0)\n\t" \
  "v_exp_f32 v192, v192\n\t" \
  "v_exp_f32 v193, v193\n\t" \
  "v_smfmac_f32_32x32x32_bf16 %[ob], v[244:247], v[216:223], v248\n\t" \
  "v_exp_f32 v194, v194\n\t" \
  "v_exp_f32 v195, v195\n\t" \
  "v_exp_f32 v196, v196\n\t" \
  "v_mfma_f32_32x32x16_bf16 v[208:223], v[224:227], %[qb], 0\n\t" \
  "ds_read_b128 v[224:227], %[ka] offset:8320\n\t" \
  "ds_read_b128 v[236:239], %[va0] offset:4160\n\t" \
  "ds_read_b128 v[244:247], %[va1] offset:4160\n\t" \
  "v_exp_f32 v197, v197\n\t" \
  "v_exp_f32 v198, v198\n\t" \
  "v_exp_f32 v199, v199\n\t" \
  "v_exp_f32 v200, v200\n\t" \
  "v_exp_f32 v201, v201\n\t" \
  "v_exp_f32 v202, v202\n\t" \
  "v_smfmac_f32_32x32x32_bf16 %[oa], v[232:235], v[192:199], v248\n\t" \
  "v_exp_f32 v203, v203\n\t" \
  "v_exp_f32 v204, v204\n\t" \
  "v_exp_f32 v205, v205\n\t" \
  "v_exp_f32 v206, v206\n\t" \
  "v_exp_f32 v207, v207\n\t" \
  "v_exp_f32 v208, v208\n\t" \
  "v_exp_f32 v209, v209\n\t" \
  "v_smfmac_f32_32x32x32_bf16 %[oa], v[240:243], v[200:207], v248\n\t" \
  "v_exp_f32 v210, v210\n\t" \
  "v_exp_f32 v211, v211\n\t" \
  "v_exp_f32 v212, v212\n\t" \
  "v_mfma_f32_32x32x16_bf16 v[192:207], v[228:231], %[qa], 0\n\t" \
  "v_exp_f32 v213, v213\n\t" \
  "v_exp_f32 v214, v214\n\t" \
  "v_exp_f32 v215, v215\n\t" \
  "v_exp_f32 v216, v216\n\t" \
  "v_exp_f32 v217, v217\n\t" \
  "v_exp_f32 v218, v218\n\t" \
  "v_smfmac_f32_32x32x32_bf16 %[ob], v[232:235], v[208:215], v248\n\t" \
  "v_exp_f32 v219, v219\n\t" \
  "v_exp_f32 v220, v220\n\t" \
  "v_exp_f32 v221, v221\n\t" \
  "v_exp_f32 v222, v222\n\t" \
  "v_exp_f32 v223, v223\n\t" \
  "s_waitcnt lgkmcnt(0)\n\t" \
  "v_exp_f32 v192, v192\n\t" \
  "v_exp_f32 v193, v193\n\t" \
  "v_smfmac_f32_32x32x32_bf16 %[ob], v[240:243], v[216:223], v248\n\t" \
  "v_exp_f32 v194, v194\n\t" \
  "v_exp_f32 v195, v195\n\t" \
  "v_exp_f32 v196, v196\n\t" \
  "v_mfma_f32_32x32x16_bf16 v[208:223], v[228:231], %[qb], 0\n\t" \
  "ds_read_b128 v[228:231], %[ka] offset:12480\n\t" \
  "ds_read_b128 v[232:235], %[va0] offset:8320\n\t" \
  "ds_read_b128 v[240:243], %[va1] offset:8320\n\t" \
  "v_exp_f32 v197, v197\n\t" \
  "v_exp_f32 v198, v198\n\t" \
  "v_exp_f32 v199, v199\n\t" \
  "v_exp_f32 v200, v200\n\t" \
  "v_exp_f32 v201, v201\n\t" \
  "v_exp_f32 v202, v202\n\t" \
  "v_smfmac_f32_32x32x32_bf16 %[oa], v[236:239], v[192:199], v248\n\t" \
  "v_exp_f32 v203, v203\n\t" \
  "v_exp_f32 v204, v204\n\t" \
  "v_exp_f32 v205, v205\n\t" \
  "v_exp_f32 v206, v206\n\t" \
  "v_exp_f32 v207, v207\n\t" \
  "v_exp_f32 v208, v208\n\t" \
  "v_exp_f32 v209, v209\n\t" \
  "v_smfmac_f32_32x32x32_bf16 %[oa], v[244:247], v[200:207], v248\n\t" \
  "v_exp_f32 v210, v210\n\t" \
  "v_exp_f32 v211, v211\n\t" \
  "v_exp_f32 v212, v212\n\t" \
  "v_mfma_f32_32x32x16_bf16 v[192:207], v[224:227], %[qa], 0\n\t" \
  "v_exp_f32 v213, v213\n\t" \
  "v_exp_f32 v214, v214\n\t" \
  "v_exp_f32 v215, v215\n\t" \
  "v_exp_f32 v216, v216\n\t" \
  "v_exp_f32 v217, v217\n\t" \
  "v_exp_f32 v218, v218\n\t" \
  "v_smfmac_f32_32x32x32_bf16 %[ob], v[236:239], v[208:215], v248\n\t" \
  "v_exp_f32 v219, v219\n\t" \
  "v_exp_f32 v220, v220\n\t" \
  "v_exp_f32 v221, v221\n\t" \
  "v_exp_f32 v222, v222\n\t" \
  "v_exp_f32 v223, v223\n\t" \
  "s_waitcnt lgkmcnt(0)\n\t" \
  "v_exp_f32 v192, v192\n\t" \
  "v_exp_f32 v193, v193\n\t" \
  "v_smfmac_f32_32x32x32_bf16 %[ob], v[244:247], v[216:223], v248\n\t" \
  "v_exp_f32 v194, v194\n\t" \
  "v_exp_f32 v195, v195\n\t" \
  "v_exp_f32 v196, v196\n\t" \
  "v_mfma_f32_32x32x16_bf16 v[208:223], v[224:227], %[qb], 0\n\t" \
  "ds_read_b128 v[224:227], %[ka] offset:16640\n\t" \
  "ds_read_b128 v[236:239], %[va0] offset:12480\n\t" \
  "ds_read_b128 v[244:247], %[va1] offset:12480\n\t" \
  "v_exp_f32 v197, v197\n\t" \
  "v_exp_f32 v198, v198\n\t" \
  "v_exp_f32 v199, v199\n\t" \
  "v_exp_f32 v200, v200\n\t" \
  "v_exp_f32 v201, v201\n\t" \
  "v_exp_f32 v202, v202\n\t" \
  "v_smfmac_f32_32x32x32_bf16 %[oa], v[232:235], v[192:199], v248\n\t" \
  "v_exp_f32 v203, v203\n\t" \
  "v_exp_f32 v204, v204\n\t" \
  "v_exp_f32 v205, v205\n\t" \
  "v_exp_f32 v206, v206\n\t" \
  "v_exp_f32 v207, v207\n\t" \
  "v_exp_f32 v208, v208\n\t" \
  "v_exp_f32 v209, v209\n\t" \
  "v_smfmac_f32_32x32x32_bf16 %[oa], v[240:243], v[200:207], v248\n\t" \
  "v_exp_f32 v210, v210\n\t" \
  "v_exp_f32 v211, v211\n\t" \
  "v_exp_f32 v212, v212\n\t" \
  "v_mfma_f32_32x32x16_bf16 v[192:207], v[228:231], %[qa], 0\n\t" \
  "v_exp_f32 v213, v213\n\t" \
  "v_exp_f32 v214, v214\n\t" \
  "v_exp_f32 v215, v215\n\t" \
  "v_exp_f32 v216, v216\n\t" \
  "v_exp_f32 v217, v217\n\t" \
  "v_exp_f32 v218, v218\n\t" \
  "v_smfmac_f32_32x32x32_bf16 %[ob], v[232:235], v[208:215], v248\n\t" \
  "v_exp_f32 v219, v219\n\t" \
  "v_exp_f32 v220, v220\n\t" \
  "v_exp_f32 v221, v221\n\t" \
  "v_exp_f32 v222, v222\n\t" \
  "v_exp_f32 v223, v223\n\t" \
  "s_waitcnt lgkmcnt(0)\n\t" \
  "v_exp_f32 v192, v192\n\t" \
  "v_exp_f32 v193, v193\n\t" \
  "v_smfmac_f32_32x32x32_bf16 %[ob], v[240:243], v[216:223], v248\n\t" \
  "v_exp_f32 v194, v194\n\t" \
  "v_exp_f32 v195, v195\n\t" \
  "v_exp_f32 v196, v196\n\t" \
  "v_mfma_f32_32x32x16_bf16 v[208:223], v[228:231], %[qb], 0\n\t" \
  "ds_read_b128 v[228:231], %[ka] offset:20800\n\t" \
  "ds_read_b128 v[232:235], %[va0] offset:16640\n\t" \
  "ds_read_b128 v[240:243], %[va1] offset:16640\n\t" \
  "v_exp_f32 v197, v197\n\t" \
  "v_exp_f32 v198, v198\n\t" \
  "v_exp_f32 v199, v199\n\t" \
  "v_exp_f32 v200, v200\n\t" \
  "v_exp_f32 v201, v201\n\t" \
  "v_exp_f32 v202, v202\n\t" \
  "v_smfmac_f32_32x32x32_bf16 %[oa], v[236:239], v[192:199], v248\n\t" \
  "v_exp_f32 v203, v203\n\t" \
  "v_exp_f32 v204, v204\n\t" \
  "v_exp_f32 v205, v205\n\t" \
  "v_exp_f32 v206, v206\n\t" \
  "v_exp_f32 v207, v207\n\t" \
  "v_exp_f32 v208, v208\n\t" \
  "v_exp_f32 v209, v209\n\t" \
  "v_smfmac_f32_32x32x32_bf16 %[oa], v[244:247], v[200:207], v248\n\t" \
  "v_exp_f32 v210, v210\n\t" \
  "v_exp_f32 v211, v211\n\t" \
  "v_exp_f32 v212, v212\n\t" \
  "v_mfma_f32_32x32x16_bf16 v[192:207], v[224:227], %[qa], 0\n\t" \
  "v_exp_f32 v213, v213\n\t" \
  "v_exp_f32 v214, v214\n\t" \
  "v_exp_f32 v215, v215\n\t" \
  "v_exp_f32 v216, v216\n\t" \
  "v_exp_f32 v217, v217\n\t" \
  "v_exp_f32 v218, v218\n\t" \
  "v_smfmac_f32_32x32x32_bf16 %[ob], v[236:239], v[208:215], v248\n\t" \
  "v_exp_f32 v219, v219\n\t" \
  "v_exp_f32 v220, v220\n\t" \
  "v_exp_f32 v221, v221\n\t" \
  "v_exp_f32 v222, v222\n\t" \
  "v_exp_f32 v223, v223\n\t" \
  "v_add_u32 %[ka], 16640, %[ka]\n\t" \
  "v_add_u32 %[va0], 16640, %[va0]\n\t" \
  "v_add_u32 %[va1], 16640, %[va1]\n\t" \
  "s_add_u32 %[cnt], %[cnt], 1\n\t" \
  "s_cmp_lg_u32 %[cnt], 2\n\t" \
  "s_cbranch_scc1 1b\n\t" \
  "s_waitcnt vmcnt(0) lgkmcnt(0)\n\t" \
  "v_smfmac_f32_32x32x32_bf16 %[ob], v[244:247], v[216:223], v248\n\t" \
  "s_mov_b32 m0, %[keep]\n\t" \
  "s_nop 11\n\t" \
  "s_branch 9f\n\t" \
  "8:\n\t" \
  "s_mov_b32 %[so], %[p0]\n\t" \
  "buffer_load_dwordx4 v[64:67], %[vo], %[rs], %[so] offen offset:0 sc1\n\t" \
  "buffer_load_dwordx4 v[68:71], %[vo], %[rs], %[so] offen offset:1024 sc1\n\t" \
  "buffer_load_dwordx4 v[72:75], %[vo], %[rs], %[so] offen offset:2048 sc1\n\t" \
  "buffer_load_dwordx4 v[76:79], %[vo], %[rs], %[so] offen offset:3072 sc1\n\t" \
  "s_add_u32 %[so], %[so], 4096\n\t" \
  "buffer_load_dwordx4 v[80:83], %[vo], %[rs], %[so] offen offset:0 sc1\n\t" \
  "buffer_load_dwordx4 v[84:87], %[vo], %[rs], %[so] offen offset:1024 sc1\n\t" \
  "buffer_load_dwordx4 v[88:91], %[vo], %[rs], %[so] offen offset:2048 sc1\n\t" \
  "buffer_load_dwordx4 v[92:95], %[vo], %[rs], %[so] offen offset:3072 sc1\n\t" \
  "s_add_u32 %[so], %[so], 4096\n\t" \
  "buffer_load_dwordx4 v[96:99], %[vo], %[rs], %[so] offen offset:0 sc1\n\t" \
  "buffer_load_dwordx4 v[100:103], %[vo], %[rs], %[so] offen offset:1024 sc1\n\t" \
  "buffer_load_dwordx4 v[104:107], %[vo], %[rs], %[so] offen offset:2048 sc1\n\t" \
  "buffer_load_dwordx4 v[108:111], %[vo], %[rs], %[so] offen offset:3072 sc1\n\t" \
  "s_add_u32 %[so], %[so], 4096\n\t" \
  "buffer_load_dwordx4 v[112:115], %[vo], %[rs], %[so] offen offset:0 sc1\n\t" \
  "buffer_load_dwordx4 v[116:119], %[vo], %[rs], %[so] offen offset:1024 sc1\n\t" \
  "buffer_load_dwordx4 v[120:123], %[vo], %[rs], %[so] offen offset:2048 sc1\n\t" \
  "buffer_load_dwordx4 v[124:127], %[vo], %[rs], %[so] offen offset:3072 sc1\n\t" \
  "s_mov_b32 %[so], %[p1]\n\t" \
  "buffer_load_dwordx4 v[128:131], %[vo], %[rs], %[so] offen offset:0 sc1\n\t" \
  "buffer_load_dwordx4 v[132:135], %[vo], %[rs], %[so] offen offset:1024 sc1\n\t" \
  "buffer_load_dwordx4 v[136:139], %[vo], %[rs], %[so] offen offset:2048 sc1\n\t" \
  "buffer_load_dwordx4 v[140:143], %[vo], %[rs], %[so] offen offset:3072 sc1\n\t" \
  "s_add_u32 %[so], %[so], 4096\n\t" \
  "buffer_load_dwordx4 v[144:147], %[vo], %[rs], %[so] offen offset:0 sc1\n\t" \
  "buffer_load_dwordx4 v[148:151], %[vo], %[rs], %[so] offen offset:1024 sc1\n\t" \
  "buffer_load_dwordx4 v[152:155], %[vo], %[rs], %[so] offen offset:2048 sc1\n\t" \
  "buffer_load_dwordx4 v[156:159], %[vo], %[rs], %[so] offen offset:3072 sc1\n\t" \
  "s_add_u32 %[so], %[so], 4096\n\t" \
  "buffer_load_dwordx4 v[160:163], %[vo], %[rs], %[so] offen offset:0 sc1\n\t" \
  "s_mov_b32 %[so], %[p2]\n\t" \
  "buffer_load_dwordx4 v[164:167], %[vo], %[rs], %[so] offen offset:0 sc1\n\t" \
  "buffer_load_dwordx4 v[168:171], %[vo], %[rs], %[so] offen offset:1024 sc1\n\t" \
  "buffer_load_dwordx4 v[172:175], %[vo], %[rs], %[so] offen offset:2048 sc1\n\t" \
  "buffer_load_dwordx4 v[176:179], %[vo], %[rs], %[so] offen offset:3072 sc1\n\t" \
  "s_add_u32 %[so], %[so], 4096\n\t" \
  "buffer_load_dwordx4 v[180:183], %[vo], %[rs], %[so] offen offset:0 sc1\n\t" \
  "buffer_load_dwordx4 v[184:187], %[vo], %[rs], %[so] offen offset:1024 sc1\n\t" \
  "buffer_load_dwordx4 v[188:191], %[vo], %[rs], %[so] offen offset:2048 sc1\n\t" \
  "s_waitcnt vmcnt(0)\n\t" \
  "9:\n\t"
#define ATTN_FAST_CLOBBERS "v192", "v193", "v194", "v195", "v196", "v197", "v198", "v199", "v200", "v201", "v202", "v203", "v204", "v205", "v206", "v207", "v208", "v209", "v210", "v211", "v212", "v213", "v214", "v215", "v216", "v217", "v218", "v219", "v220", "v221", "v222", "v223", "v224", "v225", "v226", "v227", "v228", "v229", "v230", "v231", "v232", "v233", "v234", "v235", "v236", "v237", "v238", "v239", "v240", "v241", "v242", "v243", "v244", "v245", "v246", "v247", "v248", "v249", "v250", "v251", "vcc"


template <bool HELPER> __device__ __forceinline__ void attn_main(const Params &p, const Ctx &c0, int layer, f32x16 (&o)[2], bool &fast, v16u (&pre)[8]) {
  const Ctx c = fresh(c0);
  const int head = 4 * c.bx + (c.bj >> 3), qb = c.bj & 7, l = lane_id(), h = l >> 5, tt = qb * 8 + 2 * (c.w & 3);
  if (!HELPER && c.w == 0) {
    const int hp = head >> 1, cq = hp / 3, ck = (16 + hp) / 3, cv = (32 + hp) / 3;
    const unsigned fo = (l < 16 || l >= 48 ? ck * 16 + (l & 15) : l < 32 ? cv * 16 + (l & 15) : cq * 16 + 2 * qb + (l & 1)) * FSTR + (l >= 48 ? 8 + 8 * (2 * (16 + hp - 3 * ck) + (head & 1)) : 0);
    bool wait = l < 34 || l >= 48;
    v2u v = {};
    __builtin_amdgcn_s_sleep(D_ATT);
#pragma unroll 1
    for (unsigned spins = 0; spins < (1u << 20); ++spins) {
      asm volatile("" ::: "memory");
      if (wait) {
        v = __builtin_amdgcn_raw_buffer_load_b64(c.rws, fo, OFF_FLAGS + (layer * 5 + PH_QKV) * 256 * FSTR, SC1);
        wait = l >= 48 ? v.y != (unsigned)c.tag : (v.x != (unsigned)c.tag || v.y != (unsigned)(c.tag >> 32));
      }
      if (!__any(wait)) break;
      __builtin_amdgcn_s_sleep(POLL_SLEEP);
    }
    float m = __uint_as_float(v.x);
    m = dppmax<0x111, 0x8>(m); m = dppmax<0x112, 0x8>(m); m = dppmax<0x114, 0x8>(m); m = dppmax<0x118, 0x8>(m);
    if (l == 63) *cnt_ptr(c, 80) = __float_as_uint(m);
  }
  BAR();
  v4u qa, qb2;
  const unsigned skv = OFF_KV + layer * 4 * MB + head * 131072 + c.w * 2048;
  {
    const unsigned sq = OFF_QF + layer * 2 * MB + (head * 64 + tt) * 1024;
    asm volatile("buffer_load_dwordx4 %0, %2, %3, %4 offen sc1\n\tbuffer_load_dwordx4 %1, %2, %3, %4 offen offset:1024 sc1"
                 : "=&v"(qa), "=&v"(qb2) : "v"(l * 16), "s"(c.rws), "s"(sq) : "memory");
    glds<true>(c.rws, l * 16, skv, c.lds0 + c.w * KVP);
    glds<true>(c.rws, l * 16, skv + 1024, c.lds0 + c.w * KVP + 1024);
    if (l < 16) { unsigned one = (l & 1) ? 0u : ONES2; OPAQUE_V(one); *(LAS v4u *)(c.lds + (8 * c.w + (l >> 1)) * KVP + 2048 + (l & 1) * 16) = v4u{one, one, one, one}; }
    asm volatile("s_waitcnt vmcnt(2)" : "+v"(qa), "+v"(qb2) :: "memory");
  }
  const float km = __uint_as_float(*cnt_ptr(c, 80));
  const float qn = halfsum2(dot2(qa.w, qa.w, dot2(qa.z, qa.z, dot2(qa.y, qa.y, dot2(qa.x, qa.x, 0.f)))),
                            dot2(qb2.w, qb2.w, dot2(qb2.z, qb2.z, dot2(qb2.y, qb2.y, dot2(qb2.x, qb2.x, 0.f)))));
  const unsigned kb0 = c.lds0 + (HELPER ? KVP : 0);
  unsigned ka = kb0 + l * 16;
  unsigned va0 = kb0 + ((l & 16) ? ((l & 15) ? 2064 : 2048) : 1024 + h * 256 + (l & 15) * 16), va1 = va0 + ((l & 16) ? 0 : 512);
  fast = rfl(__all(qn * km <= 4096.f));
  asm volatile("" : "=v"(o[0]), "=v"(o[1]));
  unsigned p0, p1, p2;
  if (!HELPER) { p0 = OFF_WTO + (2 * (c.bj & 7) * 32 + (c.w >> 1) * 16) * 1024; p1 = p0 + 32768; p2 = p1 + 9 * 1024; }
  else { p0 = OFF_WT1 + up_nt0(c.bj & 15) * NTP + 25 * (c.w - 4) * 1024; p1 = p0 + 16384; p2 = OFF_WT2 + ((2 * (c.bj & 7) + (c.w & 1)) * 86 + ((c.w - 4) >> 1) * 43) * 1024; }
  asm volatile("s_waitcnt vmcnt(0) lgkmcnt(0)\n\ts_barrier" ::: "memory");
  if (HELPER && c.w == 4 && layer == 0)
    __builtin_amdgcn_raw_buffer_store_b64(v2u{(unsigned)c.tag, (unsigned)(c.tag >> 32)}, c.rws, (unsigned)(l / (FSTR / 8)) << 26 | l * 8,
                                          OFF_FLAGS + (SLOT_PREP * 256 + c.bj * 8 + c.bx) * FSTR, SC1);
  const int si = (c.w & 3) * 64 + l;
  if (!fast) {
#pragma unroll
    for (int i = 1; i < 8; ++i) {
      glds<true>(c.rws, l * 16, skv + i * 16384, c.lds0 + (8 * i + c.w) * KVP);
      glds<true>(c.rws, l * 16, skv + i * 16384 + 1024, c.lds0 + (8 * i + c.w) * KVP + 1024);
    }
    poll<0>(c, SLOT_PREP, si);
    asm volatile("s_waitcnt vmcnt(0)\n\ts_barrier\n\ts_barrier\n\ts_barrier" ::: "memory");
    int idx = 0xdddd;
    OPAQUE_V(idx);
#pragma unroll
    for (int j = 0; j < 2; ++j) {
      const v4u q = j ? qb2 : qa;
      f32x16 acc = {};
      float m = -__builtin_inff();
#pragma unroll 1
      for (int t = 0; t < 32; ++t) {
        const v4u kf = *(const LAS v4u *)(uintptr_t)(ka + t * 2 * KVP);
        const v4u v0 = *(const LAS v4u *)(uintptr_t)(va0 + t * 2 * KVP), v1 = *(const LAS v4u *)(uintptr_t)(va1 + t * 2 * KVP);
        f32x16 s = mfma(kf, q, f32x16{});
        float tm = s[0];
#pragma unroll
        for (int r = 1; r < 16; ++r) tm = __builtin_fmaxf(tm, s[r]);
        auto sw = __builtin_amdgcn_permlane32_swap(__builtin_bit_cast(unsigned, tm), __builtin_bit_cast(unsigned, tm), false, false);
        const unsigned sw0 = sw[0], sw1 = sw[1];
        const float mn = __builtin_fmaxf(m, __builtin_fmaxf(__uint_as_float(sw0), __uint_as_float(sw1)));
        const float al = __builtin_amdgcn_exp2f(m - mn);
        m = mn;
        v8u e0, e1;
#pragma unroll
        for (int r = 0; r < 8; ++r) {
          e0[r] = __float_as_uint(__builtin_amdgcn_exp2f(s[r] - mn));
          e1[r] = __float_as_uint(__builtin_amdgcn_exp2f(s[8 + r] - mn));
        }
#pragma unroll
        for (int r = 0; r < 16; ++r) acc[r] *= al;
        acc = __builtin_amdgcn_smfmac_f32_32x32x32_bf16(__builtin_bit_cast(bf16x8, v0), __builtin_bit_cast(bf16x16, e0), acc, idx, 0, 0);
        acc = __builtin_amdgcn_smfmac_f32_32x32x32_bf16(__builtin_bit_cast(bf16x8, v1), __builtin_bit_cast(bf16x16, e1), acc, idx, 0, 0);
      }
      acc[9] = m;
      o[j] = acc;
    }
  }
  unsigned cnt = rfl(fast), keep, so = rfl(skv + 16384), m0v = rfl(c.lds0 + (8 + c.w) * KVP), wo = rfl(OFF_WT2 + (c.bj * 8 + c.w) * 4096);
  const rsrc_t rw = mkrsrc(p.ws, layer == 0 ? OFF_FLAGS : OFF_WTQ);
  asm volatile(ATTN_FAST_ASM
               : [oa] "+v"(o[0]), [ob] "+v"(o[1]), [cnt] "+s"(cnt), [keep] "=&s"(keep), [so] "+s"(so), [m0v] "+s"(m0v), [wo] "+s"(wo),
                 [ka] "+v"(ka), [va0] "+v"(va0), [va1] "+v"(va1),
                 "=&{v[64:79]}"(pre[0]), "=&{v[80:95]}"(pre[1]), "=&{v[96:111]}"(pre[2]), "=&{v[112:127]}"(pre[3]),
                 "=&{v[128:143]}"(pre[4]), "=&{v[144:159]}"(pre[5]), "=&{v[160:175]}"(pre[6]), "=&{v[176:191]}"(pre[7])
               : [qa] "v"(qa), [qb] "v"(qb2), [vo] "v"(l * 16), [rs] "s"(c.rws), [rw] "s"(rw), [dummy] "s"(rfl(c.lds0 + L_DUMMY)),
                 [p0] "s"(rfl(p0)), [p1] "s"(rfl(p1)), [p2] "s"(rfl(p2)), [fo] "v"(OFF_FLAGS + (SLOT_PREP * 256 + si) * FSTR), [tag] "s"(rfl64(c.tag))
               : "memory", "scc", ATTN_FAST_CLOBBERS);
}
__device__ __forceinline__ void attn_send(const Ctx &c0, const f32x16 (&o)[2]) {
  const Ctx c = fresh(c0);
  const int l = lane_id();
  LAS float *xp = (LAS float *)(c.lds + L_APART + c.w * 5120);
#pragma unroll
  for (int j = 0; j < 2; ++j) {
    *(LAS f32x4 *)(xp + j * 640 + l * 4) = f32x4{o[j][0], o[j][1], o[j][2], o[j][3]};
    *(LAS f32x4 *)(xp + j * 640 + 256 + l * 4) = f32x4{o[j][4], o[j][5], o[j][6], o[j][7]};
    *(LAS v2u *)(xp + j * 640 + 512 + l * 2) = v2u{__float_as_uint(o[j][8]), __float_as_uint(o[j][9])};
  }
  if ((c.bx | c.bj | c.w) == 0) __builtin_amdgcn_raw_buffer_store_b64(v2u{(unsigned)c.tag, (unsigned)(c.tag >> 32)}, c.rws, (unsigned)l << 26, OFF_EPOCH, SC1);
}
__device__ __forceinline__ void attn_finish(const Ctx &c0, int layer, const f32x16 (&o)[2], bool fast) {
  const Ctx c = fresh(c0);
  const int head = 4 * c.bx + (c.bj >> 3), qb = c.bj & 7, l = lane_id(), tt = qb * 8 + 2 * (c.w & 3);
  const LAS float *xp = (const LAS float *)(c.lds + L_APART + (c.w & 3) * 5120);
  BAR();
#pragma unroll
  for (int j = 0; j < 2; ++j) {
    const f32x4 a0 = *(const LAS f32x4 *)(xp + j * 640 + l * 4), a1 = *(const LAS f32x4 *)(xp + j * 640 + 256 + l * 4);
    const v2u a2 = *(const LAS v2u *)(xp + j * 640 + 512 + l * 2);
    float f0 = 1.f, f1 = 1.f;
    if (!fast) {
      const float m1 = __uint_as_float(a2.y), mn = __builtin_fmaxf(o[j][9], m1);
      f0 = __builtin_amdgcn_exp2f(o[j][9] - mn); f1 = __builtin_amdgcn_exp2f(m1 - mn);
    }
    const float lp = o[j][8] * f0 + __uint_as_float(a2.x) * f1;
    const float inv = __builtin_amdgcn_rcpf(halfsum2(lp, lp));
    f0 *= inv; f1 *= inv;
    st16(v4u{pk2(o[j][0] * f0 + a0[0] * f1, o[j][1] * f0 + a0[1] * f1), pk2(o[j][2] * f0 + a0[2] * f1, o[j][3] * f0 + a0[3] * f1),
             pk2(o[j][4] * f0 + a1[0] * f1, o[j][5] * f0 + a1[1] * f1), pk2(o[j][6] * f0 + a1[2] * f1, o[j][7] * f0 + a1[3] * f1)},
         c.rws, l * 16, OFF_OF + layer * 2 * MB + ((tt + j) * 32 + head) * 1024);
  }
}

__global__ __launch_bounds__(512, 2) void fused_kernel(Params p) {
  extern __shared__ __attribute__((aligned(16))) char smem[];
  Ctx c;
  c.rws = mkrsrc(p.ws, WS_BYTES);
  c.lds = (LAS char *)smem;
  c.lds0 = (unsigned)(uintptr_t)c.lds;
  c.w = __builtin_amdgcn_readfirstlane(threadIdx.x >> 6);
  c.bx = blockIdx.x & 7; c.bj = blockIdx.x >> 3;
  c.tag = 0;
  if (c.w < 2) *cnt_ptr(c, c.w * 64 + lane_id()) = 0;
  if (c.w == 7) {
    u64 pc; int len;
    asm volatile("s_getpc_b64 %0\n.Lwarm_from:\n\ts_mov_b32 %1, .Lwarm_to-.Lwarm_from\n\ts_max_i32 %1, %1, 0" : "=s"(pc), "=s"(len) :: "scc");
    const rsrc_t rc = mkrsrc((const void *)(pc & ~(u64)15), (unsigned)len & ~15u);
    glds<false>(rc, lane_id() * 16, c.bj * 2048, c.lds0 + L_JUNK);
    glds<false>(rc, lane_id() * 16, c.bj * 2048 + 1024, c.lds0 + L_JUNK);
  }
  BAR();
  if (c.w >= 4) helpers_start(p, c); else ln_gemm<PH_QKV, true>(p, c, 0);
  c.tag = rfl64(c.tag);
  c = fresh(c);
  if (c.w >= 4) {
#pragma unroll 1
    for (int layer = 0; layer < 2; ++layer) {
      f32x16 o[2];
      bool fast;
      v16u pre[8];
      attn_main<true>(p, c, layer, o, fast, pre);
      attn_finish(c, layer, o, fast);
      helpers_chain(p, c, layer, pre);
    }
  } else {
#pragma unroll 1
    for (int layer = 0; layer < 2; ++layer) {
      f32x16 o[2];
      bool fast;
      v16u pre[8];
      attn_main<false>(p, c, layer, o, fast, pre);
      attn_send(c, o);
      res_gemm<PH_WO>(p, c, layer, pre);
      ln_gemm<PH_UP, false>(p, c, layer);
      res_gemm<PH_DOWN>(p, c, layer, pre);
      if (layer == 0) ln_gemm<PH_QKV, false>(p, c, 1);
    }
  }
  asm volatile(".Lwarm_to:");
}

extern "C" void kernel_launch(void *const *d_in, const int *in_sizes, int n_in, void *d_out, int out_size, void *d_ws, size_t ws_size,
                              hipStream_t stream) {
  Params p;
  p.x = d_in[0]; p.wqkv = d_in[1]; p.wo = d_in[2]; p.w1 = d_in[4]; p.b1 = d_in[5]; p.w2 = d_in[6];
  p.out = d_out; p.ws = (char *)d_ws;
  (void)hipFuncSetAttribute((const void *)fused_kernel, hipFuncAttributeMaxDynamicSharedMemorySize, LDS_BYTES);
  fused_kernel<<<256, 512, LDS_BYTES, stream>>>(p);
}
```
